```python
import torch

def _softmax(x, axis=-1):
    return torch.softmax(x, dim=axis)
def _gelu(x):
    return torch.nn.functional.gelu(x, approximate="tanh")
def _silu(x):
    return torch.nn.functional.silu(x)
def _sigmoid(x):
    return torch.sigmoid(x)
def _layernorm(x, axis=-1, eps=1e-5):
    m = x.mean(dim=axis, keepdim=True)
    v = x.var(dim=axis, keepdim=True, unbiased=False)
    return (x - m) / torch.sqrt(v + eps)
def _scatter_add(tgt, idx, src, axis=0):
    return tgt.clone().index_add_(axis, idx, src)
def _segment_sum(src, seg, n):
    out = torch.zeros((n,) + src.shape[1:], dtype=src.dtype)
    return out.index_add_(0, seg, src)
def _pad(x, pw):
    flat = tuple(v for pair in reversed(pw) for v in pair)
    return torch.nn.functional.pad(x, flat)

kernel_name = 'gated_mix__ssm_conv_h3__dense_activation_silu__prompt'

def setup_inputs():

    _w = (lambda a: (lambda t: t.to(torch.bfloat16) if t.is_floating_point() else t)(torch.as_tensor(a)))
    def _A_setup():
        B, S, D = 1, 2048, 640
        ks = 4
        return {
            "q": _w((lambda *s: torch.randn(*s, dtype=torch.bfloat16) * 0.05)(B, S, D)),
            "k": _w((lambda *s: torch.randn(*s, dtype=torch.bfloat16) * 0.2)(B, S, D)),
            "v": _w((lambda *s: torch.randn(*s, dtype=torch.bfloat16) * 0.2)(B, S, D)),
            "shift_k": _w((lambda *s: torch.randn(*s, dtype=torch.bfloat16) * 0.2)(D, ks) * 0.5),
            "ssm_k": _w((lambda *s: torch.randn(*s, dtype=torch.bfloat16) * 0.4)(D, S) * (1.0 / S ** 0.5)),
            "ssm_D": _w((lambda *s: torch.randn(*s, dtype=torch.bfloat16) * 0.2)(D)),
        }
    _A_in = _A_setup()

    def _B_setup():
        B, S, D, F = 1, 2048, 640, 1536
        return {
            "x": _w((lambda *s: torch.randn(*s, dtype=torch.bfloat16) * 0.05)(B, S, D)),
            "w1": _w((lambda *s: torch.randn(*s, dtype=torch.bfloat16) * 3.2)(D, F) * (D ** -0.5)),
            "w2": _w((lambda *s: torch.randn(*s, dtype=torch.bfloat16) * 0.05)(F, D) * (F ** -0.5)),
        }
    _B_in = _B_setup()

    return {
        "A_q": _A_in["q"],
        "A_k": _A_in["k"],
        "A_v": _A_in["v"],
        "A_shift_k": _A_in["shift_k"],
        "A_ssm_k": _A_in["ssm_k"],
        "A_ssm_D": _A_in["ssm_D"],
        "B_x": _B_in["x"],
        "B_w1": _B_in["w1"],
        "B_w2": _B_in["w2"],
        "gate_w": _w((lambda *s: torch.randn(*s, dtype=torch.bfloat16) * 0.05)(640) * 16.0),
    }

def reference(A_q, A_k, A_v, A_shift_k, A_ssm_k, A_ssm_D, B_x, B_w1, B_w2, gate_w):
    def _A_ref(q, k, v, shift_k, ssm_k, ssm_D):
        B, S, D = q.shape
        ks = shift_k.shape[-1]
        k_pad = _pad(k, ((0, 0), (ks - 1, 0), (0, 0)))
        k_shift = torch.zeros_like(k)
        for t in range(ks):
            k_shift = k_shift + k_pad[:, t:t + S, :] * shift_k[:, ks - 1 - t][None, None, :]
        kv = k_shift * v
        L = 2 * S
        kv_pad = _pad(kv, ((0, 0), (0, S), (0, 0)))
        kf_pad = _pad(ssm_k, ((0, 0), (0, S)))
        kv_f = (lambda x, axes=(-2,-1): torch.fft.rfft2(x.float(), dim=axes))(kv_pad[:, None, :, :], axes=(1, 2))
        kf_f = (lambda x, axes=(-2,-1): torch.fft.rfft2(x.float(), dim=axes))(kf_pad[:, None, :], axes=(1, 2))
        kf_f = (lambda x, *d: x.permute(*d))(kf_f, 1, 2, 0)
        y_f = kv_f * kf_f[None, :, :, :]
        y = (lambda x, s, axes=(-2,-1): torch.fft.irfft2(x, s=s, dim=axes).to(torch.bfloat16))(y_f, (1, L), axes=(1, 2))[:, 0, :S, :]
        y = y + kv * ssm_D[None, None, :]
        return q * y

    def _B_ref(x, w1, w2):
        h = torch.einsum("bsd,df->bsf", x, w1)
        h = _silu(h)
        out = torch.einsum("bsf,fd->bsd", h, w2)
        return out

    x = A_q
    g = _sigmoid((x * gate_w).sum(-1, keepdims=True))
    a = _A_ref(x, A_k, A_v, A_shift_k, A_ssm_k, A_ssm_D)
    b = _B_ref(x, B_w1, B_w2)
    return g * a + (1 - g) * b
```

```cpp
#include <hip/hip_runtime.h>
#include <hip/hip_bf16.h>
#include <stdint.h>

typedef unsigned short u16;
static __device__ __forceinline__ float bf2f(u16 b){ return __builtin_bit_cast(float,(unsigned)b<<16); }
static __device__ __forceinline__ u16 f2bf(float f){ unsigned u=__builtin_bit_cast(unsigned,f); u+=0x7FFFu+((u>>16)&1u); return (u16)(u>>16); }
static __device__ __forceinline__ float rbf(float f){ return bf2f(f2bf(f)); }
constexpr int S=2048,D=640,F=1536;

__global__ void k_kv(const u16* k,const u16* v,const u16* sk,u16* kvT){
  int idx=blockIdx.x*256+threadIdx.x; int s=idx/D,d=idx%D;
  float acc=0.f;
  for(int t=0;t<4;t++){ int ss=s+t-3; float kk= ss>=0? bf2f(k[ss*D+d]):0.f; float p=rbf(kk*bf2f(sk[d*4+3-t])); acc=rbf(acc+p);}
  kvT[d*S+s]=f2bf(acc*bf2f(v[s*D+d]));
}
__global__ void k_conv(const u16* kvT,const u16* h,const u16* Dv,u16* yT){
  __shared__ float su[S], sh[S];
  int d=blockIdx.x;
  for(int i=threadIdx.x;i<S;i+=256){su[i]=bf2f(kvT[d*S+i]); sh[i]=bf2f(h[d*S+i]);}
  __syncthreads();
  for(int t=threadIdx.x;t<S;t+=256){ float a=0.f; for(int j=0;j<=t;j++) a+=sh[t-j]*su[j];
    float y=rbf(a); y=rbf(y+rbf(su[t]*bf2f(Dv[d]))); yT[d*S+t]=f2bf(y);}
}
__global__ void k_g1(const u16* x,const u16* w1,u16* H){
  int idx=blockIdx.x*256+threadIdx.x; int s=idx/F,f=idx%F; float a=0.f;
  for(int d=0;d<D;d++) a+=bf2f(x[s*D+d])*bf2f(w1[d*F+f]);
  float h=rbf(a); H[s*F+f]=f2bf(h/(1.f+__expf(-h)));
}
__global__ void k_gate(const u16* x,const u16* gw,float* g){
  int s=blockIdx.x; float a=0.f; for(int d=threadIdx.x;d<D;d+=64) a+=rbf(bf2f(x[s*D+d])*bf2f(gw[d]));
  for(int o=32;o>0;o>>=1) a+=__shfl_xor(a,o);
  if(threadIdx.x==0){ float z=rbf(a); g[s]=rbf(1.f/(1.f+__expf(-z))); }
}
__global__ void k_g2(const u16* H,const u16* w2,const u16* q,const u16* yT,const float* g,u16* out){
  int idx=blockIdx.x*256+threadIdx.x; int s=idx/D,d=idx%D; float a=0.f;
  for(int f=0;f<F;f++) a+=bf2f(H[s*F+f])*bf2f(w2[f*D+d]);
  float b=rbf(a); float A=rbf(bf2f(q[s*D+d])*bf2f(yT[d*S+s])); float gg=g[s];
  out[s*D+d]=f2bf(rbf(gg*A)+rbf(rbf(1.f-gg)*b));
}
extern "C" void kernel_launch(void* const* d_in, const int* in_sizes, int n_in, void* d_out, int out_size, void* d_ws, size_t ws_size, hipStream_t stream){
  const u16* q=(const u16*)d_in[0]; const u16* k=(const u16*)d_in[1]; const u16* v=(const u16*)d_in[2];
  const u16* sk=(const u16*)d_in[3]; const u16* h=(const u16*)d_in[4]; const u16* Dv=(const u16*)d_in[5];
  const u16* w1=(const u16*)d_in[7]; const u16* w2=(const u16*)d_in[8]; const u16* gw=(const u16*)d_in[9];
  char* ws=(char*)d_ws; u16* kvT=(u16*)ws; u16* yT=(u16*)(ws+(size_t)S*D*2); u16* H=(u16*)(ws+(size_t)S*D*4); float* g=(float*)(ws+(size_t)S*D*4+(size_t)S*F*2);
  k_kv<<<S*D/256,256,0,stream>>>(k,v,sk,kvT);
  k_conv<<<D,256,0,stream>>>(kvT,h,Dv,yT);
  k_g1<<<S*F/256,256,0,stream>>>(q,w1,H);
  k_gate<<<S,64,0,stream>>>(q,gw,g);
  k_g2<<<S*D/256,256,0,stream>>>(H,w2,q,yT,g,(u16*)d_out);
}
```

```cpp
#include <hip/hip_runtime.h>
#include <type_traits>
#include <stdint.h>

typedef unsigned short u16;
typedef unsigned v4u __attribute__((ext_vector_type(4)));
typedef unsigned v2u __attribute__((ext_vector_type(2)));
typedef float f32x4 __attribute__((ext_vector_type(4)));
typedef float f32x2 __attribute__((ext_vector_type(2)));
typedef __bf16 bf16x8 __attribute__((ext_vector_type(8)));
typedef __bf16 bf16x2 __attribute__((ext_vector_type(2)));
typedef short v4s __attribute__((ext_vector_type(4)));
typedef short v8s __attribute__((ext_vector_type(8)));
#define GAS __attribute__((address_space(1)))
#define LAS __attribute__((address_space(3)))
#define DEV __device__ __forceinline__
typedef __amdgpu_buffer_rsrc_t rsrc_t;

constexpr int S = 2048, D = 640, F = 1536;

template <int N> DEV void waitv() { asm volatile("s_waitcnt vmcnt(%0)" ::"i"(N) : "memory"); }
template <int N> DEV void waitl() { asm volatile("s_waitcnt lgkmcnt(%0)" ::"i"(N) : "memory"); }
DEV void sbar() { asm volatile("s_barrier" ::: "memory"); }
#define SCHED() __builtin_amdgcn_sched_barrier(0)
DEV rsrc_t mkrs(const void* p, unsigned bytes) { return __builtin_amdgcn_make_buffer_rsrc((void*)p, 0, bytes, 0x00020000); }
DEV f32x4 mfma(v4u a, v4u b, f32x4 c) {
  return __builtin_amdgcn_mfma_f32_16x16x32_bf16(__builtin_bit_cast(bf16x8, a), __builtin_bit_cast(bf16x8, b), c, 0, 0, 0);
}
DEV v4u lds128(LAS const char* p) { return *(LAS const v4u*)p; }
DEV v2u lds64(LAS const char* p) { return *(LAS const v2u*)p; }
DEV v2u ldstr(LAS const char* p) { return __builtin_bit_cast(v2u, __builtin_amdgcn_ds_read_tr16_b64_v4i16((LAS v4s*)p)); }
DEV v4u cat(v2u a, v2u b) { return v4u{a.x, a.y, b.x, b.y}; }
DEV unsigned pk2(float a, float b) { bf16x2 r = __builtin_convertvector(f32x2{a, b}, bf16x2); return __builtin_bit_cast(unsigned, r); }
DEV float blo(unsigned d) { return __builtin_bit_cast(float, d << 16); }
DEV float bhi(unsigned d) { return __builtin_bit_cast(float, d & 0xffff0000u); }
DEV void setm0(unsigned v) { asm volatile("s_mov_b32 m0, %0\n\ts_nop 0" ::"s"(v)); }
template <int OFF> DEV void dma16(unsigned voff, rsrc_t rs, unsigned soff) {
  asm volatile("buffer_load_dwordx4 %0, %1, %2 offen offset:%3 lds" ::"v"(voff), "s"(rs), "s"(soff), "i"(OFF));
}
typedef float f32x16 __attribute__((ext_vector_type(16)));
DEV f32x16 mfma32(v4u a, v4u b, f32x16 c) {
  return __builtin_amdgcn_mfma_f32_32x32x16_bf16(__builtin_bit_cast(bf16x8, a), __builtin_bit_cast(bf16x8, b), c, 0, 0, 0);
}
template <int IMM> DEV void setm0_add(unsigned base) { asm volatile("s_add_u32 m0, %0, %1" ::"s"(base), "i"(IMM) : "scc"); }
template <int I, int N, class Fn> DEV void static_for(Fn&& f) { if constexpr (I < N) { f(std::integral_constant<int, I>{}); static_for<I + 1, N>(f); } }
DEV void mfma_ip(f32x4& c, const v4u& a, const v4u& b) { asm volatile("v_mfma_f32_16x16x32_bf16 %0, %1, %2, %0" : "+v"(c) : "v"(a), "v"(b)); }
DEV void mfma_ipA(f32x4& c, const v4u& a, const v4u& b) { asm volatile("v_mfma_f32_16x16x32_bf16 %0, %1, %2, %0" : "+v"(c) : "a"(a), "v"(b)); }
DEV void mfma_z(f32x4& c, const v4u& a, const v4u& b) { asm volatile("v_mfma_f32_16x16x32_bf16 %0, %1, %2, 0" : "=&v"(c) : "v"(a), "v"(b)); }
DEV void mfma_zA(f32x4& c, const v4u& a, const v4u& b) { asm volatile("v_mfma_f32_16x16x32_bf16 %0, %1, %2, 0" : "=&v"(c) : "a"(a), "v"(b)); }
template <int AUX> DEV void ldA(v4u& d, unsigned voff, rsrc_t rs, unsigned soff) {
  if constexpr (AUX) asm volatile("buffer_load_dwordx4 %0, %1, %2, %3 offen sc1" : "=a"(d) : "v"(voff), "s"(rs), "s"(soff));
  else asm volatile("buffer_load_dwordx4 %0, %1, %2, %3 offen" : "=a"(d) : "v"(voff), "s"(rs), "s"(soff));
}
template <int AUX> DEV void ldV(v4u& d, unsigned voff, rsrc_t rs, unsigned soff) {
  if constexpr (AUX) asm volatile("buffer_load_dwordx4 %0, %1, %2, %3 offen sc1" : "=v"(d) : "v"(voff), "s"(rs), "s"(soff));
  else asm volatile("buffer_load_dwordx4 %0, %1, %2, %3 offen" : "=v"(d) : "v"(voff), "s"(rs), "s"(soff));
}
DEV void nop8() { asm volatile("s_nop 7"); }

constexpr size_t WS_CTRL = 0;
constexpr size_t WS_KVT = 262144;
constexpr size_t WS_YT = WS_KVT + (size_t)D * S * 2;
constexpr size_t WS_W2T = WS_YT + (size_t)D * S * 2;
constexpr size_t WS_HF = WS_W2T + (size_t)8 * 48 * 5 * 1024;
constexpr size_t WS_G = WS_HF + (size_t)128 * 48 * 1024;
constexpr size_t WS_END = WS_G + S * 4;
constexpr unsigned C_EPOCH = 0, C_TMO = 256;
constexpr unsigned C_FW = 4096;
constexpr unsigned C_FKV = 16384;
constexpr unsigned C_FH = 32768;
constexpr unsigned C_FY = 98304;
constexpr unsigned C_BYTES = 262144;

#define NSTAMP 16
#define STAMP_OFF (size_t)(32u << 20)
#define STAMP_DECL
#define STAMP(i)
#define STAMP_FLUSH()
#define FT_DECL
#define FT(i)
#define FT_FLUSH()

struct Params {
  const u16 *q, *k, *v, *sk, *h, *Dv, *w1, *w2, *gw;
  u16* out;
  char* ws;
};

constexpr int LDS_BYTES = 160 * 1024;
constexpr int SC1 = 16;
#define IC(n) std::integral_constant<int, (n)>{}

DEV void ld32_sc1(unsigned& v, unsigned voff, rsrc_t rs) { asm volatile("buffer_load_dword %0, %1, %2, 0 offen sc1" : "=v"(v) : "v"(voff), "s"(rs)); }
DEV void st32_sc1(unsigned v, unsigned voff, rsrc_t rs) { asm volatile("buffer_store_dword %0, %1, %2, 0 offen sc1" ::"v"(v), "v"(voff), "s"(rs)); }
DEV void poll_finish(unsigned v, unsigned voff, rsrc_t rsC, unsigned tag) {
  asm volatile("" : "+v"(v));
  if (__builtin_amdgcn_ballot_w64(v != tag) == 0) return;
#pragma unroll 1
  for (int spins = 0; spins < (1 << 15); ++spins) {
    __builtin_amdgcn_s_sleep(1);
    ld32_sc1(v, voff, rsC);
    asm volatile("s_waitcnt vmcnt(0)" : "+v"(v)::"memory");
    if (__builtin_amdgcn_ballot_w64(v != tag) == 0) return;
  }
  st32_sc1(1u, C_TMO, rsC);
}

DEV void mfma32_ip(f32x16& c, const v4u& a, const v4u& b) { asm volatile("v_mfma_f32_32x32x16_bf16 %0, %1, %2, %0" : "+v"(c) : "v"(a), "v"(b)); }
DEV void mfma32_z(f32x16& c, const v4u& a, const v4u& b) { asm volatile("v_mfma_f32_32x32x16_bf16 %0, %1, %2, 0" : "=&v"(c) : "v"(a), "v"(b)); }

constexpr int KV_BASE = 32768, KV_GS = 1152, KV_V = 9 * KV_GS, KV_CG = 17 * KV_GS;
constexpr int CV_BASE = 32768;
constexpr int CV_HC = 4160;
constexpr int CV_U = 4 * CV_HC;
constexpr int CV_CH = CV_U + 4608;
constexpr int CV_EX = CV_BASE + 4 * CV_CH;
constexpr int G2_PART = 0;
constexpr int G2_Y = 81920;
constexpr int HW = 24;

template <int AUX> DEV void ldV2(v2u& d, unsigned voff, rsrc_t rs, unsigned soff) {
  if constexpr (AUX) asm volatile("buffer_load_dwordx2 %0, %1, %2, %3 offen sc1" : "=v"(d) : "v"(voff), "s"(rs), "s"(soff));
  else asm volatile("buffer_load_dwordx2 %0, %1, %2, %3 offen" : "=v"(d) : "v"(voff), "s"(rs), "s"(soff));
}
DEV void dma16s(unsigned voff, rsrc_t rs, unsigned soff, unsigned m0v) {
  asm volatile("s_mov_b32 m0, %3\n\ts_nop 0\n\tbuffer_load_dwordx4 %0, %1, %2 offen lds" ::"v"(voff), "s"(rs), "s"(soff), "s"(m0v));
}
DEV unsigned mktag(unsigned& epoch) {
  asm volatile("" : "+v"(epoch));
  return __builtin_amdgcn_readfirstlane(epoch * 0x9E3779B1u + 0x7F4A7C15u);
}

template <int NR, class ST> DEV void kv_math(const v4u (&kk)[NR + 3], const v4u (&vv)[NR], const v4u (&ww)[4], ST&& st) {
#pragma unroll
  for (int cp = 0; cp < 4; ++cp) {
    f32x2 w[4];
    w[0] = f32x2{blo(ww[cp].x), blo(ww[cp].z)}; w[1] = f32x2{bhi(ww[cp].x), bhi(ww[cp].z)};
    w[2] = f32x2{blo(ww[cp].y), blo(ww[cp].w)}; w[3] = f32x2{bhi(ww[cp].y), bhi(ww[cp].w)};
    f32x2 kf[NR + 3];
#pragma unroll
    for (int i = 0; i < NR + 3; ++i) kf[i] = f32x2{blo(kk[i][cp]), bhi(kk[i][cp])};
    f32x2 r[NR];
#pragma unroll
    for (int i = 0; i < NR; ++i) {
      f32x2 a = kf[i + 3] * w[0];
      a = __builtin_elementwise_fma(kf[i + 2], w[1], a);
      a = __builtin_elementwise_fma(kf[i + 1], w[2], a);
      a = __builtin_elementwise_fma(kf[i], w[3], a);
      r[i] = a * f32x2{blo(vv[i][cp]), bhi(vv[i][cp])};
    }
    st(cp, r);
  }
}

template <bool G1R>
DEV void body(const Params& p, LAS char* lds, const int x, const int r, const int wave, const int lane) {
  const int g = lane >> 4, q4 = (lane >> 2) & 3, p4 = lane & 3, c16 = lane & 15;
  STAMP_DECL; FT_DECL;
  STAMP(0);
  const rsrc_t rsC = mkrs(p.ws + WS_CTRL, C_BYTES);
  unsigned epoch, tag;
  ld32_sc1(epoch, C_EPOCH, rsC);
  const int cl = G1R ? (wave >> 1) : wave, half = G1R ? (wave & 1) : 0;
  const int ch = (G1R ? 80 * x + 2 * r : 80 * x + 48 + 4 * (r - 24)) + cl;
  const unsigned cb = CV_BASE + cl * CV_CH;
  constexpr int NCHK = G1R ? 2 : 4;
  unsigned dv;
  { const rsrc_t rsD = mkrs(p.Dv, D * 2); asm volatile("buffer_load_ushort %0, %1, %2, 0 offen" : "=v"(dv) : "v"(ch * 2), "s"(rsD)); }
  const rsrc_t rsHh = mkrs(p.h, D * S * 2), rsU = mkrs(p.ws + WS_KVT, D * S * 2), rsY = mkrs(p.ws + WS_YT, D * S * 2);
  const rsrc_t rsW2 = mkrs(p.ws + WS_W2T, 8 * 48 * 5 * 1024), rsH = mkrs(p.ws + WS_HF, 128 * 48 * 1024);
  const int rb = 4 * x + (r & 3), nt = r >> 2;
  const unsigned vl = lane * 16;
  const unsigned sW = (nt * 48 + 12 * wave) * 5 * 1024;
  v4u W[12][5];
  auto ldW = [&](auto I) { constexpr int i = I.value; if constexpr (i < 60) ldA<SC1>(W[i / 5][i % 5], vl, rsW2, sW + i * 1024); };
  v4u hc[NCHK], uc[NCHK]; v2u hp[NCHK];
  auto ld_h = [&]() {
#pragma unroll
    for (int i = 0; i < NCHK; ++i) {
      const int n = (half * NCHK + i) * 64 + lane;
      ldV<0>(hc[i], ch * 4096 + n * 16, rsHh, 0);
      ldV2<0>(hp[i], n > 0 ? (unsigned)(ch * 4096 + n * 16 - 8) : 0x80000000u, rsHh, 0);
    }
  };
  auto ld_u = [&]() {
#pragma unroll
    for (int i = 0; i < NCHK; ++i) ldV<SC1>(uc[i], ch * 4096 + ((half * NCHK + i) * 64 + lane) * 16, rsU, 0);
  };
  auto fill_h = [&]() {
#pragma unroll
    for (int i = 0; i < NCHK; ++i) {
      asm volatile("" : "+v"(hc[i]), "+v"(hp[i]));
      const int n = (half * NCHK + i) * 64 + lane;
      const v4u cur = hc[i]; const v2u prv = hp[i];
      const unsigned om2 = __builtin_amdgcn_alignbit(prv.y, prv.x, 16), om1 = __builtin_amdgcn_alignbit(cur.x, prv.y, 16);
      const unsigned o0 = __builtin_amdgcn_alignbit(cur.y, cur.x, 16), o1 = __builtin_amdgcn_alignbit(cur.z, cur.y, 16), o2 = __builtin_amdgcn_alignbit(cur.w, cur.z, 16);
      LAS char* dst = lds + cb + (n + 2) * 16;
      *(LAS v4u*)(dst) = cur;
      *(LAS v4u*)(dst + CV_HC) = v4u{om1, o0, o1, o2};
      *(LAS v4u*)(dst + 2 * CV_HC) = v4u{prv.y, cur.x, cur.y, cur.z};
      *(LAS v4u*)(dst + 3 * CV_HC) = v4u{om2, om1, o0, o1};
    }
    if (half == 0) {
      if (lane < 8) *(LAS v4u*)(lds + cb + (lane >> 1) * CV_HC + (lane & 1) * 16) = v4u{0, 0, 0, 0};
      if (lane >= 32 && lane < 62) *(LAS v4u*)(lds + cb + CV_U + (lane - 32) * 16) = v4u{0, 0, 0, 0};
    }
  };
  auto fill_u = [&]() {
#pragma unroll
    for (int i = 0; i < NCHK; ++i) {
      asm volatile("" : "+v"(uc[i]));
      *(LAS v4u*)(lds + cb + CV_U + 480 + ((half * NCHK + i) * 64 + lane) * 16) = uc[i];
    }
  };

  if constexpr (G1R) {
    const int sb = 4 * (x >> 1) + (r & 3), fs = 6 * (x & 1) + (r >> 2);
    f32x16 acc1[2][2];
    const rsrc_t rsX = mkrs(p.q, S * D * 2), rsW = mkrs(p.w1, D * F * 2);
    unsigned vX[4], vW[4];
#pragma unroll
    for (int ii = 0; ii < 4; ++ii) {
      vX[ii] = (32 * wave + 8 * ii + (lane >> 3)) * 1280 + (((lane & 7) ^ (4 * (ii & 1) + g)) << 4) - 1024 * ii;
      vW[ii] = (16 * wave + 4 * ii + g) * 3072 + (((((c16 >> 1) ^ (g << 1)) << 1) | (c16 & 1)) << 4) - 1024 * ii;
    }
    const unsigned sX0 = sb * 128 * 1280, sW0 = fs * 256;
    const unsigned mX = wave * 4096, mW = 16384 + wave * 4096;
    auto dmaX = [&](auto T, auto I) { constexpr int t = T.value, i = I.value; if constexpr (t < 10) dma16<1024 * i>(vX[i], rsX, sX0 + t * 128); };
    auto dmaW = [&](auto T, auto I) { constexpr int t = T.value, i = I.value; if constexpr (t < 10) dma16<1024 * i>(vW[i], rsW, sW0 + t * 64 * 3072); };
    auto m0X = [&](auto T) { constexpr int t = T.value; if constexpr (t < 10) setm0_add<(t % 5) * 32768>(mX); };
    auto m0W = [&](auto T) { constexpr int t = T.value; if constexpr (t < 10) setm0_add<(t % 5) * 32768>(mW); };
    static_for<0, 4>([&](auto T) {
      m0X(T); asm volatile("s_nop 0");
      static_for<0, 4>([&](auto I) { dmaX(T, I); });
      if constexpr (T.value < 3) { m0W(T); asm volatile("s_nop 0"); static_for<0, 4>([&](auto I) { dmaW(T, I); }); }
    });
    const int ws_ = wave & 1, wf = wave >> 1, h = lane >> 5, r32 = lane & 31;
    unsigned aX[3][4], aW[3][2];
#pragma unroll
    for (int w = 0; w < 3; ++w) {
#pragma unroll
      for (int ks = 0; ks < 4; ++ks) aX[w][ks] = w * 65536 + (64 * ws_ + r32) * 128 + (((2 * ks + h) ^ ((r32 >> 1) & 7)) << 4);
#pragma unroll
      for (int ft = 0; ft < 2; ++ft) aW[w][ft] = w * 65536 + 16384 + (8 * h + q4) * 256 + (((2 * (2 * wf + ft) + (g & 1)) ^ (q4 << 1)) << 5) + 8 * p4;
    }
    v4u A[2][2][2], B[2][2][2];
    v2u At;
    auto rd = [&](auto T, auto HF, auto SET, auto N) {
      constexpr int t = T.value, hf = HF.value, set = SET.value, n = N.value;
      if constexpr (t < 10) {
        constexpr int b = t % 5, w = b >> 1, bo = (b & 1) * 32768;
        constexpr int kk = n / 6, rr = n % 6, ks = 2 * hf + kk;
        if constexpr (rr == 0) B[set][kk][0] = lds128(lds + aX[w][ks] + bo);
        else if constexpr (rr == 3) B[set][kk][1] = lds128(lds + aX[w][ks] + bo + 32 * 128);
        else { constexpr int ft = rr / 3, jj = (rr % 3) - 1;
          v2u xx = ldstr(lds + aW[w][ft] + bo + ks * 4096 + jj * 1024);
          if constexpr (jj == 0) At = xx; else A[set][kk][ft] = cat(At, xx); }
      }
    };
    unsigned fw, fk;
    waitv<20>(); sbar();
    STAMP(1);
    static_for<0, 12>([&](auto N) { rd(IC(0), IC(0), IC(0), N); });
    SCHED();
    static_for<0, 10>([&](auto T) {
      constexpr int t = T.value;
      static_for<0, 2>([&](auto HF) {
        constexpr int hf = HF.value;
        static_for<0, 8>([&](auto M) {
          constexpr int m = M.value, kk = m >> 2, ft = (m >> 1) & 1, st = m & 1;
          if constexpr (t == 0 && hf == 0 && kk == 0) mfma32_z(acc1[ft][st], A[hf][kk][ft], B[hf][kk][st]);
          else mfma32_ip(acc1[ft][st], A[hf][kk][ft], B[hf][kk][st]);
          if constexpr (m < 6) {
            if constexpr (hf == 0) { rd(T, IC(1), IC(1), IC(2 * m)); rd(T, IC(1), IC(1), IC(2 * m + 1)); }
            else { rd(IC(t + 1), IC(0), IC(0), IC(2 * m)); rd(IC(t + 1), IC(0), IC(0), IC(2 * m + 1)); }
          }
          if constexpr (m == 0) { if constexpr (hf == 0) m0W(IC(t + 3)); else m0X(IC(t + 4)); }
          if constexpr (m & 1) { if constexpr (hf == 0) dmaW(IC(t + 3), IC(m >> 1)); else dmaX(IC(t + 4), IC(m >> 1)); }
          if constexpr (t == 6 && hf == 1 && m == 1) ld32_sc1(fw, C_FW + lane * 128, rsC);
          if constexpr (t == 6 && hf == 1 && m == 3) ld32_sc1(fk, C_FKV + lane * 128, rsC);
          if constexpr (t == 6 && hf == 1 && m == 5) ld_h();
          SCHED();
        });
        if constexpr (hf == 0 && t < 9) {
          constexpr int younger = (t + 2 < 10 ? 8 : 0) + (t + 3 < 10 ? 8 : 0) + (t >= 7 ? 6 : 0);
          waitv<younger>(); sbar(); SCHED();
        }
      });
      FT(t); SCHED();
    });
    asm volatile("s_nop 15");
    waitv<0>();
    sbar();
    STAMP(2);
    tag = mktag(epoch);
    poll_finish(fw, C_FW + lane * 128, rsC, tag);
    poll_finish(fk, C_FKV + lane * 128, rsC, tag);
    STAMP(3);
    ld_u();
    const unsigned vo = (g & 1) * (48 * 1024) + (c16 + 16 * h) * 16;
    static_for<0, 4>([&](auto Q) {
      constexpr int qd = Q.value, st = qd >> 1, ft = qd & 1;
      const f32x16 a = acc1[ft][st];
      unsigned o[8];
      static_for<0, 8>([&](auto E) {
        constexpr int e = 2 * E.value;
        const float r0 = a[e] * __builtin_amdgcn_rcpf(1.f + __builtin_amdgcn_exp2f(a[e] * -1.44269504088896f));
        const float r1 = a[e + 1] * __builtin_amdgcn_rcpf(1.f + __builtin_amdgcn_exp2f(a[e + 1] * -1.44269504088896f));
        o[e >> 1] = pk2(r0, r1);
        if constexpr ((E.value % 4) != 3) ldW(IC(qd * 6 + E.value - E.value / 4));
        SCHED();
      });
      const unsigned so = ((8 * sb + 4 * ws_ + 2 * st) * 48 + 4 * fs + 2 * wf + ft) * 1024;
      __builtin_amdgcn_raw_buffer_store_b128(v4u{o[0], o[1], o[4], o[5]}, rsH, vo, so, SC1);
      __builtin_amdgcn_raw_buffer_store_b128(v4u{o[2], o[3], o[6], o[7]}, rsH, vo + 512, so, SC1);
      SCHED();
    });
    STAMP(4);
    waitv<32>();
    fill_h(); fill_u();
    waitv<0>();
    if (lane == 0) st32_sc1(tag, C_FH + ((sb * 12 + fs) * 4 + wave) * 64, rsC);
    waitl<0>(); sbar();
  } else {
    const int j = (r - 24) * 8 + x, rbk = j >> 1, cg0 = 5 * (j & 1);
    const rsrc_t rsK = mkrs(p.k, S * D * 2), rsV = mkrs(p.v, S * D * 2), rsS = mkrs(p.sk, D * 8), rsO = mkrs(p.ws + WS_KVT, D * S * 2);
    const unsigned vr = (lane >> 3) * 1280 + ((lane & 7) << 4), vr4 = vr ^ 64;
    auto dmaK = [&](int cgl, int G) {
      const int row = 64 * rbk - 8 + 8 * G;
      dma16s(((G >> 1) & 1) ? vr4 : vr, rsK, row >= 0 ? (unsigned)(row * 1280 + (cg0 + cgl) * 128) : 0x80000000u, KV_BASE + cgl * KV_CG + G * KV_GS);
    };
    auto dmaV = [&](int cgl, int G) {
      dma16s(((G >> 1) & 1) ? vr4 : vr, rsV, (64 * rbk + 8 * G) * 1280 + (cg0 + cgl) * 128, KV_BASE + cgl * KV_CG + KV_V + G * KV_GS);
    };
    const int id = 4 * j + wave, pk = id / 5, blk = id % 5;
    const rsrc_t rsWs = mkrs(p.w2, F * D * 2);
    const unsigned ldsb = wave * 8192;
#pragma unroll
    for (int i = 0; i < 8; ++i) {
      const int row = 4 * i + g;
      dma16s((32 * pk + row) * 1280 + blk * 256 + (((((c16 >> 1) ^ (row & 7)) << 1) | (c16 & 1)) << 4), rsWs, id < 240 ? 0u : 0x80000000u, ldsb + i * 1024);
    }
#pragma unroll
    for (int G = 0; G < 9; ++G) dmaK(wave, G);
#pragma unroll
    for (int G = 0; G < 8; ++G) dmaV(wave, G);
    dmaK(4, wave); dmaK(4, wave + 4); dmaV(4, wave); dmaV(4, wave + 4);
    if (wave == 0) dmaK(4, 8);
    const int cc = lane >> 3, rg = lane & 7;
    v4u ww[4], w4[4];
#pragma unroll
    for (int i = 0; i < 4; ++i) ldV<0>(ww[i], (64 * (cg0 + wave) + 8 * cc) * 8 + i * 16, rsS, 0);
#pragma unroll
    for (int i = 0; i < 4; ++i) ldV<0>(w4[i], (64 * (cg0 + 4) + 8 * (2 * wave + (lane >> 5))) * 8 + i * 16, rsS, 0);
    const rsrc_t rsX = mkrs(p.q, S * D * 2), rsG = mkrs(p.gw, D * 2), rsGo = mkrs(p.ws + WS_G, S * 4);
    const int grow = 8 * id + (lane >> 3), part = lane & 7;
    v4u xx[10], gg[10];
#pragma unroll
    for (int i = 0; i < 10; ++i) { ldV<0>(xx[i], grow * 1280 + i * 128 + part * 16, rsX, 0); ldV<0>(gg[i], i * 128 + part * 16, rsG, 0); }
    waitv<49>();
    STAMP(1);
    {
      const rsrc_t rsWo = mkrs(p.ws + WS_W2T, 8 * 48 * 5 * 1024);
      const int r0 = 4 * g + q4;
#pragma unroll
      for (int i = 0; i < 8; ++i) {
        const unsigned a = ldsb + r0 * 256 + ((i ^ (r0 & 7)) << 5) + 8 * p4;
        const v4u fr = cat(ldstr(lds + a), ldstr(lds + a + 4096));
        const int ctg = 8 * blk + i, ntt = ctg / 5, ct = ctg % 5;
        __builtin_amdgcn_raw_buffer_store_b128(fr, rsWo, vl, id < 240 ? (unsigned)(((ntt * 48 + pk) * 5 + ct) * 1024) : 0x80000000u, SC1);
      }
    }
    waitv<32>();
    {
      const unsigned kb = KV_BASE + wave * KV_CG;
      const unsigned a0 = kb + rg * KV_GS + ((cc ^ (4 * ((rg >> 1) & 1))) << 4), a1 = kb + (rg + 1) * KV_GS + ((cc ^ (4 * (((rg + 1) >> 1) & 1))) << 4);
      v4u kk[11], vv[8];
#pragma unroll
      for (int i = 0; i < 11; ++i) kk[i] = i < 3 ? lds128(lds + a0 + (5 + i) * 128) : lds128(lds + a1 + (i - 3) * 128);
#pragma unroll
      for (int i = 0; i < 8; ++i) vv[i] = lds128(lds + a0 + KV_V + i * 128);
#pragma unroll
      for (int i = 0; i < 4; ++i) asm volatile("" : "+v"(ww[i]));
      const unsigned ob = (64 * (cg0 + wave) + 8 * cc) * 4096 + (64 * rbk + 8 * rg) * 2;
      kv_math<8>(kk, vv, ww, [&](int cp, const f32x2 (&rr)[8]) {
#pragma unroll
        for (int c = 0; c < 2; ++c) {
          v4u o{pk2(rr[0][c], rr[1][c]), pk2(rr[2][c], rr[3][c]), pk2(rr[4][c], rr[5][c]), pk2(rr[6][c], rr[7][c])};
          __builtin_amdgcn_raw_buffer_store_b128(o, rsO, ob + (2 * cp + c) * 4096, 0, SC1);
        }
      });
    }
    waitv<8>(); sbar();
    tag = mktag(epoch);
    if (threadIdx.x == 0) st32_sc1(tag, C_FW + j * 128, rsC);
    STAMP(2);
    {
      const int cc4 = 2 * wave + (lane >> 5), rp = lane & 31;
      const unsigned kb = KV_BASE + 4 * KV_CG;
      v4u kk[5], vv[2];
#pragma unroll
      for (int i = 0; i < 5; ++i) { const int row = 2 * rp + 5 + i; kk[i] = lds128(lds + kb + (row >> 3) * KV_GS + (row & 7) * 128 + ((cc4 ^ (4 * ((row >> 4) & 1))) << 4)); }
#pragma unroll
      for (int i = 0; i < 2; ++i) { const int row = 2 * rp + i; vv[i] = lds128(lds + kb + KV_V + (row >> 3) * KV_GS + (row & 7) * 128 + ((cc4 ^ (4 * ((row >> 4) & 1))) << 4)); }
#pragma unroll
      for (int i = 0; i < 4; ++i) asm volatile("" : "+v"(w4[i]));
      const unsigned ob = (64 * (cg0 + 4) + 8 * cc4) * 4096 + (64 * rbk + 2 * rp) * 2;
      kv_math<2>(kk, vv, w4, [&](int cp, const f32x2 (&rr)[2]) {
#pragma unroll
        for (int c = 0; c < 2; ++c) __builtin_amdgcn_raw_buffer_store_b32(pk2(rr[0][c], rr[1][c]), rsO, ob + (2 * cp + c) * 4096, 0, SC1);
      });
    }
    {
      float a = 0.f;
#pragma unroll
      for (int i = 0; i < 10; ++i) {
        asm volatile("" : "+v"(xx[i]), "+v"(gg[i]));
#pragma unroll
        for (int jj = 0; jj < 4; ++jj) { a = fmaf(blo(xx[i][jj]), blo(gg[i][jj]), a); a = fmaf(bhi(xx[i][jj]), bhi(gg[i][jj]), a); }
      }
      a += __shfl_xor(a, 1); a += __shfl_xor(a, 2); a += __shfl_xor(a, 4);
      const float gt = __builtin_amdgcn_rcpf(1.f + __builtin_amdgcn_exp2f(a * -1.44269504088896f));
      if (part == 0) __builtin_amdgcn_raw_buffer_store_b32(__builtin_bit_cast(unsigned, gt), rsGo, grow * 4, 0, SC1);
    }
    ld_h();
    waitl<0>(); sbar();
    waitv<0>();
    sbar();
    if (threadIdx.x == 0) st32_sc1(tag, C_FKV + j * 128, rsC);
    STAMP(3);
    fill_h();
    unsigned fw, fk;
    ld32_sc1(fw, C_FW + lane * 128, rsC);
    ld32_sc1(fk, C_FKV + lane * 128, rsC);
    waitv<0>();
    poll_finish(fw, C_FW + lane * 128, rsC, tag);
    poll_finish(fk, C_FKV + lane * 128, rsC, tag);
    ld_u();
    STAMP(4);
    static_for<0, 12>([&](auto I) { ldW(I); });
    waitv<12>();
    fill_u();
    waitl<0>();
  }
  STAMP(5);
  constexpr int NZ = G1R ? 4 : 8, W0 = G1R ? 24 : 12;
  f32x4 acc[8];
  {
    const unsigned aA = cb + q4 * CV_HC + 56 - 16 * g + 8 * p4 + 256 * half;
    const unsigned aB = cb + CV_U + 448 + 32 * c16 + 16 * g - 256 * half - 64 * (NZ - 1);
    v4u A[8], B[8];
#pragma unroll
    for (int i = 0; i < 8; ++i) {
      B[7 - i] = lds128(lds + aB + 64 * (NZ - 1) + 512 * (7 - i));
      A[i] = cat(ldstr(lds + aA + 512 * i + 8), ldstr(lds + aA + 512 * i));
    }
    SCHED();
    static_for<0, NZ>([&](auto Z) {
      constexpr int z = Z.value;
      static_for<0, 8>([&](auto I) {
        constexpr int i = I.value;
        static_for<0, 8 - i>([&](auto DD) {
          constexpr int d = 7 - i - DD.value;
          constexpr int gap = z * 36 + (i * (17 - i)) / 2 + DD.value;
          if constexpr (z == 0 && i == 0) mfma_z(acc[i + d], A[i], B[d]); else mfma_ip(acc[i + d], A[i], B[d]);
          if constexpr (d == 7 - i && z + 1 < NZ) B[7 - i] = lds128(lds + aB + 64 * (NZ - 2 - z) + 512 * (7 - i));
          constexpr int WSTEP = G1R ? 4 : 6;
          if constexpr (gap % WSTEP == 2) ldW(IC(W0 + gap / WSTEP));
          SCHED();
        });
        if constexpr (z + 1 < NZ) { A[i] = cat(ldstr(lds + aA + 64 * (z + 1) + 512 * i + 8), ldstr(lds + aA + 64 * (z + 1) + 512 * i)); SCHED(); }
      });
    });
  }
  static_assert(W0 + (NZ * 36 - 3) / (G1R ? 4 : 6) >= 59, "all W2 fragments issued");
  nop8(); SCHED();
  STAMP(6);
  const unsigned foH = C_FH + (((rb >> 1) * 12 + (lane < 24 ? lane >> 1 : 0)) * 4 + (rb & 1) + 2 * (lane & 1)) * 64;
  unsigned fh;
  ld32_sc1(fh, foH, rsC);
  int ctb = 0;
  if constexpr (G1R) {
    LAS char* ex = lds + CV_EX + cl * 8192;
f32x4 keep[4];
#pragma unroll
    for (int i = 0; i < 4; ++i) {
      f32x4 lo = acc[i], hi = acc[4 + i];
      asm volatile("" : "+v"(lo), "+v"(hi));
      *(LAS f32x4*)(ex + half * 4096 + i * 1024 + lane * 16) = half == 0 ? hi : lo;
      keep[i] = half == 0 ? lo : hi;
    }
    waitl<0>(); sbar();
#pragma unroll
    for (int i = 0; i < 4; ++i) acc[i] = keep[i] + *(LAS f32x4*)(ex + (1 - half) * 4096 + i * 1024 + lane * 16);
    ctb = 4 * half;
  }
  constexpr int NT = G1R ? 4 : 8;
  {
    asm volatile("" : "+v"(dv));
    const float Dc = blo(dv);
#pragma unroll
    for (int i = 0; i < NT; i += 2) {
      unsigned w0[2][2];
#pragma unroll
      for (int e = 0; e < 2; ++e) {
        const int ct = ctb + i + e;
        const f32x4 a = acc[i + e];
        const v2u uu = lds64(lds + cb + CV_U + 480 + 512 * ct + 32 * c16 + 8 * g);
        w0[e][0] = pk2(fmaf(blo(uu.x), Dc, a[0]), fmaf(bhi(uu.x), Dc, a[1]));
        w0[e][1] = pk2(fmaf(blo(uu.y), Dc, a[2]), fmaf(bhi(uu.y), Dc, a[3]));
      }
      auto s0 = __builtin_amdgcn_permlane16_swap(w0[0][0], w0[1][0], false, false);
      auto s1 = __builtin_amdgcn_permlane16_swap(w0[0][1], w0[1][1], false, false);
      const int ct = ctb + i + (g & 1);
      __builtin_amdgcn_raw_buffer_store_b128(v4u{s0[0], s1[0], s0[1], s1[1]}, rsY, ch * 4096 + 512 * ct + 32 * c16 + 8 * (g & 2), 0, SC1);
    }
  }
  waitl<0>(); sbar();
  STAMP(7);
  waitv<NT / 2>();
  poll_finish(fh, foH, rsC, tag);
  STAMP(8);
  FT(14);
  v4u Hf[HW];
  const unsigned sH = (4 * rb * 48 + 12 * wave) * 1024;
  static_for<0, HW>([&](auto I) { constexpr int i = I.value; ldV<SC1>(Hf[i], vl, rsH, sH + ((i / 12) * 48 + i % 12) * 1024); });
  FT(15);
  f32x4 ac2[5];
  const unsigned foY0 = C_FY + (((lane >> 2) * 8 + nt) * 4 + (lane & 3)) * 64, foY1 = foY0 + 16 * 8 * 4 * 64;
  unsigned fy0, fy1, gtu;
  v4u yv[3]; v2u qq[5];
  const int s = 64 * rb + 16 * wave + c16;
  constexpr int SP = 20;
  constexpr int SY = SP + HW + 1 < 47 ? SP + HW + 1 : 47;
  static_for<0, 48>([&](auto I) {
    constexpr int i = I.value, rt = i / 12, k = i % 12;
    constexpr int yH = (i + HW - 1 < 47 ? i + HW - 1 : 47) - i;
    constexpr int ex = ((0 < i && i <= HW) ? 1 : 0) + ((SP < i && i <= SP + HW) ? 8 : 0) + ((SY < i && i <= SY + HW) ? 3 : 0);
    waitv<yH + ex>();
    static_for<0, 5>([&](auto C) { constexpr int ct = C.value;
      if constexpr (k == 0) mfma_zA(ac2[ct], W[k][ct], Hf[i % HW]); else mfma_ipA(ac2[ct], W[k][ct], Hf[i % HW]);
    });
    if constexpr (i + HW < 48) { constexpr int n = i + HW; ldV<SC1>(Hf[n % HW], vl, rsH, sH + ((n / 12) * 48 + n % 12) * 1024); }
    if constexpr (i == 0) { if (lane == 0) st32_sc1(tag, C_FY + (blockIdx.x * 4 + wave) * 64, rsC); }
    if constexpr (i == SP) {
      ld32_sc1(fy0, foY0, rsC); ld32_sc1(fy1, foY1, rsC);
      const rsrc_t rsG = mkrs(p.ws + WS_G, S * 4), rsQ = mkrs(p.q, S * D * 2);
      ld32_sc1(gtu, s * 4, rsG);
#pragma unroll
      for (int ct = 0; ct < 5; ++ct) ldV2<0>(qq[ct], (s * D + 80 * nt + 16 * ct + 4 * g) * 2, rsQ, 0);
    }
    if constexpr (i == SY) {
      poll_finish(fy0, foY0, rsC, tag); poll_finish(fy1, foY1, rsC, tag);
#pragma unroll
      for (int u = 0; u < 3; ++u) {
        const int idx = u * 256 + wave * 64 + lane, d = idx >> 3, cch = idx & 7;
        ldV<SC1>(yv[u], idx < 640 ? (unsigned)((80 * nt + d) * 4096 + rb * 128 + cch * 16) : 0x80000000u, rsY, 0);
      }
    }
    if constexpr (i % 4 == 3) { FT(16 + i / 4); }
    if constexpr (k == 11) {
      nop8();
#pragma unroll
      for (int ct = 0; ct < 5; ++ct) *(LAS f32x4*)(lds + G2_PART + ((rt * 4 + wave) * 5 + ct) * 1024 + lane * 16) = ac2[ct];
    }
    SCHED();
  });
  STAMP(9);
  unsigned fe[16];
  if (blockIdx.x == 0 && wave == 0) {
#pragma unroll
    for (int i = 0; i < 16; ++i) ld32_sc1(fe[i], C_FY + (i * 64 + lane) * 64, rsC);
  }
  waitv<0>();
#pragma unroll
  for (int u = 0; u < 3; ++u) {
    asm volatile("" : "+v"(yv[u]));
    const int idx = u * 256 + wave * 64 + lane, d = idx >> 3, cch = idx & 7;
    if (idx < 640) { *(LAS v2u*)(lds + G2_Y + d * 136 + cch * 16) = v2u{yv[u].x, yv[u].y}; *(LAS v2u*)(lds + G2_Y + d * 136 + cch * 16 + 8) = v2u{yv[u].z, yv[u].w}; }
  }
  asm volatile("" : "+v"(gtu));
#pragma unroll
  for (int ct = 0; ct < 5; ++ct) asm volatile("" : "+v"(qq[ct]));
  waitl<0>(); sbar();
  STAMP(10);
  const float gt = __builtin_bit_cast(float, gtu);
#pragma unroll
  for (int ct = 0; ct < 5; ++ct) {
    f32x4 b = *(LAS f32x4*)(lds + G2_PART + ((wave * 4 + 0) * 5 + ct) * 1024 + lane * 16);
#pragma unroll
    for (int w = 1; w < 4; ++w) b += *(LAS f32x4*)(lds + G2_PART + ((wave * 4 + w) * 5 + ct) * 1024 + lane * 16);
    const v2u yy = ldstr(lds + G2_Y + (16 * ct + 4 * g + q4) * 136 + (16 * wave + 4 * p4) * 2);
    const float o0 = gt * (blo(qq[ct].x) * blo(yy.x)) + (1.f - gt) * b[0];
    const float o1 = gt * (bhi(qq[ct].x) * bhi(yy.x)) + (1.f - gt) * b[1];
    const float o2 = gt * (blo(qq[ct].y) * blo(yy.y)) + (1.f - gt) * b[2];
    const float o3 = gt * (bhi(qq[ct].y) * bhi(yy.y)) + (1.f - gt) * b[3];
    *(GAS v2u*)(p.out + s * D + 80 * nt + 16 * ct + 4 * g) = v2u{pk2(o0, o1), pk2(o2, o3)};
  }
  if (blockIdx.x == 0 && wave == 0) {
#pragma unroll 1
    for (int i = 0; i < 1; ++i) {
#pragma unroll
      for (int e = 0; e < 16; ++e) poll_finish(fe[e], C_FY + (e * 64 + lane) * 64, rsC, tag);
    }
    if (lane == 0) st32_sc1(tag, C_EPOCH, rsC);
  }
  STAMP(11);
  STAMP_FLUSH(); FT_FLUSH();
}

__global__ __launch_bounds__(256, 1) void fused(Params p) {
  extern __shared__ __attribute__((aligned(16))) char smem[];
  LAS char* lds = (LAS char*)smem;
  const int lane = threadIdx.x & 63, wave = __builtin_amdgcn_readfirstlane(threadIdx.x >> 6);
  const int x = blockIdx.x & 7, r = blockIdx.x >> 3;
  if (r < 24) body<true>(p, lds, x, r, wave, lane);
  else body<false>(p, lds, x, r, wave, lane);
}

extern "C" void kernel_launch(void* const* d_in, const int* in_sizes, int n_in, void* d_out, int out_size, void* d_ws, size_t ws_size, hipStream_t stream) {
  Params p;
  p.q = (const u16*)d_in[0]; p.k = (const u16*)d_in[1]; p.v = (const u16*)d_in[2]; p.sk = (const u16*)d_in[3];
  p.h = (const u16*)d_in[4]; p.Dv = (const u16*)d_in[5]; p.w1 = (const u16*)d_in[7]; p.w2 = (const u16*)d_in[8]; p.gw = (const u16*)d_in[9];
  p.out = (u16*)d_out; p.ws = (char*)d_ws;
  (void)hipFuncSetAttribute((const void*)fused, hipFuncAttributeMaxDynamicSharedMemorySize, LDS_BYTES);
  fused<<<256, 256, LDS_BYTES, stream>>>(p);
}
```

```cpp
#include <hip/hip_runtime.h>
#include <type_traits>
#include <stdint.h>

typedef unsigned short u16;
typedef unsigned v4u __attribute__((ext_vector_type(4)));
typedef unsigned v2u __attribute__((ext_vector_type(2)));
typedef float f32x4 __attribute__((ext_vector_type(4)));
typedef float f32x2 __attribute__((ext_vector_type(2)));
typedef __bf16 bf16x8 __attribute__((ext_vector_type(8)));
typedef __bf16 bf16x2 __attribute__((ext_vector_type(2)));
typedef short v4s __attribute__((ext_vector_type(4)));
typedef short v8s __attribute__((ext_vector_type(8)));
#define GAS __attribute__((address_space(1)))
#define LAS __attribute__((address_space(3)))
#define DEV __device__ __forceinline__
typedef __amdgpu_buffer_rsrc_t rsrc_t;

constexpr int S = 2048, D = 640, F = 1536;

template <int N> DEV void waitv() { asm volatile("s_waitcnt vmcnt(%0)" ::"i"(N) : "memory"); }
template <int N> DEV void waitl() { asm volatile("s_waitcnt lgkmcnt(%0)" ::"i"(N) : "memory"); }
DEV void sbar() { asm volatile("s_barrier" ::: "memory"); }
#define SCHED() __builtin_amdgcn_sched_barrier(0)
DEV rsrc_t mkrs(const void* p, unsigned bytes) { return __builtin_amdgcn_make_buffer_rsrc((void*)p, 0, bytes, 0x00020000); }
DEV f32x4 mfma(v4u a, v4u b, f32x4 c) {
  return __builtin_amdgcn_mfma_f32_16x16x32_bf16(__builtin_bit_cast(bf16x8, a), __builtin_bit_cast(bf16x8, b), c, 0, 0, 0);
}
DEV v4u lds128(LAS const char* p) { return *(LAS const v4u*)p; }
DEV v2u lds64(LAS const char* p) { return *(LAS const v2u*)p; }
DEV v2u ldstr(LAS const char* p) { return __builtin_bit_cast(v2u, __builtin_amdgcn_ds_read_tr16_b64_v4i16((LAS v4s*)p)); }
DEV v4u cat(v2u a, v2u b) { return v4u{a.x, a.y, b.x, b.y}; }
DEV unsigned pk2(float a, float b) { bf16x2 r = __builtin_convertvector(f32x2{a, b}, bf16x2); return __builtin_bit_cast(unsigned, r); }
DEV float blo(unsigned d) { return __builtin_bit_cast(float, d << 16); }
DEV float bhi(unsigned d) { return __builtin_bit_cast(float, d & 0xffff0000u); }
DEV void setm0(unsigned v) { asm volatile("s_mov_b32 m0, %0\n\ts_nop 0" ::"s"(v)); }
template <int OFF> DEV void dma16(unsigned voff, rsrc_t rs, unsigned soff) {
  asm volatile("buffer_load_dwordx4 %0, %1, %2 offen offset:%3 lds" ::"v"(voff), "s"(rs), "s"(soff), "i"(OFF));
}
typedef float f32x16 __attribute__((ext_vector_type(16)));
DEV f32x16 mfma32(v4u a, v4u b, f32x16 c) {
  return __builtin_amdgcn_mfma_f32_32x32x16_bf16(__builtin_bit_cast(bf16x8, a), __builtin_bit_cast(bf16x8, b), c, 0, 0, 0);
}
template <int IMM> DEV void setm0_add(unsigned base) { asm volatile("s_add_u32 m0, %0, %1" ::"s"(base), "i"(IMM) : "scc"); }
template <int I, int N, class Fn> DEV void static_for(Fn&& f) { if constexpr (I < N) { f(std::integral_constant<int, I>{}); static_for<I + 1, N>(f); } }
DEV void mfma_ip(f32x4& c, const v4u& a, const v4u& b) { asm volatile("v_mfma_f32_16x16x32_bf16 %0, %1, %2, %0" : "+v"(c) : "v"(a), "v"(b)); }
DEV void mfma_ipA(f32x4& c, const v4u& a, const v4u& b) { asm volatile("v_mfma_f32_16x16x32_bf16 %0, %1, %2, %0" : "+v"(c) : "a"(a), "v"(b)); }
DEV void mfma_z(f32x4& c, const v4u& a, const v4u& b) { asm volatile("v_mfma_f32_16x16x32_bf16 %0, %1, %2, 0" : "=&v"(c) : "v"(a), "v"(b)); }
DEV void mfma_zA(f32x4& c, const v4u& a, const v4u& b) { asm volatile("v_mfma_f32_16x16x32_bf16 %0, %1, %2, 0" : "=&v"(c) : "a"(a), "v"(b)); }
template <int AUX> DEV void ldA(v4u& d, unsigned voff, rsrc_t rs, unsigned soff) {
  if constexpr (AUX) asm volatile("buffer_load_dwordx4 %0, %1, %2, %3 offen sc1" : "=a"(d) : "v"(voff), "s"(rs), "s"(soff));
  else asm volatile("buffer_load_dwordx4 %0, %1, %2, %3 offen" : "=a"(d) : "v"(voff), "s"(rs), "s"(soff));
}
template <int AUX> DEV void ldV(v4u& d, unsigned voff, rsrc_t rs, unsigned soff) {
  if constexpr (AUX) asm volatile("buffer_load_dwordx4 %0, %1, %2, %3 offen sc1" : "=v"(d) : "v"(voff), "s"(rs), "s"(soff));
  else asm volatile("buffer_load_dwordx4 %0, %1, %2, %3 offen" : "=v"(d) : "v"(voff), "s"(rs), "s"(soff));
}
DEV void nop8() { asm volatile("s_nop 7"); }

constexpr size_t WS_CTRL = 0;
constexpr size_t WS_KVT = 262144;
constexpr size_t WS_YT = WS_KVT + (size_t)D * S * 2;
constexpr size_t WS_W2T = WS_YT + (size_t)D * S * 2;
constexpr size_t WS_HF = WS_W2T + (size_t)8 * 48 * 5 * 1024;
constexpr size_t WS_G = WS_HF + (size_t)128 * 48 * 1024;
constexpr size_t WS_END = WS_G + S * 4;
constexpr unsigned C_EPOCH = 0, C_TMO = 256;
constexpr unsigned C_FW = 4096;
constexpr unsigned C_FKV = 16384;
constexpr unsigned C_FH = 32768;
constexpr unsigned C_FY = 98304;
constexpr unsigned C_BYTES = 262144;

#define NSTAMP 16
#define STAMP_OFF (size_t)(32u << 20)
#define STAMP_DECL
#define STAMP(i)
#define STAMP_FLUSH()
#define FT_DECL
#define FT(i)
#define FT_FLUSH()

struct Params {
  const u16 *q, *k, *v, *sk, *h, *Dv, *w1, *w2, *gw;
  u16* out;
  char* ws;
};

constexpr int LDS_BYTES = 160 * 1024;
constexpr int SC1 = 16;
#define IC(n) std::integral_constant<int, (n)>{}

DEV void ld32_sc1(unsigned& v, unsigned voff, rsrc_t rs) { asm volatile("buffer_load_dword %0, %1, %2, 0 offen sc1" : "=v"(v) : "v"(voff), "s"(rs)); }
DEV void st32_sc1(unsigned v, unsigned voff, rsrc_t rs) { asm volatile("buffer_store_dword %0, %1, %2, 0 offen sc1" ::"v"(v), "v"(voff), "s"(rs)); }
DEV void poll_finish(unsigned v, unsigned voff, rsrc_t rsC, unsigned tag) {
  asm volatile("" : "+v"(v));
  if (__builtin_amdgcn_ballot_w64(v != tag) == 0) return;
#pragma unroll 1
  for (int spins = 0; spins < (1 << 15); ++spins) {
    __builtin_amdgcn_s_sleep(1);
    ld32_sc1(v, voff, rsC);
    asm volatile("s_waitcnt vmcnt(0)" : "+v"(v)::"memory");
    if (__builtin_amdgcn_ballot_w64(v != tag) == 0) return;
  }
  st32_sc1(1u, C_TMO, rsC);
}

DEV void mfma32_ip(f32x16& c, const v4u& a, const v4u& b) { asm volatile("v_mfma_f32_32x32x16_bf16 %0, %1, %2, %0" : "+v"(c) : "v"(a), "v"(b)); }
DEV void mfma32_z(f32x16& c, const v4u& a, const v4u& b) { asm volatile("v_mfma_f32_32x32x16_bf16 %0, %1, %2, 0" : "=&v"(c) : "v"(a), "v"(b)); }

constexpr int KV_BASE = 32768, KV_GS = 1152, KV_V = 9 * KV_GS, KV_CG = 17 * KV_GS;
constexpr int CV_BASE = 32768;
constexpr int CV_HC = 4160;
constexpr int CV_U = 4 * CV_HC;
constexpr int CV_CH = CV_U + 4608;
constexpr int CV_EX = CV_BASE + 4 * CV_CH;
constexpr int G2_PART = 0;
constexpr int G2_Y = 81920;
constexpr int HW = 24;

template <int AUX> DEV void ldV2(v2u& d, unsigned voff, rsrc_t rs, unsigned soff) {
  if constexpr (AUX) asm volatile("buffer_load_dwordx2 %0, %1, %2, %3 offen sc1" : "=v"(d) : "v"(voff), "s"(rs), "s"(soff));
  else asm volatile("buffer_load_dwordx2 %0, %1, %2, %3 offen" : "=v"(d) : "v"(voff), "s"(rs), "s"(soff));
}
DEV void dma16s(unsigned voff, rsrc_t rs, unsigned soff, unsigned m0v) {
  asm volatile("s_mov_b32 m0, %3\n\ts_nop 0\n\tbuffer_load_dwordx4 %0, %1, %2 offen lds" ::"v"(voff), "s"(rs), "s"(soff), "s"(m0v));
}
DEV unsigned mktag(unsigned& epoch) {
  asm volatile("" : "+v"(epoch));
  return __builtin_amdgcn_readfirstlane(epoch * 0x9E3779B1u + 0x7F4A7C15u);
}

template <int NR, class ST> DEV void kv_math(const v4u (&kk)[NR + 3], const v4u (&vv)[NR], const v4u (&ww)[4], ST&& st) {
#pragma unroll
  for (int cp = 0; cp < 4; ++cp) {
    f32x2 w[4];
    w[0] = f32x2{blo(ww[cp].x), blo(ww[cp].z)}; w[1] = f32x2{bhi(ww[cp].x), bhi(ww[cp].z)};
    w[2] = f32x2{blo(ww[cp].y), blo(ww[cp].w)}; w[3] = f32x2{bhi(ww[cp].y), bhi(ww[cp].w)};
    f32x2 kf[NR + 3];
#pragma unroll
    for (int i = 0; i < NR + 3; ++i) kf[i] = f32x2{blo(kk[i][cp]), bhi(kk[i][cp])};
    f32x2 r[NR];
#pragma unroll
    for (int i = 0; i < NR; ++i) {
      f32x2 a = kf[i + 3] * w[0];
      a = __builtin_elementwise_fma(kf[i + 2], w[1], a);
      a = __builtin_elementwise_fma(kf[i + 1], w[2], a);
      a = __builtin_elementwise_fma(kf[i], w[3], a);
      r[i] = a * f32x2{blo(vv[i][cp]), bhi(vv[i][cp])};
    }
    st(cp, r);
  }
}

template <bool G1R>
DEV void body(const Params& p, LAS char* lds, const int x, const int r, const int wave, const int lane) {
  const int g = lane >> 4, q4 = (lane >> 2) & 3, p4 = lane & 3, c16 = lane & 15;
  STAMP_DECL; FT_DECL;
  STAMP(0);
  const rsrc_t rsC = mkrs(p.ws + WS_CTRL, C_BYTES);
  unsigned epoch, tag;
  ld32_sc1(epoch, C_EPOCH, rsC);
  const int cl = G1R ? (wave >> 1) : wave, half = G1R ? (wave & 1) : 0;
  const int ch = (G1R ? 80 * x + 2 * r : 80 * x + 48 + 4 * (r - 24)) + cl;
  const unsigned cb = CV_BASE + cl * CV_CH;
  constexpr int NCHK = G1R ? 2 : 4;
  unsigned dv;
  { const rsrc_t rsD = mkrs(p.Dv, D * 2); asm volatile("buffer_load_ushort %0, %1, %2, 0 offen" : "=v"(dv) : "v"(ch * 2), "s"(rsD)); }
  const rsrc_t rsHh = mkrs(p.h, D * S * 2), rsU = mkrs(p.ws + WS_KVT, D * S * 2), rsY = mkrs(p.ws + WS_YT, D * S * 2);
  const rsrc_t rsW2 = mkrs(p.ws + WS_W2T, 8 * 48 * 5 * 1024), rsH = mkrs(p.ws + WS_HF, 128 * 48 * 1024);
  const int rb = 4 * x + (r & 3), nt = r >> 2;
  const unsigned vl = lane * 16;
  const unsigned sW = (nt * 48 + 12 * wave) * 5 * 1024;
  v4u W[12][5];
  auto ldW = [&](auto I) { constexpr int i = I.value; if constexpr (i < 60) ldA<SC1>(W[i / 5][i % 5], vl, rsW2, sW + i * 1024); };
  v4u hc[NCHK], uc[NCHK]; v2u hp[NCHK];
  auto ld_h = [&]() {
#pragma unroll
    for (int i = 0; i < NCHK; ++i) {
      const int n = (half * NCHK + i) * 64 + lane;
      ldV<0>(hc[i], ch * 4096 + n * 16, rsHh, 0);
      ldV2<0>(hp[i], n > 0 ? (unsigned)(ch * 4096 + n * 16 - 8) : 0x80000000u, rsHh, 0);
    }
  };
  auto ld_u = [&]() {
#pragma unroll
    for (int i = 0; i < NCHK; ++i) ldV<SC1>(uc[i], ch * 4096 + ((half * NCHK + i) * 64 + lane) * 16, rsU, 0);
  };
  auto fill_h = [&]() {
#pragma unroll
    for (int i = 0; i < NCHK; ++i) {
      asm volatile("" : "+v"(hc[i]), "+v"(hp[i]));
      const int n = (half * NCHK + i) * 64 + lane;
      const v4u cur = hc[i]; const v2u prv = hp[i];
      const unsigned om2 = __builtin_amdgcn_alignbit(prv.y, prv.x, 16), om1 = __builtin_amdgcn_alignbit(cur.x, prv.y, 16);
      const unsigned o0 = __builtin_amdgcn_alignbit(cur.y, cur.x, 16), o1 = __builtin_amdgcn_alignbit(cur.z, cur.y, 16), o2 = __builtin_amdgcn_alignbit(cur.w, cur.z, 16);
      LAS char* dst = lds + cb + (n + 2) * 16;
      *(LAS v4u*)(dst) = cur;
      *(LAS v4u*)(dst + CV_HC) = v4u{om1, o0, o1, o2};
      *(LAS v4u*)(dst + 2 * CV_HC) = v4u{prv.y, cur.x, cur.y, cur.z};
      *(LAS v4u*)(dst + 3 * CV_HC) = v4u{om2, om1, o0, o1};
    }
    if (half == 0) {
      if (lane < 8) *(LAS v4u*)(lds + cb + (lane >> 1) * CV_HC + (lane & 1) * 16) = v4u{0, 0, 0, 0};
      if (lane >= 32 && lane < 62) *(LAS v4u*)(lds + cb + CV_U + (lane - 32) * 16) = v4u{0, 0, 0, 0};
    }
  };
  auto fill_u = [&]() {
#pragma unroll
    for (int i = 0; i < NCHK; ++i) {
      asm volatile("" : "+v"(uc[i]));
      *(LAS v4u*)(lds + cb + CV_U + 480 + ((half * NCHK + i) * 64 + lane) * 16) = uc[i];
    }
  };

  if constexpr (G1R) {
    const int sb = 4 * (x >> 1) + (r & 3), fs = 6 * (x & 1) + (r >> 2);
    f32x16 acc1[2][2];
    const rsrc_t rsX = mkrs(p.q, S * D * 2), rsW = mkrs(p.w1, D * F * 2);
    unsigned vX[4], vW[4];
#pragma unroll
    for (int ii = 0; ii < 4; ++ii) {
      vX[ii] = (32 * wave + 8 * ii + (lane >> 3)) * 1280 + (((lane & 7) ^ (4 * (ii & 1) + g)) << 4) - 1024 * ii;
      vW[ii] = (16 * wave + 4 * ii + g) * 3072 + (((((c16 >> 1) ^ (g << 1)) << 1) | (c16 & 1)) << 4) - 1024 * ii;
    }
    const unsigned sX0 = sb * 128 * 1280, sW0 = fs * 256;
    const unsigned mX = wave * 4096, mW = 16384 + wave * 4096;
    auto dmaX = [&](auto T, auto I) { constexpr int t = T.value, i = I.value; if constexpr (t < 10) dma16<1024 * i>(vX[i], rsX, sX0 + t * 128); };
    auto dmaW = [&](auto T, auto I) { constexpr int t = T.value, i = I.value; if constexpr (t < 10) dma16<1024 * i>(vW[i], rsW, sW0 + t * 64 * 3072); };
    auto m0X = [&](auto T) { constexpr int t = T.value; if constexpr (t < 10) setm0_add<(t % 5) * 32768>(mX); };
    auto m0W = [&](auto T) { constexpr int t = T.value; if constexpr (t < 10) setm0_add<(t % 5) * 32768>(mW); };
    static_for<0, 4>([&](auto T) {
      m0X(T); asm volatile("s_nop 0");
      static_for<0, 4>([&](auto I) { dmaX(T, I); });
      if constexpr (T.value < 3) { m0W(T); asm volatile("s_nop 0"); static_for<0, 4>([&](auto I) { dmaW(T, I); }); }
    });
    const int ws_ = wave & 1, wf = wave >> 1, h = lane >> 5, r32 = lane & 31;
    unsigned aX[3][4], aW[3][2];
#pragma unroll
    for (int w = 0; w < 3; ++w) {
#pragma unroll
      for (int ks = 0; ks < 4; ++ks) aX[w][ks] = w * 65536 + (64 * ws_ + r32) * 128 + (((2 * ks + h) ^ ((r32 >> 1) & 7)) << 4);
#pragma unroll
      for (int ft = 0; ft < 2; ++ft) aW[w][ft] = w * 65536 + 16384 + (8 * h + q4) * 256 + (((2 * (2 * wf + ft) + (g & 1)) ^ (q4 << 1)) << 5) + 8 * p4;
    }
    v4u A[2][2][2], B[2][2][2];
    v2u At;
    auto rd = [&](auto T, auto HF, auto SET, auto N) {
      constexpr int t = T.value, hf = HF.value, set = SET.value, n = N.value;
      if constexpr (t < 10) {
        constexpr int b = t % 5, w = b >> 1, bo = (b & 1) * 32768;
        constexpr int kk = n / 6, rr = n % 6, ks = 2 * hf + kk;
        if constexpr (rr == 0) B[set][kk][0] = lds128(lds + aX[w][ks] + bo);
        else if constexpr (rr == 3) B[set][kk][1] = lds128(lds + aX[w][ks] + bo + 32 * 128);
        else { constexpr int ft = rr / 3, jj = (rr % 3) - 1;
          v2u xx = ldstr(lds + aW[w][ft] + bo + ks * 4096 + jj * 1024);
          if constexpr (jj == 0) At = xx; else A[set][kk][ft] = cat(At, xx); }
      }
    };
    unsigned fw, fk;
    waitv<20>(); sbar();
    STAMP(1);
    static_for<0, 12>([&](auto N) { rd(IC(0), IC(0), IC(0), N); });
    SCHED();
    static_for<0, 10>([&](auto T) {
      constexpr int t = T.value;
      static_for<0, 2>([&](auto HF) {
        constexpr int hf = HF.value;
        static_for<0, 8>([&](auto M) {
          constexpr int m = M.value, kk = m >> 2, ft = (m >> 1) & 1, st = m & 1;
          if constexpr (t == 0 && hf == 0 && kk == 0) mfma32_z(acc1[ft][st], A[hf][kk][ft], B[hf][kk][st]);
          else mfma32_ip(acc1[ft][st], A[hf][kk][ft], B[hf][kk][st]);
          if constexpr (m < 6) {
            if constexpr (hf == 0) { rd(T, IC(1), IC(1), IC(2 * m)); rd(T, IC(1), IC(1), IC(2 * m + 1)); }
            else { rd(IC(t + 1), IC(0), IC(0), IC(2 * m)); rd(IC(t + 1), IC(0), IC(0), IC(2 * m + 1)); }
          }
          if constexpr (m == 0) { if constexpr (hf == 0) m0W(IC(t + 3)); else m0X(IC(t + 4)); }
          if constexpr (m & 1) { if constexpr (hf == 0) dmaW(IC(t + 3), IC(m >> 1)); else dmaX(IC(t + 4), IC(m >> 1)); }
          if constexpr (t == 6 && hf == 1 && m == 1) ld32_sc1(fw, C_FW + lane * 128, rsC);
          if constexpr (t == 6 && hf == 1 && m == 3) ld32_sc1(fk, C_FKV + lane * 128, rsC);
          if constexpr (t == 6 && hf == 1 && m == 5) ld_h();
          SCHED();
        });
        if constexpr (hf == 0 && t < 9) {
          constexpr int younger = (t + 2 < 10 ? 8 : 0) + (t + 3 < 10 ? 8 : 0) + (t >= 7 ? 6 : 0);
          waitv<younger>(); sbar(); SCHED();
        }
      });
      FT(t); SCHED();
    });
    asm volatile("s_nop 15");
    waitv<0>();
    sbar();
    STAMP(2);
    tag = mktag(epoch);
    poll_finish(fw, C_FW + lane * 128, rsC, tag);
    poll_finish(fk, C_FKV + lane * 128, rsC, tag);
    STAMP(3);
    ld_u();
    const unsigned vo = (g & 1) * (48 * 1024) + (c16 + 16 * h) * 16;
    static_for<0, 4>([&](auto Q) {
      constexpr int qd = Q.value, st = qd >> 1, ft = qd & 1;
      const f32x16 a = acc1[ft][st];
      unsigned o[8];
      static_for<0, 8>([&](auto E) {
        constexpr int e = 2 * E.value;
        const float r0 = a[e] * __builtin_amdgcn_rcpf(1.f + __builtin_amdgcn_exp2f(a[e] * -1.44269504088896f));
        const float r1 = a[e + 1] * __builtin_amdgcn_rcpf(1.f + __builtin_amdgcn_exp2f(a[e + 1] * -1.44269504088896f));
        o[e >> 1] = pk2(r0, r1);
        if constexpr ((E.value % 4) != 3) ldW(IC(qd * 6 + E.value - E.value / 4));
        SCHED();
      });
      const unsigned so = ((8 * sb + 4 * ws_ + 2 * st) * 48 + 4 * fs + 2 * wf + ft) * 1024;
      __builtin_amdgcn_raw_buffer_store_b128(v4u{o[0], o[1], o[4], o[5]}, rsH, vo, so, SC1);
      __builtin_amdgcn_raw_buffer_store_b128(v4u{o[2], o[3], o[6], o[7]}, rsH, vo + 512, so, SC1);
      SCHED();
    });
    STAMP(4);
    waitv<32>();
    fill_h(); fill_u();
    waitv<0>();
    if (lane == 0) st32_sc1(tag, C_FH + ((sb * 12 + fs) * 4 + wave) * 64, rsC);
    waitl<0>(); sbar();
  } else {
    const int j = (r - 24) * 8 + x, rbk = j >> 1, cg0 = 5 * (j & 1);
    const rsrc_t rsK = mkrs(p.k, S * D * 2), rsV = mkrs(p.v, S * D * 2), rsS = mkrs(p.sk, D * 8), rsO = mkrs(p.ws + WS_KVT, D * S * 2);
    const unsigned vr = (lane >> 3) * 1280 + ((lane & 7) << 4), vr4 = vr ^ 64;
    auto dmaK = [&](int cgl, int G) {
      const int row = 64 * rbk - 8 + 8 * G;
      dma16s(((G >> 1) & 1) ? vr4 : vr, rsK, row >= 0 ? (unsigned)(row * 1280 + (cg0 + cgl) * 128) : 0x80000000u, KV_BASE + cgl * KV_CG + G * KV_GS);
    };
    auto dmaV = [&](int cgl, int G) {
      dma16s(((G >> 1) & 1) ? vr4 : vr, rsV, (64 * rbk + 8 * G) * 1280 + (cg0 + cgl) * 128, KV_BASE + cgl * KV_CG + KV_V + G * KV_GS);
    };
    const int id = 4 * j + wave, pk = id / 5, blk = id % 5;
    const rsrc_t rsWs = mkrs(p.w2, F * D * 2);
    const unsigned ldsb = wave * 8192;
#pragma unroll
    for (int i = 0; i < 8; ++i) {
      const int row = 4 * i + g;
      dma16s((32 * pk + row) * 1280 + blk * 256 + (((((c16 >> 1) ^ (row & 7)) << 1) | (c16 & 1)) << 4), rsWs, id < 240 ? 0u : 0x80000000u, ldsb + i * 1024);
    }
#pragma unroll
    for (int G = 0; G < 9; ++G) dmaK(wave, G);
#pragma unroll
    for (int G = 0; G < 8; ++G) dmaV(wave, G);
    dmaK(4, wave); dmaK(4, wave + 4); dmaV(4, wave); dmaV(4, wave + 4);
    if (wave == 0) dmaK(4, 8);
    const int cc = lane >> 3, rg = lane & 7;
    v4u ww[4], w4[4];
#pragma unroll
    for (int i = 0; i < 4; ++i) ldV<0>(ww[i], (64 * (cg0 + wave) + 8 * cc) * 8 + i * 16, rsS, 0);
#pragma unroll
    for (int i = 0; i < 4; ++i) ldV<0>(w4[i], (64 * (cg0 + 4) + 8 * (2 * wave + (lane >> 5))) * 8 + i * 16, rsS, 0);
    const rsrc_t rsX = mkrs(p.q, S * D * 2), rsG = mkrs(p.gw, D * 2), rsGo = mkrs(p.ws + WS_G, S * 4);
    const int grow = 8 * id + (lane >> 3), part = lane & 7;
    v4u xx[10], gg[10];
#pragma unroll
    for (int i = 0; i < 10; ++i) { ldV<0>(xx[i], grow * 1280 + i * 128 + part * 16, rsX, 0); ldV<0>(gg[i], i * 128 + part * 16, rsG, 0); }
    waitv<49>();
    STAMP(1);
    {
      const rsrc_t rsWo = mkrs(p.ws + WS_W2T, 8 * 48 * 5 * 1024);
      const int r0 = 4 * g + q4;
#pragma unroll
      for (int i = 0; i < 8; ++i) {
        const unsigned a = ldsb + r0 * 256 + ((i ^ (r0 & 7)) << 5) + 8 * p4;
        const v4u fr = cat(ldstr(lds + a), ldstr(lds + a + 4096));
        const int ctg = 8 * blk + i, ntt = ctg / 5, ct = ctg % 5;
        __builtin_amdgcn_raw_buffer_store_b128(fr, rsWo, vl, id < 240 ? (unsigned)(((ntt * 48 + pk) * 5 + ct) * 1024) : 0x80000000u, SC1);
      }
    }
    waitv<32>();
    {
      const unsigned kb = KV_BASE + wave * KV_CG;
      const unsigned a0 = kb + rg * KV_GS + ((cc ^ (4 * ((rg >> 1) & 1))) << 4), a1 = kb + (rg + 1) * KV_GS + ((cc ^ (4 * (((rg + 1) >> 1) & 1))) << 4);
      v4u kk[11], vv[8];
#pragma unroll
      for (int i = 0; i < 11; ++i) kk[i] = i < 3 ? lds128(lds + a0 + (5 + i) * 128) : lds128(lds + a1 + (i - 3) * 128);
#pragma unroll
      for (int i = 0; i < 8; ++i) vv[i] = lds128(lds + a0 + KV_V + i * 128);
#pragma unroll
      for (int i = 0; i < 4; ++i) asm volatile("" : "+v"(ww[i]));
      const unsigned ob = (64 * (cg0 + wave) + 8 * cc) * 4096 + (64 * rbk + 8 * rg) * 2;
      kv_math<8>(kk, vv, ww, [&](int cp, const f32x2 (&rr)[8]) {
#pragma unroll
        for (int c = 0; c < 2; ++c) {
          v4u o{pk2(rr[0][c], rr[1][c]), pk2(rr[2][c], rr[3][c]), pk2(rr[4][c], rr[5][c]), pk2(rr[6][c], rr[7][c])};
          __builtin_amdgcn_raw_buffer_store_b128(o, rsO, ob + (2 * cp + c) * 4096, 0, SC1);
        }
      });
    }
    waitv<8>(); sbar();
    tag = mktag(epoch);
    if (threadIdx.x == 0) st32_sc1(tag, C_FW + j * 128, rsC);
    STAMP(2);
    {
      const int cc4 = 2 * wave + (lane >> 5), rp = lane & 31;
      const unsigned kb = KV_BASE + 4 * KV_CG;
      v4u kk[5], vv[2];
#pragma unroll
      for (int i = 0; i < 5; ++i) { const int row = 2 * rp + 5 + i; kk[i] = lds128(lds + kb + (row >> 3) * KV_GS + (row & 7) * 128 + ((cc4 ^ (4 * ((row >> 4) & 1))) << 4)); }
#pragma unroll
      for (int i = 0; i < 2; ++i) { const int row = 2 * rp + i; vv[i] = lds128(lds + kb + KV_V + (row >> 3) * KV_GS + (row & 7) * 128 + ((cc4 ^ (4 * ((row >> 4) & 1))) << 4)); }
#pragma unroll
      for (int i = 0; i < 4; ++i) asm volatile("" : "+v"(w4[i]));
      const unsigned ob = (64 * (cg0 + 4) + 8 * cc4) * 4096 + (64 * rbk + 2 * rp) * 2;
      kv_math<2>(kk, vv, w4, [&](int cp, const f32x2 (&rr)[2]) {
#pragma unroll
        for (int c = 0; c < 2; ++c) __builtin_amdgcn_raw_buffer_store_b32(pk2(rr[0][c], rr[1][c]), rsO, ob + (2 * cp + c) * 4096, 0, SC1);
      });
    }
    {
      float a = 0.f;
#pragma unroll
      for (int i = 0; i < 10; ++i) {
        asm volatile("" : "+v"(xx[i]), "+v"(gg[i]));
#pragma unroll
        for (int jj = 0; jj < 4; ++jj) { a = fmaf(blo(xx[i][jj]), blo(gg[i][jj]), a); a = fmaf(bhi(xx[i][jj]), bhi(gg[i][jj]), a); }
      }
      a += __shfl_xor(a, 1); a += __shfl_xor(a, 2); a += __shfl_xor(a, 4);
      const float gt = __builtin_amdgcn_rcpf(1.f + __builtin_amdgcn_exp2f(a * -1.44269504088896f));
      if (part == 0) __builtin_amdgcn_raw_buffer_store_b32(__builtin_bit_cast(unsigned, gt), rsGo, grow * 4, 0, SC1);
    }
    ld_h();
    waitl<0>(); sbar();
    waitv<0>();
    sbar();
    if (threadIdx.x == 0) st32_sc1(tag, C_FKV + j * 128, rsC);
    STAMP(3);
    fill_h();
    unsigned fw, fk;
    ld32_sc1(fw, C_FW + lane * 128, rsC);
    ld32_sc1(fk, C_FKV + lane * 128, rsC);
    waitv<0>();
    poll_finish(fw, C_FW + lane * 128, rsC, tag);
    poll_finish(fk, C_FKV + lane * 128, rsC, tag);
    ld_u();
    STAMP(4);
    static_for<0, 12>([&](auto I) { ldW(I); });
    waitv<12>();
    fill_u();
    waitl<0>();
  }
  STAMP(5);
  constexpr int NZ = G1R ? 4 : 8, W0 = G1R ? 24 : 12;
  f32x4 acc[8];
  {
    const unsigned aA = cb + q4 * CV_HC + 56 - 16 * g + 8 * p4 + 256 * half;
    const unsigned aB = cb + CV_U + 448 + 32 * c16 + 16 * g - 256 * half - 64 * (NZ - 1);
    v4u A[8], B[8];
#pragma unroll
    for (int i = 0; i < 8; ++i) {
      B[7 - i] = lds128(lds + aB + 64 * (NZ - 1) + 512 * (7 - i));
      A[i] = cat(ldstr(lds + aA + 512 * i + 8), ldstr(lds + aA + 512 * i));
    }
    SCHED();
    static_for<0, NZ>([&](auto Z) {
      constexpr int z = Z.value;
      static_for<0, 8>([&](auto I) {
        constexpr int i = I.value;
        static_for<0, 8 - i>([&](auto DD) {
          constexpr int d = 7 - i - DD.value;
          constexpr int gap = z * 36 + (i * (17 - i)) / 2 + DD.value;
          if constexpr (z == 0 && i == 0) mfma_z(acc[i + d], A[i], B[d]); else mfma_ip(acc[i + d], A[i], B[d]);
          if constexpr (d == 7 - i && z + 1 < NZ) B[7 - i] = lds128(lds + aB + 64 * (NZ - 2 - z) + 512 * (7 - i));
          constexpr int WSTEP = G1R ? 4 : 6;
          if constexpr (gap % WSTEP == 2) ldW(IC(W0 + gap / WSTEP));
          SCHED();
        });
        if constexpr (z + 1 < NZ) { A[i] = cat(ldstr(lds + aA + 64 * (z + 1) + 512 * i + 8), ldstr(lds + aA + 64 * (z + 1) + 512 * i)); SCHED(); }
      });
    });
  }
  static_assert(W0 + (NZ * 36 - 3) / (G1R ? 4 : 6) >= 59, "all W2 fragments issued");
  nop8(); SCHED();
  STAMP(6);
  const unsigned foH = C_FH + (((rb >> 1) * 12 + (lane < 24 ? lane >> 1 : 0)) * 4 + (rb & 1) + 2 * (lane & 1)) * 64;
  unsigned fh;
  ld32_sc1(fh, foH, rsC);
  int ctb = 0;
  if constexpr (G1R) {
    LAS char* ex = lds + CV_EX + cl * 8192;
f32x4 keep[4];
#pragma unroll
    for (int i = 0; i < 4; ++i) {
      f32x4 lo = acc[i], hi = acc[4 + i];
      asm volatile("" : "+v"(lo), "+v"(hi));
      *(LAS f32x4*)(ex + half * 4096 + i * 1024 + lane * 16) = half == 0 ? hi : lo;
      keep[i] = half == 0 ? lo : hi;
    }
    waitl<0>(); sbar();
#pragma unroll
    for (int i = 0; i < 4; ++i) acc[i] = keep[i] + *(LAS f32x4*)(ex + (1 - half) * 4096 + i * 1024 + lane * 16);
    ctb = 4 * half;
  }
  constexpr int NT = G1R ? 4 : 8;
  {
    asm volatile("" : "+v"(dv));
    const float Dc = blo(dv);
#pragma unroll
    for (int i = 0; i < NT; i += 2) {
      unsigned w0[2][2];
#pragma unroll
      for (int e = 0; e < 2; ++e) {
        const int ct = ctb + i + e;
        const f32x4 a = acc[i + e];
        const v2u uu = lds64(lds + cb + CV_U + 480 + 512 * ct + 32 * c16 + 8 * g);
        w0[e][0] = pk2(fmaf(blo(uu.x), Dc, a[0]), fmaf(bhi(uu.x), Dc, a[1]));
        w0[e][1] = pk2(fmaf(blo(uu.y), Dc, a[2]), fmaf(bhi(uu.y), Dc, a[3]));
      }
      auto s0 = __builtin_amdgcn_permlane16_swap(w0[0][0], w0[1][0], false, false);
      auto s1 = __builtin_amdgcn_permlane16_swap(w0[0][1], w0[1][1], false, false);
      const int ct = ctb + i + (g & 1);
      __builtin_amdgcn_raw_buffer_store_b128(v4u{s0[0], s1[0], s0[1], s1[1]}, rsY, ch * 4096 + 512 * ct + 32 * c16 + 8 * (g & 2), 0, SC1);
    }
  }
  waitl<0>(); sbar();
  STAMP(7);
  waitv<NT / 2>();
  poll_finish(fh, foH, rsC, tag);
  STAMP(8);
  FT(14);
  v4u Hf[HW];
  const unsigned sH = (4 * rb * 48 + 12 * wave) * 1024;
  static_for<0, HW>([&](auto I) { constexpr int i = I.value; ldV<SC1>(Hf[i], vl, rsH, sH + ((i / 12) * 48 + i % 12) * 1024); });
  FT(15);
  f32x4 ac2[5];
  const unsigned foY0 = C_FY + (((lane >> 2) * 8 + nt) * 4 + (lane & 3)) * 64, foY1 = foY0 + 16 * 8 * 4 * 64;
  unsigned fy0, fy1, gtu;
  v4u yv[3]; v2u qq[5];
  const int s = 64 * rb + 16 * wave + c16;
  constexpr int SP = 20;
  constexpr int SY = SP + HW + 1 < 47 ? SP + HW + 1 : 47;
  static_for<0, 48>([&](auto I) {
    constexpr int i = I.value, rt = i / 12, k = i % 12;
    constexpr int yH = (i + HW - 1 < 47 ? i + HW - 1 : 47) - i;
    constexpr int ex = ((0 < i && i <= HW) ? 1 : 0) + ((SP < i && i <= SP + HW) ? 8 : 0) + ((SY < i && i <= SY + HW) ? 3 : 0);
    waitv<yH + ex>();
    static_for<0, 5>([&](auto C) { constexpr int ct = C.value;
      if constexpr (k == 0) mfma_zA(ac2[ct], W[k][ct], Hf[i % HW]); else mfma_ipA(ac2[ct], W[k][ct], Hf[i % HW]);
    });
    if constexpr (i + HW < 48) { constexpr int n = i + HW; ldV<SC1>(Hf[n % HW], vl, rsH, sH + ((n / 12) * 48 + n % 12) * 1024); }
    if constexpr (i == 0) { if (lane == 0) st32_sc1(tag, C_FY + (blockIdx.x * 4 + wave) * 64, rsC); }
    if constexpr (i == SP) {
      ld32_sc1(fy0, foY0, rsC); ld32_sc1(fy1, foY1, rsC);
      const rsrc_t rsG = mkrs(p.ws + WS_G, S * 4), rsQ = mkrs(p.q, S * D * 2);
      ld32_sc1(gtu, s * 4, rsG);
#pragma unroll
      for (int ct = 0; ct < 5; ++ct) ldV2<0>(qq[ct], (s * D + 80 * nt + 16 * ct + 4 * g) * 2, rsQ, 0);
    }
    if constexpr (i == SY) {
      poll_finish(fy0, foY0, rsC, tag); poll_finish(fy1, foY1, rsC, tag);
#pragma unroll
      for (int u = 0; u < 3; ++u) {
        const int idx = u * 256 + wave * 64 + lane, d = idx >> 3, cch = idx & 7;
        ldV<SC1>(yv[u], idx < 640 ? (unsigned)((80 * nt + d) * 4096 + rb * 128 + cch * 16) : 0x80000000u, rsY, 0);
      }
    }
    if constexpr (i % 4 == 3) { FT(16 + i / 4); }
    if constexpr (k == 11) {
      nop8();
#pragma unroll
      for (int ct = 0; ct < 5; ++ct) *(LAS f32x4*)(lds + G2_PART + ((rt * 4 + wave) * 5 + ct) * 1024 + lane * 16) = ac2[ct];
    }
    SCHED();
  });
  STAMP(9);
  unsigned fe[16];
  if (blockIdx.x == 0 && wave == 0) {
#pragma unroll
    for (int i = 0; i < 16; ++i) ld32_sc1(fe[i], C_FY + (i * 64 + lane) * 64, rsC);
  }
  waitv<0>();
#pragma unroll
  for (int u = 0; u < 3; ++u) {
    asm volatile("" : "+v"(yv[u]));
    const int idx = u * 256 + wave * 64 + lane, d = idx >> 3, cch = idx & 7;
    if (idx < 640) { *(LAS v2u*)(lds + G2_Y + d * 136 + cch * 16) = v2u{yv[u].x, yv[u].y}; *(LAS v2u*)(lds + G2_Y + d * 136 + cch * 16 + 8) = v2u{yv[u].z, yv[u].w}; }
  }
  asm volatile("" : "+v"(gtu));
#pragma unroll
  for (int ct = 0; ct < 5; ++ct) asm volatile("" : "+v"(qq[ct]));
  waitl<0>(); sbar();
  STAMP(10);
  const float gt = __builtin_bit_cast(float, gtu);
#pragma unroll
  for (int ct = 0; ct < 5; ++ct) {
    f32x4 b = *(LAS f32x4*)(lds + G2_PART + ((wave * 4 + 0) * 5 + ct) * 1024 + lane * 16);
#pragma unroll
    for (int w = 1; w < 4; ++w) b += *(LAS f32x4*)(lds + G2_PART + ((wave * 4 + w) * 5 + ct) * 1024 + lane * 16);
    const v2u yy = ldstr(lds + G2_Y + (16 * ct + 4 * g + q4) * 136 + (16 * wave + 4 * p4) * 2);
    const float o0 = gt * (blo(qq[ct].x) * blo(yy.x)) + (1.f - gt) * b[0];
    const float o1 = gt * (bhi(qq[ct].x) * bhi(yy.x)) + (1.f - gt) * b[1];
    const float o2 = gt * (blo(qq[ct].y) * blo(yy.y)) + (1.f - gt) * b[2];
    const float o3 = gt * (bhi(qq[ct].y) * bhi(yy.y)) + (1.f - gt) * b[3];
    *(GAS v2u*)(p.out + s * D + 80 * nt + 16 * ct + 4 * g) = v2u{pk2(o0, o1), pk2(o2, o3)};
  }
  if (blockIdx.x == 0 && wave == 0) {
#pragma unroll 1
    for (int i = 0; i < 1; ++i) {
#pragma unroll
      for (int e = 0; e < 16; ++e) poll_finish(fe[e], C_FY + (e * 64 + lane) * 64, rsC, tag);
    }
    if (lane == 0) st32_sc1(tag, C_EPOCH, rsC);
  }
  STAMP(11);
  STAMP_FLUSH(); FT_FLUSH();
}

__global__ __launch_bounds__(256, 1) void fused(Params p) {
  extern __shared__ __attribute__((aligned(16))) char smem[];
  LAS char* lds = (LAS char*)smem;
  const int lane = threadIdx.x & 63, wave = __builtin_amdgcn_readfirstlane(threadIdx.x >> 6);
  const int x = blockIdx.x & 7, r = blockIdx.x >> 3;
  unsigned pf = 0;
  {
    unsigned long long pc; unsigned len;
    asm volatile("s_getpc_b64 %0\nLpf_here:\n\ts_mov_b32 %1, Lpf_end-Lpf_here" : "=s"(pc), "=s"(len));
    const unsigned off = (r * 4 + wave) * 1024 + lane * 16;
    const rsrc_t rs = mkrs((const void*)pc, len);
    v4u t;
    asm volatile("buffer_load_dwordx4 %0, %1, %2, 0 offen" : "=v"(t) : "v"(off), "s"(rs));
    if (r < 24) { body<true>(p, lds, x, r, wave, lane); }
    else { body<false>(p, lds, x, r, wave, lane); }
    asm volatile("s_waitcnt vmcnt(0)\nLpf_end:" : "+v"(t));
    pf = t.x;
  }
  asm volatile("" ::"v"(pf));
}

extern "C" void kernel_launch(void* const* d_in, const int* in_sizes, int n_in, void* d_out, int out_size, void* d_ws, size_t ws_size, hipStream_t stream) {
  Params p;
  p.q = (const u16*)d_in[0]; p.k = (const u16*)d_in[1]; p.v = (const u16*)d_in[2]; p.sk = (const u16*)d_in[3];
  p.h = (const u16*)d_in[4]; p.Dv = (const u16*)d_in[5]; p.w1 = (const u16*)d_in[7]; p.w2 = (const u16*)d_in[8]; p.gw = (const u16*)d_in[9];
  p.out = (u16*)d_out; p.ws = (char*)d_ws;
  (void)hipFuncSetAttribute((const void*)fused, hipFuncAttributeMaxDynamicSharedMemorySize, LDS_BYTES);
  fused<<<256, 256, LDS_BYTES, stream>>>(p);
}
```

```cpp
#include <hip/hip_runtime.h>
#include <type_traits>
#include <cstdio>
#include <stdint.h>

typedef unsigned short u16;
typedef unsigned v4u __attribute__((ext_vector_type(4)));
typedef unsigned v2u __attribute__((ext_vector_type(2)));
typedef float f32x4 __attribute__((ext_vector_type(4)));
typedef float f32x2 __attribute__((ext_vector_type(2)));
typedef __bf16 bf16x8 __attribute__((ext_vector_type(8)));
typedef __bf16 bf16x2 __attribute__((ext_vector_type(2)));
typedef short v4s __attribute__((ext_vector_type(4)));
typedef short v8s __attribute__((ext_vector_type(8)));
#define GAS __attribute__((address_space(1)))
#define LAS __attribute__((address_space(3)))
#define DEV __device__ __forceinline__
typedef __amdgpu_buffer_rsrc_t rsrc_t;

constexpr int S = 2048, D = 640, F = 1536;

template <int N> DEV void waitv() { asm volatile("s_waitcnt vmcnt(%0)" ::"i"(N) : "memory"); }
template <int N> DEV void waitl() { asm volatile("s_waitcnt lgkmcnt(%0)" ::"i"(N) : "memory"); }
DEV void sbar() { asm volatile("s_barrier" ::: "memory"); }
#define SCHED() __builtin_amdgcn_sched_barrier(0)
DEV rsrc_t mkrs(const void* p, unsigned bytes) { return __builtin_amdgcn_make_buffer_rsrc((void*)p, 0, bytes, 0x00020000); }
DEV f32x4 mfma(v4u a, v4u b, f32x4 c) {
  return __builtin_amdgcn_mfma_f32_16x16x32_bf16(__builtin_bit_cast(bf16x8, a), __builtin_bit_cast(bf16x8, b), c, 0, 0, 0);
}
DEV v4u lds128(LAS const char* p) { return *(LAS const v4u*)p; }
DEV v2u lds64(LAS const char* p) { return *(LAS const v2u*)p; }
DEV v2u ldstr(LAS const char* p) { return __builtin_bit_cast(v2u, __builtin_amdgcn_ds_read_tr16_b64_v4i16((LAS v4s*)p)); }
DEV v4u cat(v2u a, v2u b) { return v4u{a.x, a.y, b.x, b.y}; }
DEV unsigned pk2(float a, float b) { bf16x2 r = __builtin_convertvector(f32x2{a, b}, bf16x2); return __builtin_bit_cast(unsigned, r); }
DEV float blo(unsigned d) { return __builtin_bit_cast(float, d << 16); }
DEV float bhi(unsigned d) { return __builtin_bit_cast(float, d & 0xffff0000u); }
DEV void setm0(unsigned v) { asm volatile("s_mov_b32 m0, %0\n\ts_nop 0" ::"s"(v)); }
template <int OFF, int NT = 0> DEV void dma16(unsigned voff, rsrc_t rs, unsigned soff) {
  if constexpr (NT) asm volatile("buffer_load_dwordx4 %0, %1, %2 offen offset:%3 nt lds" ::"v"(voff), "s"(rs), "s"(soff), "i"(OFF));
  else asm volatile("buffer_load_dwordx4 %0, %1, %2 offen offset:%3 lds" ::"v"(voff), "s"(rs), "s"(soff), "i"(OFF));
}
typedef float f32x16 __attribute__((ext_vector_type(16)));
DEV f32x16 mfma32(v4u a, v4u b, f32x16 c) {
  return __builtin_amdgcn_mfma_f32_32x32x16_bf16(__builtin_bit_cast(bf16x8, a), __builtin_bit_cast(bf16x8, b), c, 0, 0, 0);
}
template <int IMM> DEV void setm0_add(unsigned base) { asm volatile("s_add_u32 m0, %0, %1" ::"s"(base), "i"(IMM) : "scc"); }
template <int I, int N, class Fn> DEV void static_for(Fn&& f) { if constexpr (I < N) { f(std::integral_constant<int, I>{}); static_for<I + 1, N>(f); } }
DEV void mfma_ip(f32x4& c, const v4u& a, const v4u& b) { asm volatile("v_mfma_f32_16x16x32_bf16 %0, %1, %2, %0" : "+v"(c) : "v"(a), "v"(b)); }
DEV void mfma_ipA(f32x4& c, const v4u& a, const v4u& b) { asm volatile("v_mfma_f32_16x16x32_bf16 %0, %1, %2, %0" : "+v"(c) : "a"(a), "v"(b)); }
DEV void mfma_z(f32x4& c, const v4u& a, const v4u& b) { asm volatile("v_mfma_f32_16x16x32_bf16 %0, %1, %2, 0" : "=&v"(c) : "v"(a), "v"(b)); }
DEV void mfma_zA(f32x4& c, const v4u& a, const v4u& b) { asm volatile("v_mfma_f32_16x16x32_bf16 %0, %1, %2, 0" : "=&v"(c) : "a"(a), "v"(b)); }
template <int AUX> DEV void ldA(v4u& d, unsigned voff, rsrc_t rs, unsigned soff) {
  if constexpr (AUX) asm volatile("buffer_load_dwordx4 %0, %1, %2, %3 offen sc1" : "=a"(d) : "v"(voff), "s"(rs), "s"(soff));
  else asm volatile("buffer_load_dwordx4 %0, %1, %2, %3 offen" : "=a"(d) : "v"(voff), "s"(rs), "s"(soff));
}
template <int AUX> DEV void ldV(v4u& d, unsigned voff, rsrc_t rs, unsigned soff) {
  if constexpr (AUX == 2) asm volatile("buffer_load_dwordx4 %0, %1, %2, %3 offen nt" : "=v"(d) : "v"(voff), "s"(rs), "s"(soff));
  else if constexpr (AUX == 16) asm volatile("buffer_load_dwordx4 %0, %1, %2, %3 offen sc1" : "=v"(d) : "v"(voff), "s"(rs), "s"(soff));
  else asm volatile("buffer_load_dwordx4 %0, %1, %2, %3 offen" : "=v"(d) : "v"(voff), "s"(rs), "s"(soff));
}
DEV void nop8() { asm volatile("s_nop 7"); }

constexpr size_t WS_CTRL = 0;
constexpr size_t WS_KVT = 262144;
constexpr size_t WS_YT = WS_KVT + (size_t)D * S * 2;
constexpr size_t WS_W2T = WS_YT + (size_t)D * S * 2;
constexpr size_t WS_HF = WS_W2T + (size_t)8 * 48 * 5 * 1024;
constexpr size_t WS_G = WS_HF + (size_t)128 * 48 * 1024;
constexpr size_t WS_END = WS_G + S * 4;
constexpr unsigned C_EPOCH = 0, C_TMO = 256;
constexpr unsigned C_FW = 4096;
constexpr unsigned C_FKV = 16384;
constexpr unsigned C_FH = 32768;
constexpr unsigned C_FY = 98304;
constexpr unsigned C_BYTES = 262144;

#define NSTAMP 16
#define STAMP_OFF (size_t)(32u << 20)
#define STAMP_DECL
#define STAMP(i)
#define STAMP_FLUSH()
#define FT_DECL
#define FT(i)
#define FT_FLUSH()

struct Params {
  const u16 *q, *k, *v, *sk, *h, *Dv, *w1, *w2, *gw;
  u16* out;
  char* ws;
};

constexpr int LDS_BYTES = 160 * 1024;
constexpr int SC1 = 16;
#define NT_X 0
#define NT_W1 0
#define NT_P 0
constexpr int AUXP = NT_P ? 2 : 0;
#define IC(n) std::integral_constant<int, (n)>{}

DEV void ld32_sc1(unsigned& v, unsigned voff, rsrc_t rs) { asm volatile("buffer_load_dword %0, %1, %2, 0 offen sc1" : "=v"(v) : "v"(voff), "s"(rs)); }
DEV void st32_sc1(unsigned v, unsigned voff, rsrc_t rs) { asm volatile("buffer_store_dword %0, %1, %2, 0 offen sc1" ::"v"(v), "v"(voff), "s"(rs)); }
DEV void poll_finish(unsigned v, unsigned voff, rsrc_t rsC, unsigned tag) {
  asm volatile("" : "+v"(v));
  if (__builtin_amdgcn_ballot_w64(v != tag) == 0) return;
#pragma unroll 1
  for (int spins = 0; spins < (1 << 15); ++spins) {
    __builtin_amdgcn_s_sleep(1);
    ld32_sc1(v, voff, rsC);
    asm volatile("s_waitcnt vmcnt(0)" : "+v"(v)::"memory");
    if (__builtin_amdgcn_ballot_w64(v != tag) == 0) return;
  }
  st32_sc1(1u, C_TMO, rsC);
}

DEV void mfma32_ip(f32x16& c, const v4u& a, const v4u& b) { asm volatile("v_mfma_f32_32x32x16_bf16 %0, %1, %2, %0" : "+v"(c) : "v"(a), "v"(b)); }
DEV void mfma32_z(f32x16& c, const v4u& a, const v4u& b) { asm volatile("v_mfma_f32_32x32x16_bf16 %0, %1, %2, 0" : "=&v"(c) : "v"(a), "v"(b)); }

constexpr int KV_BASE = 32768, KV_GS = 1152, KV_V = 9 * KV_GS, KV_CG = 17 * KV_GS;
constexpr int CV_BASE = 32768;
constexpr int CV_HC = 4160;
constexpr int CV_U = 4 * CV_HC;
constexpr int CV_CH = CV_U + 4608;
constexpr int CV_EX = CV_BASE + 4 * CV_CH;
constexpr int G2_PART = 0;
constexpr int G2_Y = 81920;
constexpr int HW = 24;

template <int AUX> DEV void ldV2(v2u& d, unsigned voff, rsrc_t rs, unsigned soff) {
  if constexpr (AUX == 2) asm volatile("buffer_load_dwordx2 %0, %1, %2, %3 offen nt" : "=v"(d) : "v"(voff), "s"(rs), "s"(soff));
  else if constexpr (AUX == 16) asm volatile("buffer_load_dwordx2 %0, %1, %2, %3 offen sc1" : "=v"(d) : "v"(voff), "s"(rs), "s"(soff));
  else asm volatile("buffer_load_dwordx2 %0, %1, %2, %3 offen" : "=v"(d) : "v"(voff), "s"(rs), "s"(soff));
}
DEV void dma16s(unsigned voff, rsrc_t rs, unsigned soff, unsigned m0v) {
  if constexpr (NT_P) asm volatile("s_mov_b32 m0, %3\n\ts_nop 0\n\tbuffer_load_dwordx4 %0, %1, %2 offen nt lds" ::"v"(voff), "s"(rs), "s"(soff), "s"(m0v));
  else asm volatile("s_mov_b32 m0, %3\n\ts_nop 0\n\tbuffer_load_dwordx4 %0, %1, %2 offen lds" ::"v"(voff), "s"(rs), "s"(soff), "s"(m0v));
}
DEV unsigned mktag(unsigned& epoch) {
  asm volatile("" : "+v"(epoch));
  return __builtin_amdgcn_readfirstlane(epoch * 0x9E3779B1u + 0x7F4A7C15u);
}

template <int NR, class ST> DEV void kv_math(const v4u (&kk)[NR + 3], const v4u (&vv)[NR], const v4u (&ww)[4], ST&& st) {
#pragma unroll
  for (int cp = 0; cp < 4; ++cp) {
    f32x2 w[4];
    w[0] = f32x2{blo(ww[cp].x), blo(ww[cp].z)}; w[1] = f32x2{bhi(ww[cp].x), bhi(ww[cp].z)};
    w[2] = f32x2{blo(ww[cp].y), blo(ww[cp].w)}; w[3] = f32x2{bhi(ww[cp].y), bhi(ww[cp].w)};
    f32x2 kf[NR + 3];
#pragma unroll
    for (int i = 0; i < NR + 3; ++i) kf[i] = f32x2{blo(kk[i][cp]), bhi(kk[i][cp])};
    f32x2 r[NR];
#pragma unroll
    for (int i = 0; i < NR; ++i) {
      f32x2 a = kf[i + 3] * w[0];
      a = __builtin_elementwise_fma(kf[i + 2], w[1], a);
      a = __builtin_elementwise_fma(kf[i + 1], w[2], a);
      a = __builtin_elementwise_fma(kf[i], w[3], a);
      r[i] = a * f32x2{blo(vv[i][cp]), bhi(vv[i][cp])};
    }
    st(cp, r);
  }
}

template <bool G1R>
DEV void body(const Params& p, LAS char* lds, const int x, const int r, const int wave, const int lane) {
  const int g = lane >> 4, q4 = (lane >> 2) & 3, p4 = lane & 3, c16 = lane & 15;
  STAMP_DECL; FT_DECL;
  STAMP(0);
  const rsrc_t rsC = mkrs(p.ws + WS_CTRL, C_BYTES);
  unsigned epoch, tag;
  ld32_sc1(epoch, C_EPOCH, rsC);
  const int cl = G1R ? (wave >> 1) : wave, half = G1R ? (wave & 1) : 0;
  const int ch = (G1R ? 80 * x + 2 * r : 80 * x + 48 + 4 * (r - 24)) + cl;
  const unsigned cb = CV_BASE + cl * CV_CH;
  constexpr int NCHK = G1R ? 2 : 4;
  unsigned dv;
  { const rsrc_t rsD = mkrs(p.Dv, D * 2); asm volatile("buffer_load_ushort %0, %1, %2, 0 offen" : "=v"(dv) : "v"(ch * 2), "s"(rsD)); }
  const rsrc_t rsHh = mkrs(p.h, D * S * 2), rsU = mkrs(p.ws + WS_KVT, D * S * 2), rsY = mkrs(p.ws + WS_YT, D * S * 2);
  const rsrc_t rsW2 = mkrs(p.ws + WS_W2T, 8 * 48 * 5 * 1024), rsH = mkrs(p.ws + WS_HF, 128 * 48 * 1024);
  const int rb = 4 * x + (r & 3), nt = r >> 2;
  const unsigned vl = lane * 16;
  const unsigned sW = (nt * 48 + 12 * wave) * 5 * 1024;
  v4u W[12][5];
  auto ldW = [&](auto I) { constexpr int i = I.value; if constexpr (i < 60) ldA<SC1>(W[i / 5][i % 5], vl, rsW2, sW + i * 1024); };
  v4u hc[NCHK], uc[NCHK]; v2u hp[NCHK];
  auto ld_h = [&]() {
#pragma unroll
    for (int i = 0; i < NCHK; ++i) {
      const int n = (half * NCHK + i) * 64 + lane;
      ldV<AUXP>(hc[i], ch * 4096 + n * 16, rsHh, 0);
      ldV2<AUXP>(hp[i], n > 0 ? (unsigned)(ch * 4096 + n * 16 - 8) : 0x80000000u, rsHh, 0);
    }
  };
  auto ld_u = [&]() {
#pragma unroll
    for (int i = 0; i < NCHK; ++i) ldV<SC1>(uc[i], ch * 4096 + ((half * NCHK + i) * 64 + lane) * 16, rsU, 0);
  };
  auto fill_h = [&]() {
#pragma unroll
    for (int i = 0; i < NCHK; ++i) {
      asm volatile("" : "+v"(hc[i]), "+v"(hp[i]));
      const int n = (half * NCHK + i) * 64 + lane;
      const v4u cur = hc[i]; const v2u prv = hp[i];
      const unsigned om2 = __builtin_amdgcn_alignbit(prv.y, prv.x, 16), om1 = __builtin_amdgcn_alignbit(cur.x, prv.y, 16);
      const unsigned o0 = __builtin_amdgcn_alignbit(cur.y, cur.x, 16), o1 = __builtin_amdgcn_alignbit(cur.z, cur.y, 16), o2 = __builtin_amdgcn_alignbit(cur.w, cur.z, 16);
      LAS char* dst = lds + cb + (n + 2) * 16;
      *(LAS v4u*)(dst) = cur;
      *(LAS v4u*)(dst + CV_HC) = v4u{om1, o0, o1, o2};
      *(LAS v4u*)(dst + 2 * CV_HC) = v4u{prv.y, cur.x, cur.y, cur.z};
      *(LAS v4u*)(dst + 3 * CV_HC) = v4u{om2, om1, o0, o1};
    }
    if (half == 0) {
      if (lane < 8) *(LAS v4u*)(lds + cb + (lane >> 1) * CV_HC + (lane & 1) * 16) = v4u{0, 0, 0, 0};
      if (lane >= 32 && lane < 62) *(LAS v4u*)(lds + cb + CV_U + (lane - 32) * 16) = v4u{0, 0, 0, 0};
    }
  };
  auto fill_u = [&]() {
#pragma unroll
    for (int i = 0; i < NCHK; ++i) {
      asm volatile("" : "+v"(uc[i]));
      *(LAS v4u*)(lds + cb + CV_U + 480 + ((half * NCHK + i) * 64 + lane) * 16) = uc[i];
    }
  };

  if constexpr (G1R) {
    const int sb = 4 * (x >> 1) + (r & 3), fs = 6 * (x & 1) + (r >> 2);
    f32x16 acc1[2][2];
    const rsrc_t rsX = mkrs(p.q, S * D * 2), rsW = mkrs(p.w1, D * F * 2);
    unsigned vX[4], vW[4];
#pragma unroll
    for (int ii = 0; ii < 4; ++ii) {
      vX[ii] = (32 * wave + 8 * ii + (lane >> 3)) * 1280 + (((lane & 7) ^ (4 * (ii & 1) + g)) << 4) - 1024 * ii;
      vW[ii] = (16 * wave + 4 * ii + g) * 3072 + (((((c16 >> 1) ^ (g << 1)) << 1) | (c16 & 1)) << 4) - 1024 * ii;
    }
    const unsigned sX0 = sb * 128 * 1280, sW0 = fs * 256;
    const unsigned mX = wave * 4096, mW = 16384 + wave * 4096;
    auto dmaX = [&](auto T, auto I) { constexpr int t = T.value, i = I.value; if constexpr (t < 10) dma16<1024 * i, NT_X>(vX[i], rsX, sX0 + t * 128); };
    auto dmaW = [&](auto T, auto I) { constexpr int t = T.value, i = I.value; if constexpr (t < 10) dma16<1024 * i, NT_W1>(vW[i], rsW, sW0 + t * 64 * 3072); };
    auto m0X = [&](auto T) { constexpr int t = T.value; if constexpr (t < 10) setm0_add<(t % 5) * 32768>(mX); };
    auto m0W = [&](auto T) { constexpr int t = T.value; if constexpr (t < 10) setm0_add<(t % 5) * 32768>(mW); };
    static_for<0, 4>([&](auto T) {
      m0X(T); asm volatile("s_nop 0");
      static_for<0, 4>([&](auto I) { dmaX(T, I); });
      if constexpr (T.value < 3) { m0W(T); asm volatile("s_nop 0"); static_for<0, 4>([&](auto I) { dmaW(T, I); }); }
    });
    const int ws_ = wave & 1, wf = wave >> 1, h = lane >> 5, r32 = lane & 31;
    unsigned aX[3][4], aW[3][2];
#pragma unroll
    for (int w = 0; w < 3; ++w) {
#pragma unroll
      for (int ks = 0; ks < 4; ++ks) aX[w][ks] = w * 65536 + (64 * ws_ + r32) * 128 + (((2 * ks + h) ^ ((r32 >> 1) & 7)) << 4);
#pragma unroll
      for (int ft = 0; ft < 2; ++ft) aW[w][ft] = w * 65536 + 16384 + (8 * h + q4) * 256 + (((2 * (2 * wf + ft) + (g & 1)) ^ (q4 << 1)) << 5) + 8 * p4;
    }
    v4u A[2][2][2], B[2][2][2];
    v2u At;
    auto rd = [&](auto T, auto HF, auto SET, auto N) {
      constexpr int t = T.value, hf = HF.value, set = SET.value, n = N.value;
      if constexpr (t < 10) {
        constexpr int b = t % 5, w = b >> 1, bo = (b & 1) * 32768;
        constexpr int kk = n / 6, rr = n % 6, ks = 2 * hf + kk;
        if constexpr (rr == 0) B[set][kk][0] = lds128(lds + aX[w][ks] + bo);
        else if constexpr (rr == 3) B[set][kk][1] = lds128(lds + aX[w][ks] + bo + 32 * 128);
        else { constexpr int ft = rr / 3, jj = (rr % 3) - 1;
          v2u xx = ldstr(lds + aW[w][ft] + bo + ks * 4096 + jj * 1024);
          if constexpr (jj == 0) At = xx; else A[set][kk][ft] = cat(At, xx); }
      }
    };
    unsigned fw, fk;
    waitv<20>(); sbar();
    STAMP(1);
    static_for<0, 12>([&](auto N) { rd(IC(0), IC(0), IC(0), N); });
    SCHED();
    static_for<0, 10>([&](auto T) {
      constexpr int t = T.value;
      static_for<0, 2>([&](auto HF) {
        constexpr int hf = HF.value;
        static_for<0, 8>([&](auto M) {
          constexpr int m = M.value, kk = m >> 2, ft = (m >> 1) & 1, st = m & 1;
          if constexpr (t == 0 && hf == 0 && kk == 0) mfma32_z(acc1[ft][st], A[hf][kk][ft], B[hf][kk][st]);
          else mfma32_ip(acc1[ft][st], A[hf][kk][ft], B[hf][kk][st]);
          if constexpr (m < 6) {
            if constexpr (hf == 0) { rd(T, IC(1), IC(1), IC(2 * m)); rd(T, IC(1), IC(1), IC(2 * m + 1)); }
            else { rd(IC(t + 1), IC(0), IC(0), IC(2 * m)); rd(IC(t + 1), IC(0), IC(0), IC(2 * m + 1)); }
          }
          if constexpr (m == 0) { if constexpr (hf == 0) m0W(IC(t + 3)); else m0X(IC(t + 4)); }
          if constexpr (m & 1) { if constexpr (hf == 0) dmaW(IC(t + 3), IC(m >> 1)); else dmaX(IC(t + 4), IC(m >> 1)); }
          if constexpr (t == 6 && hf == 1 && m == 1) ld32_sc1(fw, C_FW + lane * 128, rsC);
          if constexpr (t == 6 && hf == 1 && m == 3) ld32_sc1(fk, C_FKV + lane * 128, rsC);
          if constexpr (t == 6 && hf == 1 && m == 5) ld_h();
          SCHED();
        });
        if constexpr (hf == 0 && t < 9) {
          constexpr int younger = (t + 2 < 10 ? 8 : 0) + (t + 3 < 10 ? 8 : 0) + (t >= 7 ? 6 : 0);
          waitv<younger>(); sbar(); SCHED();
        }
      });
      FT(t); SCHED();
    });
    asm volatile("s_nop 15");
    waitv<0>();
    sbar();
    STAMP(2);
    tag = mktag(epoch);
    poll_finish(fw, C_FW + lane * 128, rsC, tag);
    poll_finish(fk, C_FKV + lane * 128, rsC, tag);
    STAMP(3);
    ld_u();
    const unsigned vo = (g & 1) * (48 * 1024) + (c16 + 16 * h) * 16;
    static_for<0, 4>([&](auto Q) {
      constexpr int qd = Q.value, st = qd >> 1, ft = qd & 1;
      const f32x16 a = acc1[ft][st];
      unsigned o[8];
      static_for<0, 8>([&](auto E) {
        constexpr int e = 2 * E.value;
        const float r0 = a[e] * __builtin_amdgcn_rcpf(1.f + __builtin_amdgcn_exp2f(a[e] * -1.44269504088896f));
        const float r1 = a[e + 1] * __builtin_amdgcn_rcpf(1.f + __builtin_amdgcn_exp2f(a[e + 1] * -1.44269504088896f));
        o[e >> 1] = pk2(r0, r1);
        if constexpr ((E.value % 4) != 3) ldW(IC(qd * 6 + E.value - E.value / 4));
        SCHED();
      });
      const unsigned so = ((8 * sb + 4 * ws_ + 2 * st) * 48 + 4 * fs + 2 * wf + ft) * 1024;
      __builtin_amdgcn_raw_buffer_store_b128(v4u{o[0], o[1], o[4], o[5]}, rsH, vo, so, SC1);
      __builtin_amdgcn_raw_buffer_store_b128(v4u{o[2], o[3], o[6], o[7]}, rsH, vo + 512, so, SC1);
      SCHED();
    });
    STAMP(4);
    waitv<32>();
    fill_h(); fill_u();
    waitv<0>();
    if (lane == 0) st32_sc1(tag, C_FH + ((sb * 12 + fs) * 4 + wave) * 64, rsC);
    waitl<0>(); sbar();
  } else {
    const int j = (r - 24) * 8 + x, rbk = j >> 1, cg0 = 5 * (j & 1);
    const rsrc_t rsK = mkrs(p.k, S * D * 2), rsV = mkrs(p.v, S * D * 2), rsS = mkrs(p.sk, D * 8), rsO = mkrs(p.ws + WS_KVT, D * S * 2);
    const unsigned vr = (lane >> 3) * 1280 + ((lane & 7) << 4), vr4 = vr ^ 64;
    auto dmaK = [&](int cgl, int G) {
      const int row = 64 * rbk - 8 + 8 * G;
      dma16s(((G >> 1) & 1) ? vr4 : vr, rsK, row >= 0 ? (unsigned)(row * 1280 + (cg0 + cgl) * 128) : 0x80000000u, KV_BASE + cgl * KV_CG + G * KV_GS);
    };
    auto dmaV = [&](int cgl, int G) {
      dma16s(((G >> 1) & 1) ? vr4 : vr, rsV, (64 * rbk + 8 * G) * 1280 + (cg0 + cgl) * 128, KV_BASE + cgl * KV_CG + KV_V + G * KV_GS);
    };
    const int id = 4 * j + wave, pk = id / 5, blk = id % 5;
    const rsrc_t rsWs = mkrs(p.w2, F * D * 2);
    const unsigned ldsb = wave * 8192;
#pragma unroll
    for (int i = 0; i < 8; ++i) {
      const int row = 4 * i + g;
      dma16s((32 * pk + row) * 1280 + blk * 256 + (((((c16 >> 1) ^ (row & 7)) << 1) | (c16 & 1)) << 4), rsWs, id < 240 ? 0u : 0x80000000u, ldsb + i * 1024);
    }
#pragma unroll
    for (int G = 0; G < 9; ++G) dmaK(wave, G);
#pragma unroll
    for (int G = 0; G < 8; ++G) dmaV(wave, G);
    dmaK(4, wave); dmaK(4, wave + 4); dmaV(4, wave); dmaV(4, wave + 4);
    if (wave == 0) dmaK(4, 8);
    const int cc = lane >> 3, rg = lane & 7;
    v4u ww[4], w4[4];
#pragma unroll
    for (int i = 0; i < 4; ++i) ldV<0>(ww[i], (64 * (cg0 + wave) + 8 * cc) * 8 + i * 16, rsS, 0);
#pragma unroll
    for (int i = 0; i < 4; ++i) ldV<0>(w4[i], (64 * (cg0 + 4) + 8 * (2 * wave + (lane >> 5))) * 8 + i * 16, rsS, 0);
    const rsrc_t rsX = mkrs(p.q, S * D * 2), rsG = mkrs(p.gw, D * 2), rsGo = mkrs(p.ws + WS_G, S * 4);
    const int grow = 8 * id + (lane >> 3), part = lane & 7;
    v4u xx[10], gg[10];
#pragma unroll
    for (int i = 0; i < 10; ++i) { ldV<AUXP>(xx[i], grow * 1280 + i * 128 + part * 16, rsX, 0); ldV<0>(gg[i], i * 128 + part * 16, rsG, 0); }
    waitv<49>();
    STAMP(1);
    {
      const rsrc_t rsWo = mkrs(p.ws + WS_W2T, 8 * 48 * 5 * 1024);
      const int r0 = 4 * g + q4;
#pragma unroll
      for (int i = 0; i < 8; ++i) {
        const unsigned a = ldsb + r0 * 256 + ((i ^ (r0 & 7)) << 5) + 8 * p4;
        const v4u fr = cat(ldstr(lds + a), ldstr(lds + a + 4096));
        const int ctg = 8 * blk + i, ntt = ctg / 5, ct = ctg % 5;
        __builtin_amdgcn_raw_buffer_store_b128(fr, rsWo, vl, id < 240 ? (unsigned)(((ntt * 48 + pk) * 5 + ct) * 1024) : 0x80000000u, SC1);
      }
    }
    waitv<32>();
    {
      const unsigned kb = KV_BASE + wave * KV_CG;
      const unsigned a0 = kb + rg * KV_GS + ((cc ^ (4 * ((rg >> 1) & 1))) << 4), a1 = kb + (rg + 1) * KV_GS + ((cc ^ (4 * (((rg + 1) >> 1) & 1))) << 4);
      v4u kk[11], vv[8];
#pragma unroll
      for (int i = 0; i < 11; ++i) kk[i] = i < 3 ? lds128(lds + a0 + (5 + i) * 128) : lds128(lds + a1 + (i - 3) * 128);
#pragma unroll
      for (int i = 0; i < 8; ++i) vv[i] = lds128(lds + a0 + KV_V + i * 128);
#pragma unroll
      for (int i = 0; i < 4; ++i) asm volatile("" : "+v"(ww[i]));
      const unsigned ob = (64 * (cg0 + wave) + 8 * cc) * 4096 + (64 * rbk + 8 * rg) * 2;
      kv_math<8>(kk, vv, ww, [&](int cp, const f32x2 (&rr)[8]) {
#pragma unroll
        for (int c = 0; c < 2; ++c) {
          v4u o{pk2(rr[0][c], rr[1][c]), pk2(rr[2][c], rr[3][c]), pk2(rr[4][c], rr[5][c]), pk2(rr[6][c], rr[7][c])};
          __builtin_amdgcn_raw_buffer_store_b128(o, rsO, ob + (2 * cp + c) * 4096, 0, SC1);
        }
      });
    }
    waitv<8>(); sbar();
    tag = mktag(epoch);
    if (threadIdx.x == 0) st32_sc1(tag, C_FW + j * 128, rsC);
    STAMP(2);
    {
      const int cc4 = 2 * wave + (lane >> 5), rp = lane & 31;
      const unsigned kb = KV_BASE + 4 * KV_CG;
      v4u kk[5], vv[2];
#pragma unroll
      for (int i = 0; i < 5; ++i) { const int row = 2 * rp + 5 + i; kk[i] = lds128(lds + kb + (row >> 3) * KV_GS + (row & 7) * 128 + ((cc4 ^ (4 * ((row >> 4) & 1))) << 4)); }
#pragma unroll
      for (int i = 0; i < 2; ++i) { const int row = 2 * rp + i; vv[i] = lds128(lds + kb + KV_V + (row >> 3) * KV_GS + (row & 7) * 128 + ((cc4 ^ (4 * ((row >> 4) & 1))) << 4)); }
#pragma unroll
      for (int i = 0; i < 4; ++i) asm volatile("" : "+v"(w4[i]));
      const unsigned ob = (64 * (cg0 + 4) + 8 * cc4) * 4096 + (64 * rbk + 2 * rp) * 2;
      kv_math<2>(kk, vv, w4, [&](int cp, const f32x2 (&rr)[2]) {
#pragma unroll
        for (int c = 0; c < 2; ++c) __builtin_amdgcn_raw_buffer_store_b32(pk2(rr[0][c], rr[1][c]), rsO, ob + (2 * cp + c) * 4096, 0, SC1);
      });
    }
    {
      float a = 0.f;
#pragma unroll
      for (int i = 0; i < 10; ++i) {
        asm volatile("" : "+v"(xx[i]), "+v"(gg[i]));
#pragma unroll
        for (int jj = 0; jj < 4; ++jj) { a = fmaf(blo(xx[i][jj]), blo(gg[i][jj]), a); a = fmaf(bhi(xx[i][jj]), bhi(gg[i][jj]), a); }
      }
      a += __shfl_xor(a, 1); a += __shfl_xor(a, 2); a += __shfl_xor(a, 4);
      const float gt = __builtin_amdgcn_rcpf(1.f + __builtin_amdgcn_exp2f(a * -1.44269504088896f));
      if (part == 0) __builtin_amdgcn_raw_buffer_store_b32(__builtin_bit_cast(unsigned, gt), rsGo, grow * 4, 0, SC1);
    }
    ld_h();
    waitl<0>(); sbar();
    waitv<0>();
    sbar();
    if (threadIdx.x == 0) st32_sc1(tag, C_FKV + j * 128, rsC);
    STAMP(3);
    fill_h();
    unsigned fw, fk;
    ld32_sc1(fw, C_FW + lane * 128, rsC);
    ld32_sc1(fk, C_FKV + lane * 128, rsC);
    waitv<0>();
    poll_finish(fw, C_FW + lane * 128, rsC, tag);
    poll_finish(fk, C_FKV + lane * 128, rsC, tag);
    ld_u();
    STAMP(4);
    static_for<0, 12>([&](auto I) { ldW(I); });
    waitv<12>();
    fill_u();
    waitl<0>();
  }
  STAMP(5);
  constexpr int NZ = G1R ? 4 : 8, W0 = G1R ? 24 : 12;
  f32x4 acc[8];
  {
    const unsigned aA = cb + q4 * CV_HC + 56 - 16 * g + 8 * p4 + 256 * half;
    const unsigned aB = cb + CV_U + 448 + 32 * c16 + 16 * g - 256 * half - 64 * (NZ - 1);
    v4u A[8], B[8];
#pragma unroll
    for (int i = 0; i < 8; ++i) {
      B[7 - i] = lds128(lds + aB + 64 * (NZ - 1) + 512 * (7 - i));
      A[i] = cat(ldstr(lds + aA + 512 * i + 8), ldstr(lds + aA + 512 * i));
    }
    SCHED();
    static_for<0, NZ>([&](auto Z) {
      constexpr int z = Z.value;
      static_for<0, 8>([&](auto I) {
        constexpr int i = I.value;
        static_for<0, 8 - i>([&](auto DD) {
          constexpr int d = 7 - i - DD.value;
          constexpr int gap = z * 36 + (i * (17 - i)) / 2 + DD.value;
          if constexpr (z == 0 && i == 0) mfma_z(acc[i + d], A[i], B[d]); else mfma_ip(acc[i + d], A[i], B[d]);
          if constexpr (d == 7 - i && z + 1 < NZ) B[7 - i] = lds128(lds + aB + 64 * (NZ - 2 - z) + 512 * (7 - i));
          constexpr int WSTEP = G1R ? 4 : 6;
          if constexpr (gap % WSTEP == 2) ldW(IC(W0 + gap / WSTEP));
          SCHED();
        });
        if constexpr (z + 1 < NZ) { A[i] = cat(ldstr(lds + aA + 64 * (z + 1) + 512 * i + 8), ldstr(lds + aA + 64 * (z + 1) + 512 * i)); SCHED(); }
      });
    });
  }
  static_assert(W0 + (NZ * 36 - 3) / (G1R ? 4 : 6) >= 59, "all W2 fragments issued");
  nop8(); SCHED();
  STAMP(6);
  const unsigned foH = C_FH + (((rb >> 1) * 12 + (lane < 24 ? lane >> 1 : 0)) * 4 + (rb & 1) + 2 * (lane & 1)) * 64;
  unsigned fh;
  ld32_sc1(fh, foH, rsC);
  int ctb = 0;
  if constexpr (G1R) {
    LAS char* ex = lds + CV_EX + cl * 8192;
f32x4 keep[4];
#pragma unroll
    for (int i = 0; i < 4; ++i) {
      f32x4 lo = acc[i], hi = acc[4 + i];
      asm volatile("" : "+v"(lo), "+v"(hi));
      *(LAS f32x4*)(ex + half * 4096 + i * 1024 + lane * 16) = half == 0 ? hi : lo;
      keep[i] = half == 0 ? lo : hi;
    }
    waitl<0>(); sbar();
#pragma unroll
    for (int i = 0; i < 4; ++i) acc[i] = keep[i] + *(LAS f32x4*)(ex + (1 - half) * 4096 + i * 1024 + lane * 16);
    ctb = 4 * half;
  }
  constexpr int NT = G1R ? 4 : 8;
  {
    asm volatile("" : "+v"(dv));
    const float Dc = blo(dv);
#pragma unroll
    for (int i = 0; i < NT; i += 2) {
      unsigned w0[2][2];
#pragma unroll
      for (int e = 0; e < 2; ++e) {
        const int ct = ctb + i + e;
        const f32x4 a = acc[i + e];
        const v2u uu = lds64(lds + cb + CV_U + 480 + 512 * ct + 32 * c16 + 8 * g);
        w0[e][0] = pk2(fmaf(blo(uu.x), Dc, a[0]), fmaf(bhi(uu.x), Dc, a[1]));
        w0[e][1] = pk2(fmaf(blo(uu.y), Dc, a[2]), fmaf(bhi(uu.y), Dc, a[3]));
      }
      auto s0 = __builtin_amdgcn_permlane16_swap(w0[0][0], w0[1][0], false, false);
      auto s1 = __builtin_amdgcn_permlane16_swap(w0[0][1], w0[1][1], false, false);
      const int ct = ctb + i + (g & 1);
      __builtin_amdgcn_raw_buffer_store_b128(v4u{s0[0], s1[0], s0[1], s1[1]}, rsY, ch * 4096 + 512 * ct + 32 * c16 + 8 * (g & 2), 0, SC1);
    }
  }
  waitl<0>(); sbar();
  STAMP(7);
  waitv<NT / 2>();
  poll_finish(fh, foH, rsC, tag);
  STAMP(8);
  FT(14);
  v4u Hf[HW];
  const unsigned sH = (4 * rb * 48 + 12 * wave) * 1024;
  static_for<0, HW>([&](auto I) { constexpr int i = I.value; ldV<SC1>(Hf[i], vl, rsH, sH + ((i / 12) * 48 + i % 12) * 1024); });
  FT(15);
  f32x4 ac2[5];
  const unsigned foY0 = C_FY + (((lane >> 2) * 8 + nt) * 4 + (lane & 3)) * 64, foY1 = foY0 + 16 * 8 * 4 * 64;
  unsigned fy0, fy1, gtu;
  v4u yv[3]; v2u qq[5];
  const int s = 64 * rb + 16 * wave + c16;
  constexpr int SP = 20;
  constexpr int SY = SP + HW + 1 < 47 ? SP + HW + 1 : 47;
  static_for<0, 48>([&](auto I) {
    constexpr int i = I.value, rt = i / 12, k = i % 12;
    constexpr int yH = (i + HW - 1 < 47 ? i + HW - 1 : 47) - i;
    constexpr int ex = ((0 < i && i <= HW) ? 1 : 0) + ((SP < i && i <= SP + HW) ? 8 : 0) + ((SY < i && i <= SY + HW) ? 3 : 0);
    waitv<yH + ex>();
    static_for<0, 5>([&](auto C) { constexpr int ct = C.value;
      if constexpr (k == 0) mfma_zA(ac2[ct], W[k][ct], Hf[i % HW]); else mfma_ipA(ac2[ct], W[k][ct], Hf[i % HW]);
    });
    if constexpr (i + HW < 48) { constexpr int n = i + HW; ldV<SC1>(Hf[n % HW], vl, rsH, sH + ((n / 12) * 48 + n % 12) * 1024); }
    if constexpr (i == 0) { if (lane == 0) st32_sc1(tag, C_FY + (blockIdx.x * 4 + wave) * 64, rsC); }
    if constexpr (i == SP) {
      ld32_sc1(fy0, foY0, rsC); ld32_sc1(fy1, foY1, rsC);
      const rsrc_t rsG = mkrs(p.ws + WS_G, S * 4), rsQ = mkrs(p.q, S * D * 2);
      ld32_sc1(gtu, s * 4, rsG);
#pragma unroll
      for (int ct = 0; ct < 5; ++ct) ldV2<AUXP>(qq[ct], (s * D + 80 * nt + 16 * ct + 4 * g) * 2, rsQ, 0);
    }
    if constexpr (i == SY) {
      poll_finish(fy0, foY0, rsC, tag); poll_finish(fy1, foY1, rsC, tag);
#pragma unroll
      for (int u = 0; u < 3; ++u) {
        const int idx = u * 256 + wave * 64 + lane, d = idx >> 3, cch = idx & 7;
        ldV<SC1>(yv[u], idx < 640 ? (unsigned)((80 * nt + d) * 4096 + rb * 128 + cch * 16) : 0x80000000u, rsY, 0);
      }
    }
    if constexpr (i % 4 == 3) { FT(16 + i / 4); }
    if constexpr (k == 11) {
      nop8();
#pragma unroll
      for (int ct = 0; ct < 5; ++ct) *(LAS f32x4*)(lds + G2_PART + ((rt * 4 + wave) * 5 + ct) * 1024 + lane * 16) = ac2[ct];
    }
    SCHED();
  });
  STAMP(9);
  unsigned fe[16];
  if (blockIdx.x == 0 && wave == 0) {
#pragma unroll
    for (int i = 0; i < 16; ++i) ld32_sc1(fe[i], C_FY + (i * 64 + lane) * 64, rsC);
  }
  waitv<0>();
#pragma unroll
  for (int u = 0; u < 3; ++u) {
    asm volatile("" : "+v"(yv[u]));
    const int idx = u * 256 + wave * 64 + lane, d = idx >> 3, cch = idx & 7;
    if (idx < 640) { *(LAS v2u*)(lds + G2_Y + d * 136 + cch * 16) = v2u{yv[u].x, yv[u].y}; *(LAS v2u*)(lds + G2_Y + d * 136 + cch * 16 + 8) = v2u{yv[u].z, yv[u].w}; }
  }
  asm volatile("" : "+v"(gtu));
#pragma unroll
  for (int ct = 0; ct < 5; ++ct) asm volatile("" : "+v"(qq[ct]));
  waitl<0>(); sbar();
  STAMP(10);
  const float gt = __builtin_bit_cast(float, gtu);
#pragma unroll
  for (int ct = 0; ct < 5; ++ct) {
    f32x4 b = *(LAS f32x4*)(lds + G2_PART + ((wave * 4 + 0) * 5 + ct) * 1024 + lane * 16);
#pragma unroll
    for (int w = 1; w < 4; ++w) b += *(LAS f32x4*)(lds + G2_PART + ((wave * 4 + w) * 5 + ct) * 1024 + lane * 16);
    const v2u yy = ldstr(lds + G2_Y + (16 * ct + 4 * g + q4) * 136 + (16 * wave + 4 * p4) * 2);
    const float o0 = gt * (blo(qq[ct].x) * blo(yy.x)) + (1.f - gt) * b[0];
    const float o1 = gt * (bhi(qq[ct].x) * bhi(yy.x)) + (1.f - gt) * b[1];
    const float o2 = gt * (blo(qq[ct].y) * blo(yy.y)) + (1.f - gt) * b[2];
    const float o3 = gt * (bhi(qq[ct].y) * bhi(yy.y)) + (1.f - gt) * b[3];
    *(GAS v2u*)(p.out + s * D + 80 * nt + 16 * ct + 4 * g) = v2u{pk2(o0, o1), pk2(o2, o3)};
  }
  if (blockIdx.x == 0 && wave == 0) {
#pragma unroll 1
    for (int i = 0; i < 1; ++i) {
#pragma unroll
      for (int e = 0; e < 16; ++e) poll_finish(fe[e], C_FY + (e * 64 + lane) * 64, rsC, tag);
    }
    if (lane == 0) st32_sc1(tag, C_EPOCH, rsC);
  }
  STAMP(11);
  STAMP_FLUSH(); FT_FLUSH();
}

__global__ __launch_bounds__(256, 1) void fused(Params p) {
  extern __shared__ __attribute__((aligned(16))) char smem[];
  LAS char* lds = (LAS char*)smem;
  const int lane = threadIdx.x & 63, wave = __builtin_amdgcn_readfirstlane(threadIdx.x >> 6);
  const int x = blockIdx.x & 7, r = blockIdx.x >> 3;
  const unsigned long long dbg_r0 = __builtin_amdgcn_s_memrealtime(), dbg_c0 = __builtin_amdgcn_s_memtime();
  unsigned pf = 0;
  {
    unsigned long long pc; unsigned len;
    asm volatile("s_getpc_b64 %0\nLpf_here:\n\ts_mov_b32 %1, Lpf_end-Lpf_here" : "=s"(pc), "=s"(len));
    const unsigned off = (r * 4 + wave) * 1024 + lane * 16;
    const rsrc_t rs = mkrs((const void*)pc, len);
    v4u t;
    asm volatile("buffer_load_dwordx4 %0, %1, %2, 0 offen" : "=v"(t) : "v"(off), "s"(rs));
    if (r < 24) { body<true>(p, lds, x, r, wave, lane); }
    else { body<false>(p, lds, x, r, wave, lane); }
    asm volatile("s_waitcnt vmcnt(0)\nLpf_end:" : "+v"(t));
    pf = t.x;
  }
  asm volatile("" ::"v"(pf));
  { const unsigned long long r1 = __builtin_amdgcn_s_memrealtime(), c1 = __builtin_amdgcn_s_memtime();
    if (threadIdx.x == 0 && (blockIdx.x == 5 || blockIdx.x == 200)) printf("CLK wg %d: %llu cycles in %llu x10ns -> %.3f GHz\n", (int)blockIdx.x, c1 - dbg_c0, r1 - dbg_r0, (double)(c1 - dbg_c0) / (double)(r1 - dbg_r0) / 10.0); }
}

extern "C" void kernel_launch(void* const* d_in, const int* in_sizes, int n_in, void* d_out, int out_size, void* d_ws, size_t ws_size, hipStream_t stream) {
  Params p;
  p.q = (const u16*)d_in[0]; p.k = (const u16*)d_in[1]; p.v = (const u16*)d_in[2]; p.sk = (const u16*)d_in[3];
  p.h = (const u16*)d_in[4]; p.Dv = (const u16*)d_in[5]; p.w1 = (const u16*)d_in[7]; p.w2 = (const u16*)d_in[8]; p.gw = (const u16*)d_in[9];
  p.out = (u16*)d_out; p.ws = (char*)d_ws;
  (void)hipFuncSetAttribute((const void*)fused, hipFuncAttributeMaxDynamicSharedMemorySize, LDS_BYTES);
  fused<<<256, 256, LDS_BYTES, stream>>>(p);
}
```

```cpp
#include <hip/hip_runtime.h>
#include <type_traits>
#include <stdint.h>

typedef unsigned short u16;
typedef unsigned v4u __attribute__((ext_vector_type(4)));
typedef unsigned v2u __attribute__((ext_vector_type(2)));
typedef float f32x4 __attribute__((ext_vector_type(4)));
typedef float f32x2 __attribute__((ext_vector_type(2)));
typedef __bf16 bf16x8 __attribute__((ext_vector_type(8)));
typedef __bf16 bf16x2 __attribute__((ext_vector_type(2)));
typedef short v4s __attribute__((ext_vector_type(4)));
typedef short v8s __attribute__((ext_vector_type(8)));
#define GAS __attribute__((address_space(1)))
#define LAS __attribute__((address_space(3)))
#define DEV __device__ __forceinline__
typedef __amdgpu_buffer_rsrc_t rsrc_t;

constexpr int S = 2048, D = 640, F = 1536;

template <int N> DEV void waitv() { asm volatile("s_waitcnt vmcnt(%0)" ::"i"(N) : "memory"); }
template <int N> DEV void waitl() { asm volatile("s_waitcnt lgkmcnt(%0)" ::"i"(N) : "memory"); }
DEV void sbar() { asm volatile("s_barrier" ::: "memory"); }
#define SCHED() __builtin_amdgcn_sched_barrier(0)
DEV rsrc_t mkrs(const void* p, unsigned bytes) { return __builtin_amdgcn_make_buffer_rsrc((void*)p, 0, bytes, 0x00020000); }
DEV f32x4 mfma(v4u a, v4u b, f32x4 c) {
  return __builtin_amdgcn_mfma_f32_16x16x32_bf16(__builtin_bit_cast(bf16x8, a), __builtin_bit_cast(bf16x8, b), c, 0, 0, 0);
}
DEV v4u lds128(LAS const char* p) { return *(LAS const v4u*)p; }
DEV v2u lds64(LAS const char* p) { return *(LAS const v2u*)p; }
DEV v2u ldstr(LAS const char* p) { return __builtin_bit_cast(v2u, __builtin_amdgcn_ds_read_tr16_b64_v4i16((LAS v4s*)p)); }
DEV v4u cat(v2u a, v2u b) { return v4u{a.x, a.y, b.x, b.y}; }
DEV unsigned pk2(float a, float b) { bf16x2 r = __builtin_convertvector(f32x2{a, b}, bf16x2); return __builtin_bit_cast(unsigned, r); }
DEV float blo(unsigned d) { return __builtin_bit_cast(float, d << 16); }
DEV float bhi(unsigned d) { return __builtin_bit_cast(float, d & 0xffff0000u); }
DEV void setm0(unsigned v) { asm volatile("s_mov_b32 m0, %0\n\ts_nop 0" ::"s"(v)); }
template <int OFF, int NT = 0> DEV void dma16(unsigned voff, rsrc_t rs, unsigned soff) {
  if constexpr (NT) asm volatile("buffer_load_dwordx4 %0, %1, %2 offen offset:%3 nt lds" ::"v"(voff), "s"(rs), "s"(soff), "i"(OFF));
  else asm volatile("buffer_load_dwordx4 %0, %1, %2 offen offset:%3 lds" ::"v"(voff), "s"(rs), "s"(soff), "i"(OFF));
}
typedef float f32x16 __attribute__((ext_vector_type(16)));
DEV f32x16 mfma32(v4u a, v4u b, f32x16 c) {
  return __builtin_amdgcn_mfma_f32_32x32x16_bf16(__builtin_bit_cast(bf16x8, a), __builtin_bit_cast(bf16x8, b), c, 0, 0, 0);
}
template <int IMM> DEV void setm0_add(unsigned base) { asm volatile("s_add_u32 m0, %0, %1" ::"s"(base), "i"(IMM) : "scc"); }
template <int I, int N, class Fn> DEV void static_for(Fn&& f) { if constexpr (I < N) { f(std::integral_constant<int, I>{}); static_for<I + 1, N>(f); } }
DEV void mfma_ip(f32x4& c, const v4u& a, const v4u& b) { asm volatile("v_mfma_f32_16x16x32_bf16 %0, %1, %2, %0" : "+v"(c) : "v"(a), "v"(b)); }
DEV void mfma_ipA(f32x4& c, const v4u& a, const v4u& b) { asm volatile("v_mfma_f32_16x16x32_bf16 %0, %1, %2, %0" : "+v"(c) : "a"(a), "v"(b)); }
DEV void mfma_z(f32x4& c, const v4u& a, const v4u& b) { asm volatile("v_mfma_f32_16x16x32_bf16 %0, %1, %2, 0" : "=&v"(c) : "v"(a), "v"(b)); }
DEV void mfma_zA(f32x4& c, const v4u& a, const v4u& b) { asm volatile("v_mfma_f32_16x16x32_bf16 %0, %1, %2, 0" : "=&v"(c) : "a"(a), "v"(b)); }
template <int AUX> DEV void ldA(v4u& d, unsigned voff, rsrc_t rs, unsigned soff) {
  if constexpr (AUX) asm volatile("buffer_load_dwordx4 %0, %1, %2, %3 offen sc1" : "=a"(d) : "v"(voff), "s"(rs), "s"(soff));
  else asm volatile("buffer_load_dwordx4 %0, %1, %2, %3 offen" : "=a"(d) : "v"(voff), "s"(rs), "s"(soff));
}
template <int AUX> DEV void ldV(v4u& d, unsigned voff, rsrc_t rs, unsigned soff) {
  if constexpr (AUX == 2) asm volatile("buffer_load_dwordx4 %0, %1, %2, %3 offen nt" : "=v"(d) : "v"(voff), "s"(rs), "s"(soff));
  else if constexpr (AUX == 16) asm volatile("buffer_load_dwordx4 %0, %1, %2, %3 offen sc1" : "=v"(d) : "v"(voff), "s"(rs), "s"(soff));
  else asm volatile("buffer_load_dwordx4 %0, %1, %2, %3 offen" : "=v"(d) : "v"(voff), "s"(rs), "s"(soff));
}
DEV void nop8() { asm volatile("s_nop 7"); }

constexpr size_t WS_CTRL = 0;
constexpr size_t WS_KVT = 262144;
constexpr size_t WS_YT = WS_KVT + (size_t)D * S * 2;
constexpr size_t WS_W2T = WS_YT + (size_t)D * S * 2;
constexpr size_t WS_HF = WS_W2T + (size_t)8 * 48 * 5 * 1024;
constexpr size_t WS_G = WS_HF + (size_t)128 * 48 * 1024;
constexpr size_t WS_END = WS_G + S * 4;
constexpr unsigned C_EPOCH = 0, C_TMO = 256;
constexpr unsigned C_FW = 4096;
constexpr unsigned C_FKV = 16384;
constexpr unsigned C_FH = 32768;
constexpr unsigned C_FY = 98304;
constexpr unsigned C_BYTES = 262144;

#define NSTAMP 16
#define STAMP_OFF (size_t)(32u << 20)
#define STAMP_DECL
#define STAMP(i)
#define STAMP_FLUSH()
#define FT_DECL
#define FT(i)
#define FT_FLUSH()

struct Params {
  const u16 *q, *k, *v, *sk, *h, *Dv, *w1, *w2, *gw;
  u16* out;
  char* ws;
};

constexpr int LDS_BYTES = 160 * 1024;
constexpr int SC1 = 16;
#define NT_X 0
#define NT_W1 0
#define NT_P 0
constexpr int AUXP = NT_P ? 2 : 0;
#define IC(n) std::integral_constant<int, (n)>{}

DEV void ld32_sc1(unsigned& v, unsigned voff, rsrc_t rs) { asm volatile("buffer_load_dword %0, %1, %2, 0 offen sc1" : "=v"(v) : "v"(voff), "s"(rs)); }
DEV void st32_sc1(unsigned v, unsigned voff, rsrc_t rs) { asm volatile("buffer_store_dword %0, %1, %2, 0 offen sc1" ::"v"(v), "v"(voff), "s"(rs)); }
DEV void poll_finish(unsigned v, unsigned voff, rsrc_t rsC, unsigned tag) {
  asm volatile("" : "+v"(v));
  if (__builtin_amdgcn_ballot_w64(v != tag) == 0) return;
#pragma unroll 1
  for (int spins = 0; spins < (1 << 15); ++spins) {
    __builtin_amdgcn_s_sleep(1);
    ld32_sc1(v, voff, rsC);
    asm volatile("s_waitcnt vmcnt(0)" : "+v"(v)::"memory");
    if (__builtin_amdgcn_ballot_w64(v != tag) == 0) return;
  }
  st32_sc1(1u, C_TMO, rsC);
}

DEV void mfma32_ip(f32x16& c, const v4u& a, const v4u& b) { asm volatile("v_mfma_f32_32x32x16_bf16 %0, %1, %2, %0" : "+v"(c) : "v"(a), "v"(b)); }
DEV void mfma32_z(f32x16& c, const v4u& a, const v4u& b) { asm volatile("v_mfma_f32_32x32x16_bf16 %0, %1, %2, 0" : "=&v"(c) : "v"(a), "v"(b)); }

constexpr int KV_BASE = 32768, KV_GS = 1152, KV_V = 9 * KV_GS, KV_CG = 17 * KV_GS;
constexpr int CV_BASE = 32768;
constexpr int CV_HC = 4160;
constexpr int CV_U = 4 * CV_HC;
constexpr int CV_CH = CV_U + 4608;
constexpr int CV_EX = CV_BASE + 4 * CV_CH;
constexpr int G2_PART = 0;
constexpr int G2_Y = 81920;
constexpr int HW = 24;

template <int AUX> DEV void ldV2(v2u& d, unsigned voff, rsrc_t rs, unsigned soff) {
  if constexpr (AUX == 2) asm volatile("buffer_load_dwordx2 %0, %1, %2, %3 offen nt" : "=v"(d) : "v"(voff), "s"(rs), "s"(soff));
  else if constexpr (AUX == 16) asm volatile("buffer_load_dwordx2 %0, %1, %2, %3 offen sc1" : "=v"(d) : "v"(voff), "s"(rs), "s"(soff));
  else asm volatile("buffer_load_dwordx2 %0, %1, %2, %3 offen" : "=v"(d) : "v"(voff), "s"(rs), "s"(soff));
}
DEV void dma16s(unsigned voff, rsrc_t rs, unsigned soff, unsigned m0v) {
  if constexpr (NT_P) asm volatile("s_mov_b32 m0, %3\n\ts_nop 0\n\tbuffer_load_dwordx4 %0, %1, %2 offen nt lds" ::"v"(voff), "s"(rs), "s"(soff), "s"(m0v));
  else asm volatile("s_mov_b32 m0, %3\n\ts_nop 0\n\tbuffer_load_dwordx4 %0, %1, %2 offen lds" ::"v"(voff), "s"(rs), "s"(soff), "s"(m0v));
}
DEV unsigned mktag(unsigned& epoch) {
  asm volatile("" : "+v"(epoch));
  return __builtin_amdgcn_readfirstlane(epoch * 0x9E3779B1u + 0x7F4A7C15u);
}

template <int NR, class ST> DEV void kv_math(const v4u (&kk)[NR + 3], const v4u (&vv)[NR], const v4u (&ww)[4], ST&& st) {
#pragma unroll
  for (int cp = 0; cp < 4; ++cp) {
    f32x2 w[4];
    w[0] = f32x2{blo(ww[cp].x), blo(ww[cp].z)}; w[1] = f32x2{bhi(ww[cp].x), bhi(ww[cp].z)};
    w[2] = f32x2{blo(ww[cp].y), blo(ww[cp].w)}; w[3] = f32x2{bhi(ww[cp].y), bhi(ww[cp].w)};
    f32x2 kf[NR + 3];
#pragma unroll
    for (int i = 0; i < NR + 3; ++i) kf[i] = f32x2{blo(kk[i][cp]), bhi(kk[i][cp])};
    f32x2 r[NR];
#pragma unroll
    for (int i = 0; i < NR; ++i) {
      f32x2 a = kf[i + 3] * w[0];
      a = __builtin_elementwise_fma(kf[i + 2], w[1], a);
      a = __builtin_elementwise_fma(kf[i + 1], w[2], a);
      a = __builtin_elementwise_fma(kf[i], w[3], a);
      r[i] = a * f32x2{blo(vv[i][cp]), bhi(vv[i][cp])};
    }
    st(cp, r);
  }
}

template <bool G1R>
DEV void body(const Params& p, LAS char* lds, const int x, const int r, const int wave, const int lane) {
  const int g = lane >> 4, q4 = (lane >> 2) & 3, p4 = lane & 3, c16 = lane & 15;
  STAMP_DECL; FT_DECL;
  STAMP(0);
  const rsrc_t rsC = mkrs(p.ws + WS_CTRL, C_BYTES);
  unsigned epoch, tag;
  ld32_sc1(epoch, C_EPOCH, rsC);
  const int cl = G1R ? (wave >> 1) : wave, half = G1R ? (wave & 1) : 0;
  const int ch = (G1R ? 80 * x + 2 * r : 80 * x + 48 + 4 * (r - 24)) + cl;
  const unsigned cb = CV_BASE + cl * CV_CH;
  constexpr int NCHK = G1R ? 2 : 4;
  unsigned dv;
  { const rsrc_t rsD = mkrs(p.Dv, D * 2); asm volatile("buffer_load_ushort %0, %1, %2, 0 offen" : "=v"(dv) : "v"(ch * 2), "s"(rsD)); }
  const rsrc_t rsHh = mkrs(p.h, D * S * 2), rsU = mkrs(p.ws + WS_KVT, D * S * 2), rsY = mkrs(p.ws + WS_YT, D * S * 2);
  const rsrc_t rsW2 = mkrs(p.ws + WS_W2T, 8 * 48 * 5 * 1024), rsH = mkrs(p.ws + WS_HF, 128 * 48 * 1024);
  const int rb = 4 * x + (r & 3), nt = r >> 2;
  const unsigned vl = lane * 16;
  const unsigned sW = (nt * 48 + 12 * wave) * 5 * 1024;
  v4u W[12][5];
  auto ldW = [&](auto I) { constexpr int i = I.value; if constexpr (i < 60) ldA<SC1>(W[i / 5][i % 5], vl, rsW2, sW + i * 1024); };
  v4u hc[NCHK], uc[NCHK]; v2u hp[NCHK];
  auto ld_h = [&]() {
#pragma unroll
    for (int i = 0; i < NCHK; ++i) {
      const int n = (half * NCHK + i) * 64 + lane;
      ldV<AUXP>(hc[i], ch * 4096 + n * 16, rsHh, 0);
      ldV2<AUXP>(hp[i], n > 0 ? (unsigned)(ch * 4096 + n * 16 - 8) : 0x80000000u, rsHh, 0);
    }
  };
  auto ld_u = [&]() {
#pragma unroll
    for (int i = 0; i < NCHK; ++i) ldV<SC1>(uc[i], ch * 4096 + ((half * NCHK + i) * 64 + lane) * 16, rsU, 0);
  };
  auto fill_h = [&]() {
#pragma unroll
    for (int i = 0; i < NCHK; ++i) {
      asm volatile("" : "+v"(hc[i]), "+v"(hp[i]));
      const int n = (half * NCHK + i) * 64 + lane;
      const v4u cur = hc[i]; const v2u prv = hp[i];
      const unsigned om2 = __builtin_amdgcn_alignbit(prv.y, prv.x, 16), om1 = __builtin_amdgcn_alignbit(cur.x, prv.y, 16);
      const unsigned o0 = __builtin_amdgcn_alignbit(cur.y, cur.x, 16), o1 = __builtin_amdgcn_alignbit(cur.z, cur.y, 16), o2 = __builtin_amdgcn_alignbit(cur.w, cur.z, 16);
      LAS char* dst = lds + cb + (n + 2) * 16;
      *(LAS v4u*)(dst) = cur;
      *(LAS v4u*)(dst + CV_HC) = v4u{om1, o0, o1, o2};
      *(LAS v4u*)(dst + 2 * CV_HC) = v4u{prv.y, cur.x, cur.y, cur.z};
      *(LAS v4u*)(dst + 3 * CV_HC) = v4u{om2, om1, o0, o1};
    }
    if (half == 0) {
      if (lane < 8) *(LAS v4u*)(lds + cb + (lane >> 1) * CV_HC + (lane & 1) * 16) = v4u{0, 0, 0, 0};
      if (lane >= 32 && lane < 62) *(LAS v4u*)(lds + cb + CV_U + (lane - 32) * 16) = v4u{0, 0, 0, 0};
    }
  };
  auto fill_u = [&]() {
#pragma unroll
    for (int i = 0; i < NCHK; ++i) {
      asm volatile("" : "+v"(uc[i]));
      *(LAS v4u*)(lds + cb + CV_U + 480 + ((half * NCHK + i) * 64 + lane) * 16) = uc[i];
    }
  };

  if constexpr (G1R) {
    const int sb = 4 * (x >> 1) + (r & 3), fs = 6 * (x & 1) + (r >> 2);
    f32x16 acc1[2][2];
    const rsrc_t rsX = mkrs(p.q, S * D * 2), rsW = mkrs(p.w1, D * F * 2);
    unsigned vX[4], vW[4];
#pragma unroll
    for (int ii = 0; ii < 4; ++ii) {
      vX[ii] = (32 * wave + 8 * ii + (lane >> 3)) * 1280 + (((lane & 7) ^ (4 * (ii & 1) + g)) << 4) - 1024 * ii;
      vW[ii] = (16 * wave + 4 * ii + g) * 3072 + (((((c16 >> 1) ^ (g << 1)) << 1) | (c16 & 1)) << 4) - 1024 * ii;
    }
    const unsigned sX0 = sb * 128 * 1280, sW0 = fs * 256;
    const unsigned mX = wave * 4096, mW = 16384 + wave * 4096;
    auto dmaX = [&](auto T, auto I) { constexpr int t = T.value, i = I.value; if constexpr (t < 10) dma16<1024 * i, NT_X>(vX[i], rsX, sX0 + t * 128); };
    auto dmaW = [&](auto T, auto I) { constexpr int t = T.value, i = I.value; if constexpr (t < 10) dma16<1024 * i, NT_W1>(vW[i], rsW, sW0 + t * 64 * 3072); };
    auto m0X = [&](auto T) { constexpr int t = T.value; if constexpr (t < 10) setm0_add<(t % 5) * 32768>(mX); };
    auto m0W = [&](auto T) { constexpr int t = T.value; if constexpr (t < 10) setm0_add<(t % 5) * 32768>(mW); };
    static_for<0, 4>([&](auto T) {
      m0X(T); asm volatile("s_nop 0");
      static_for<0, 4>([&](auto I) { dmaX(T, I); });
      if constexpr (T.value < 3) { m0W(T); asm volatile("s_nop 0"); static_for<0, 4>([&](auto I) { dmaW(T, I); }); }
    });
    const int ws_ = wave & 1, wf = wave >> 1, h = lane >> 5, r32 = lane & 31;
    unsigned aX[3][4], aW[3][2];
#pragma unroll
    for (int w = 0; w < 3; ++w) {
#pragma unroll
      for (int ks = 0; ks < 4; ++ks) aX[w][ks] = w * 65536 + (64 * ws_ + r32) * 128 + (((2 * ks + h) ^ ((r32 >> 1) & 7)) << 4);
#pragma unroll
      for (int ft = 0; ft < 2; ++ft) aW[w][ft] = w * 65536 + 16384 + (8 * h + q4) * 256 + (((2 * (2 * wf + ft) + (g & 1)) ^ (q4 << 1)) << 5) + 8 * p4;
    }
    v4u A[2][2][2], B[2][2][2];
    v2u At;
    auto rd = [&](auto T, auto HF, auto SET, auto N) {
      constexpr int t = T.value, hf = HF.value, set = SET.value, n = N.value;
      if constexpr (t < 10) {
        constexpr int b = t % 5, w = b >> 1, bo = (b & 1) * 32768;
        constexpr int kk = n / 6, rr = n % 6, ks = 2 * hf + kk;
        if constexpr (rr == 0) B[set][kk][0] = lds128(lds + aX[w][ks] + bo);
        else if constexpr (rr == 3) B[set][kk][1] = lds128(lds + aX[w][ks] + bo + 32 * 128);
        else { constexpr int ft = rr / 3, jj = (rr % 3) - 1;
          v2u xx = ldstr(lds + aW[w][ft] + bo + ks * 4096 + jj * 1024);
          if constexpr (jj == 0) At = xx; else A[set][kk][ft] = cat(At, xx); }
      }
    };
    unsigned fw, fk;
    waitv<20>(); sbar();
    STAMP(1);
    static_for<0, 12>([&](auto N) { rd(IC(0), IC(0), IC(0), N); });
    SCHED();
    static_for<0, 10>([&](auto T) {
      constexpr int t = T.value;
      static_for<0, 2>([&](auto HF) {
        constexpr int hf = HF.value;
        static_for<0, 8>([&](auto M) {
          constexpr int m = M.value, kk = m >> 2, ft = (m >> 1) & 1, st = m & 1;
          if constexpr (t == 0 && hf == 0 && kk == 0) mfma32_z(acc1[ft][st], A[hf][kk][ft], B[hf][kk][st]);
          else mfma32_ip(acc1[ft][st], A[hf][kk][ft], B[hf][kk][st]);
          if constexpr (m < 6) {
            if constexpr (hf == 0) { rd(T, IC(1), IC(1), IC(2 * m)); rd(T, IC(1), IC(1), IC(2 * m + 1)); }
            else { rd(IC(t + 1), IC(0), IC(0), IC(2 * m)); rd(IC(t + 1), IC(0), IC(0), IC(2 * m + 1)); }
          }
          if constexpr (m == 0) { if constexpr (hf == 0) m0W(IC(t + 3)); else m0X(IC(t + 4)); }
          if constexpr (m & 1) { if constexpr (hf == 0) dmaW(IC(t + 3), IC(m >> 1)); else dmaX(IC(t + 4), IC(m >> 1)); }
          if constexpr (t == 6 && hf == 1 && m == 1) ld32_sc1(fw, C_FW + lane * 128, rsC);
          if constexpr (t == 6 && hf == 1 && m == 3) ld32_sc1(fk, C_FKV + lane * 128, rsC);
          if constexpr (t == 6 && hf == 1 && m == 5) ld_h();
          SCHED();
        });
        if constexpr (hf == 0 && t < 9) {
          constexpr int younger = (t + 2 < 10 ? 8 : 0) + (t + 3 < 10 ? 8 : 0) + (t >= 7 ? 6 : 0);
          waitv<younger>(); sbar(); SCHED();
        }
      });
      FT(t); SCHED();
    });
    asm volatile("s_nop 15");
    waitv<0>();
    sbar();
    STAMP(2);
    tag = mktag(epoch);
    poll_finish(fw, C_FW + lane * 128, rsC, tag);
    poll_finish(fk, C_FKV + lane * 128, rsC, tag);
    STAMP(3);
    ld_u();
    const unsigned vo = (g & 1) * (48 * 1024) + (c16 + 16 * h) * 16;
    static_for<0, 4>([&](auto Q) {
      constexpr int qd = Q.value, st = qd >> 1, ft = qd & 1;
      const f32x16 a = acc1[ft][st];
      unsigned o[8];
      static_for<0, 8>([&](auto E) {
        constexpr int e = 2 * E.value;
        const float r0 = a[e] * __builtin_amdgcn_rcpf(1.f + __builtin_amdgcn_exp2f(a[e] * -1.44269504088896f));
        const float r1 = a[e + 1] * __builtin_amdgcn_rcpf(1.f + __builtin_amdgcn_exp2f(a[e + 1] * -1.44269504088896f));
        o[e >> 1] = pk2(r0, r1);
        if constexpr ((E.value % 4) != 3) ldW(IC(qd * 6 + E.value - E.value / 4));
        SCHED();
      });
      const unsigned so = ((8 * sb + 4 * ws_ + 2 * st) * 48 + 4 * fs + 2 * wf + ft) * 1024;
      __builtin_amdgcn_raw_buffer_store_b128(v4u{o[0], o[1], o[4], o[5]}, rsH, vo, so, SC1);
      __builtin_amdgcn_raw_buffer_store_b128(v4u{o[2], o[3], o[6], o[7]}, rsH, vo + 512, so, SC1);
      SCHED();
    });
    STAMP(4);
    waitv<32>();
    fill_h(); fill_u();
    waitv<0>();
    if (lane == 0) st32_sc1(tag, C_FH + ((sb * 12 + fs) * 4 + wave) * 64, rsC);
    waitl<0>(); sbar();
  } else {
    const int j = (r - 24) * 8 + x, rbk = j >> 1, cg0 = 5 * (j & 1);
    const rsrc_t rsK = mkrs(p.k, S * D * 2), rsV = mkrs(p.v, S * D * 2), rsS = mkrs(p.sk, D * 8), rsO = mkrs(p.ws + WS_KVT, D * S * 2);
    const unsigned vr = (lane >> 3) * 1280 + ((lane & 7) << 4), vr4 = vr ^ 64;
    auto dmaK = [&](int cgl, int G) {
      const int row = 64 * rbk - 8 + 8 * G;
      dma16s(((G >> 1) & 1) ? vr4 : vr, rsK, row >= 0 ? (unsigned)(row * 1280 + (cg0 + cgl) * 128) : 0x80000000u, KV_BASE + cgl * KV_CG + G * KV_GS);
    };
    auto dmaV = [&](int cgl, int G) {
      dma16s(((G >> 1) & 1) ? vr4 : vr, rsV, (64 * rbk + 8 * G) * 1280 + (cg0 + cgl) * 128, KV_BASE + cgl * KV_CG + KV_V + G * KV_GS);
    };
    const int id = 4 * j + wave, pk = id / 5, blk = id % 5;
    const rsrc_t rsWs = mkrs(p.w2, F * D * 2);
    const unsigned ldsb = wave * 8192;
#pragma unroll
    for (int i = 0; i < 8; ++i) {
      const int row = 4 * i + g;
      dma16s((32 * pk + row) * 1280 + blk * 256 + (((((c16 >> 1) ^ (row & 7)) << 1) | (c16 & 1)) << 4), rsWs, id < 240 ? 0u : 0x80000000u, ldsb + i * 1024);
    }
#pragma unroll
    for (int G = 0; G < 9; ++G) dmaK(wave, G);
#pragma unroll
    for (int G = 0; G < 8; ++G) dmaV(wave, G);
    dmaK(4, wave); dmaK(4, wave + 4); dmaV(4, wave); dmaV(4, wave + 4);
    if (wave == 0) dmaK(4, 8);
    const int cc = lane >> 3, rg = lane & 7;
    v4u ww[4], w4[4];
#pragma unroll
    for (int i = 0; i < 4; ++i) ldV<0>(ww[i], (64 * (cg0 + wave) + 8 * cc) * 8 + i * 16, rsS, 0);
#pragma unroll
    for (int i = 0; i < 4; ++i) ldV<0>(w4[i], (64 * (cg0 + 4) + 8 * (2 * wave + (lane >> 5))) * 8 + i * 16, rsS, 0);
    const rsrc_t rsX = mkrs(p.q, S * D * 2), rsG = mkrs(p.gw, D * 2), rsGo = mkrs(p.ws + WS_G, S * 4);
    const int grow = 8 * id + (lane >> 3), part = lane & 7;
    v4u xx[10], gg[10];
#pragma unroll
    for (int i = 0; i < 10; ++i) { ldV<AUXP>(xx[i], grow * 1280 + i * 128 + part * 16, rsX, 0); ldV<0>(gg[i], i * 128 + part * 16, rsG, 0); }
    waitv<49>();
    STAMP(1);
    {
      const rsrc_t rsWo = mkrs(p.ws + WS_W2T, 8 * 48 * 5 * 1024);
      const int r0 = 4 * g + q4;
#pragma unroll
      for (int i = 0; i < 8; ++i) {
        const unsigned a = ldsb + r0 * 256 + ((i ^ (r0 & 7)) << 5) + 8 * p4;
        const v4u fr = cat(ldstr(lds + a), ldstr(lds + a + 4096));
        const int ctg = 8 * blk + i, ntt = ctg / 5, ct = ctg % 5;
        __builtin_amdgcn_raw_buffer_store_b128(fr, rsWo, vl, id < 240 ? (unsigned)(((ntt * 48 + pk) * 5 + ct) * 1024) : 0x80000000u, SC1);
      }
    }
    waitv<32>();
    {
      const unsigned kb = KV_BASE + wave * KV_CG;
      const unsigned a0 = kb + rg * KV_GS + ((cc ^ (4 * ((rg >> 1) & 1))) << 4), a1 = kb + (rg + 1) * KV_GS + ((cc ^ (4 * (((rg + 1) >> 1) & 1))) << 4);
      v4u kk[11], vv[8];
#pragma unroll
      for (int i = 0; i < 11; ++i) kk[i] = i < 3 ? lds128(lds + a0 + (5 + i) * 128) : lds128(lds + a1 + (i - 3) * 128);
#pragma unroll
      for (int i = 0; i < 8; ++i) vv[i] = lds128(lds + a0 + KV_V + i * 128);
#pragma unroll
      for (int i = 0; i < 4; ++i) asm volatile("" : "+v"(ww[i]));
      const unsigned ob = (64 * (cg0 + wave) + 8 * cc) * 4096 + (64 * rbk + 8 * rg) * 2;
      kv_math<8>(kk, vv, ww, [&](int cp, const f32x2 (&rr)[8]) {
#pragma unroll
        for (int c = 0; c < 2; ++c) {
          v4u o{pk2(rr[0][c], rr[1][c]), pk2(rr[2][c], rr[3][c]), pk2(rr[4][c], rr[5][c]), pk2(rr[6][c], rr[7][c])};
          __builtin_amdgcn_raw_buffer_store_b128(o, rsO, ob + (2 * cp + c) * 4096, 0, SC1);
        }
      });
    }
    waitv<8>(); sbar();
    tag = mktag(epoch);
    if (threadIdx.x == 0) st32_sc1(tag, C_FW + j * 128, rsC);
    STAMP(2);
    {
      const int cc4 = 2 * wave + (lane >> 5), rp = lane & 31;
      const unsigned kb = KV_BASE + 4 * KV_CG;
      v4u kk[5], vv[2];
#pragma unroll
      for (int i = 0; i < 5; ++i) { const int row = 2 * rp + 5 + i; kk[i] = lds128(lds + kb + (row >> 3) * KV_GS + (row & 7) * 128 + ((cc4 ^ (4 * ((row >> 4) & 1))) << 4)); }
#pragma unroll
      for (int i = 0; i < 2; ++i) { const int row = 2 * rp + i; vv[i] = lds128(lds + kb + KV_V + (row >> 3) * KV_GS + (row & 7) * 128 + ((cc4 ^ (4 * ((row >> 4) & 1))) << 4)); }
#pragma unroll
      for (int i = 0; i < 4; ++i) asm volatile("" : "+v"(w4[i]));
      const unsigned ob = (64 * (cg0 + 4) + 8 * cc4) * 4096 + (64 * rbk + 2 * rp) * 2;
      kv_math<2>(kk, vv, w4, [&](int cp, const f32x2 (&rr)[2]) {
#pragma unroll
        for (int c = 0; c < 2; ++c) __builtin_amdgcn_raw_buffer_store_b32(pk2(rr[0][c], rr[1][c]), rsO, ob + (2 * cp + c) * 4096, 0, SC1);
      });
    }
    {
      float a = 0.f;
#pragma unroll
      for (int i = 0; i < 10; ++i) {
        asm volatile("" : "+v"(xx[i]), "+v"(gg[i]));
#pragma unroll
        for (int jj = 0; jj < 4; ++jj) { a = fmaf(blo(xx[i][jj]), blo(gg[i][jj]), a); a = fmaf(bhi(xx[i][jj]), bhi(gg[i][jj]), a); }
      }
      a += __shfl_xor(a, 1); a += __shfl_xor(a, 2); a += __shfl_xor(a, 4);
      const float gt = __builtin_amdgcn_rcpf(1.f + __builtin_amdgcn_exp2f(a * -1.44269504088896f));
      if (part == 0) __builtin_amdgcn_raw_buffer_store_b32(__builtin_bit_cast(unsigned, gt), rsGo, grow * 4, 0, SC1);
    }
    ld_h();
    waitl<0>(); sbar();
    waitv<0>();
    sbar();
    if (threadIdx.x == 0) st32_sc1(tag, C_FKV + j * 128, rsC);
    STAMP(3);
    fill_h();
    unsigned fw, fk;
    ld32_sc1(fw, C_FW + lane * 128, rsC);
    ld32_sc1(fk, C_FKV + lane * 128, rsC);
    waitv<0>();
    poll_finish(fw, C_FW + lane * 128, rsC, tag);
    poll_finish(fk, C_FKV + lane * 128, rsC, tag);
    ld_u();
    STAMP(4);
    static_for<0, 12>([&](auto I) { ldW(I); });
    waitv<12>();
    fill_u();
    waitl<0>();
  }
  STAMP(5);
  constexpr int NZ = G1R ? 4 : 8, W0 = G1R ? 24 : 12;
  f32x4 acc[8];
  {
    const unsigned aA = cb + q4 * CV_HC + 56 - 16 * g + 8 * p4 + 256 * half;
    const unsigned aB = cb + CV_U + 448 + 32 * c16 + 16 * g - 256 * half - 64 * (NZ - 1);
    v4u A[8], B[8];
#pragma unroll
    for (int i = 0; i < 8; ++i) {
      B[7 - i] = lds128(lds + aB + 64 * (NZ - 1) + 512 * (7 - i));
      A[i] = cat(ldstr(lds + aA + 512 * i + 8), ldstr(lds + aA + 512 * i));
    }
    SCHED();
    static_for<0, NZ>([&](auto Z) {
      constexpr int z = Z.value;
      static_for<0, 8>([&](auto I) {
        constexpr int i = I.value;
        static_for<0, 8 - i>([&](auto DD) {
          constexpr int d = 7 - i - DD.value;
          constexpr int gap = z * 36 + (i * (17 - i)) / 2 + DD.value;
          if constexpr (z == 0 && i == 0) mfma_z(acc[i + d], A[i], B[d]); else mfma_ip(acc[i + d], A[i], B[d]);
          if constexpr (d == 7 - i && z + 1 < NZ) B[7 - i] = lds128(lds + aB + 64 * (NZ - 2 - z) + 512 * (7 - i));
          constexpr int WSTEP = G1R ? 4 : 6;
          if constexpr (gap % WSTEP == 2) ldW(IC(W0 + gap / WSTEP));
          SCHED();
        });
        if constexpr (z + 1 < NZ) { A[i] = cat(ldstr(lds + aA + 64 * (z + 1) + 512 * i + 8), ldstr(lds + aA + 64 * (z + 1) + 512 * i)); SCHED(); }
      });
    });
  }
  static_assert(W0 + (NZ * 36 - 3) / (G1R ? 4 : 6) >= 59, "all W2 fragments issued");
  nop8(); SCHED();
  STAMP(6);
  const unsigned foH = C_FH + (((rb >> 1) * 12 + (lane < 24 ? lane >> 1 : 0)) * 4 + (rb & 1) + 2 * (lane & 1)) * 64;
  unsigned fh;
  ld32_sc1(fh, foH, rsC);
  int ctb = 0;
  if constexpr (G1R) {
    LAS char* ex = lds + CV_EX + cl * 8192;
f32x4 keep[4];
#pragma unroll
    for (int i = 0; i < 4; ++i) {
      f32x4 lo = acc[i], hi = acc[4 + i];
      asm volatile("" : "+v"(lo), "+v"(hi));
      *(LAS f32x4*)(ex + half * 4096 + i * 1024 + lane * 16) = half == 0 ? hi : lo;
      keep[i] = half == 0 ? lo : hi;
    }
    waitl<0>(); sbar();
#pragma unroll
    for (int i = 0; i < 4; ++i) acc[i] = keep[i] + *(LAS f32x4*)(ex + (1 - half) * 4096 + i * 1024 + lane * 16);
    ctb = 4 * half;
  }
  constexpr int NT = G1R ? 4 : 8;
  {
    asm volatile("" : "+v"(dv));
    const float Dc = blo(dv);
#pragma unroll
    for (int i = 0; i < NT; i += 2) {
      unsigned w0[2][2];
#pragma unroll
      for (int e = 0; e < 2; ++e) {
        const int ct = ctb + i + e;
        const f32x4 a = acc[i + e];
        const v2u uu = lds64(lds + cb + CV_U + 480 + 512 * ct + 32 * c16 + 8 * g);
        w0[e][0] = pk2(fmaf(blo(uu.x), Dc, a[0]), fmaf(bhi(uu.x), Dc, a[1]));
        w0[e][1] = pk2(fmaf(blo(uu.y), Dc, a[2]), fmaf(bhi(uu.y), Dc, a[3]));
      }
      auto s0 = __builtin_amdgcn_permlane16_swap(w0[0][0], w0[1][0], false, false);
      auto s1 = __builtin_amdgcn_permlane16_swap(w0[0][1], w0[1][1], false, false);
      const int ct = ctb + i + (g & 1);
      __builtin_amdgcn_raw_buffer_store_b128(v4u{s0[0], s1[0], s0[1], s1[1]}, rsY, ch * 4096 + 512 * ct + 32 * c16 + 8 * (g & 2), 0, SC1);
    }
  }
  waitl<0>(); sbar();
  STAMP(7);
  waitv<NT / 2>();
  poll_finish(fh, foH, rsC, tag);
  STAMP(8);
  FT(14);
  v4u Hf[HW];
  const unsigned sH = (4 * rb * 48 + 12 * wave) * 1024;
  static_for<0, HW>([&](auto I) { constexpr int i = I.value; ldV<SC1>(Hf[i], vl, rsH, sH + ((i / 12) * 48 + i % 12) * 1024); });
  FT(15);
  f32x4 ac2[5];
  const unsigned foY0 = C_FY + (((lane >> 2) * 8 + nt) * 4 + (lane & 3)) * 64, foY1 = foY0 + 16 * 8 * 4 * 64;
  unsigned fy0, fy1, gtu;
  v4u yv[3]; v2u qq[5];
  const int s = 64 * rb + 16 * wave + c16;
  constexpr int SP = 20;
  constexpr int SY = SP + HW + 1 < 47 ? SP + HW + 1 : 47;
  static_for<0, 48>([&](auto I) {
    constexpr int i = I.value, rt = i / 12, k = i % 12;
    constexpr int yH = (i + HW - 1 < 47 ? i + HW - 1 : 47) - i;
    constexpr int ex = ((0 < i && i <= HW) ? 1 : 0) + ((SP < i && i <= SP + HW) ? 8 : 0) + ((SY < i && i <= SY + HW) ? 3 : 0);
    waitv<yH + ex>();
    static_for<0, 5>([&](auto C) { constexpr int ct = C.value;
      if constexpr (k == 0) mfma_zA(ac2[ct], W[k][ct], Hf[i % HW]); else mfma_ipA(ac2[ct], W[k][ct], Hf[i % HW]);
    });
    if constexpr (i + HW < 48) { constexpr int n = i + HW; ldV<SC1>(Hf[n % HW], vl, rsH, sH + ((n / 12) * 48 + n % 12) * 1024); }
    if constexpr (i == 0) { if (lane == 0) st32_sc1(tag, C_FY + (blockIdx.x * 4 + wave) * 64, rsC); }
    if constexpr (i == SP) {
      ld32_sc1(fy0, foY0, rsC); ld32_sc1(fy1, foY1, rsC);
      const rsrc_t rsG = mkrs(p.ws + WS_G, S * 4), rsQ = mkrs(p.q, S * D * 2);
      ld32_sc1(gtu, s * 4, rsG);
#pragma unroll
      for (int ct = 0; ct < 5; ++ct) ldV2<AUXP>(qq[ct], (s * D + 80 * nt + 16 * ct + 4 * g) * 2, rsQ, 0);
    }
    if constexpr (i == SY) {
      poll_finish(fy0, foY0, rsC, tag); poll_finish(fy1, foY1, rsC, tag);
#pragma unroll
      for (int u = 0; u < 3; ++u) {
        const int idx = u * 256 + wave * 64 + lane, d = idx >> 3, cch = idx & 7;
        ldV<SC1>(yv[u], idx < 640 ? (unsigned)((80 * nt + d) * 4096 + rb * 128 + cch * 16) : 0x80000000u, rsY, 0);
      }
    }
    if constexpr (i % 4 == 3) { FT(16 + i / 4); }
    if constexpr (k == 11) {
      nop8();
#pragma unroll
      for (int ct = 0; ct < 5; ++ct) *(LAS f32x4*)(lds + G2_PART + ((rt * 4 + wave) * 5 + ct) * 1024 + lane * 16) = ac2[ct];
    }
    SCHED();
  });
  STAMP(9);
  unsigned fe[16];
  if (blockIdx.x == 0 && wave == 0) {
#pragma unroll
    for (int i = 0; i < 16; ++i) ld32_sc1(fe[i], C_FY + (i * 64 + lane) * 64, rsC);
  }
  waitv<0>();
#pragma unroll
  for (int u = 0; u < 3; ++u) {
    asm volatile("" : "+v"(yv[u]));
    const int idx = u * 256 + wave * 64 + lane, d = idx >> 3, cch = idx & 7;
    if (idx < 640) { *(LAS v2u*)(lds + G2_Y + d * 136 + cch * 16) = v2u{yv[u].x, yv[u].y}; *(LAS v2u*)(lds + G2_Y + d * 136 + cch * 16 + 8) = v2u{yv[u].z, yv[u].w}; }
  }
  asm volatile("" : "+v"(gtu));
#pragma unroll
  for (int ct = 0; ct < 5; ++ct) asm volatile("" : "+v"(qq[ct]));
  waitl<0>(); sbar();
  STAMP(10);
  const float gt = __builtin_bit_cast(float, gtu);
#pragma unroll
  for (int ct = 0; ct < 5; ++ct) {
    f32x4 b = *(LAS f32x4*)(lds + G2_PART + ((wave * 4 + 0) * 5 + ct) * 1024 + lane * 16);
#pragma unroll
    for (int w = 1; w < 4; ++w) b += *(LAS f32x4*)(lds + G2_PART + ((wave * 4 + w) * 5 + ct) * 1024 + lane * 16);
    const v2u yy = ldstr(lds + G2_Y + (16 * ct + 4 * g + q4) * 136 + (16 * wave + 4 * p4) * 2);
    const float o0 = gt * (blo(qq[ct].x) * blo(yy.x)) + (1.f - gt) * b[0];
    const float o1 = gt * (bhi(qq[ct].x) * bhi(yy.x)) + (1.f - gt) * b[1];
    const float o2 = gt * (blo(qq[ct].y) * blo(yy.y)) + (1.f - gt) * b[2];
    const float o3 = gt * (bhi(qq[ct].y) * bhi(yy.y)) + (1.f - gt) * b[3];
    *(GAS v2u*)(p.out + s * D + 80 * nt + 16 * ct + 4 * g) = v2u{pk2(o0, o1), pk2(o2, o3)};
  }
  if (blockIdx.x == 0 && wave == 0) {
#pragma unroll 1
    for (int i = 0; i < 1; ++i) {
#pragma unroll
      for (int e = 0; e < 16; ++e) poll_finish(fe[e], C_FY + (e * 64 + lane) * 64, rsC, tag);
    }
    if (lane == 0) st32_sc1(tag, C_EPOCH, rsC);
  }
  STAMP(11);
  STAMP_FLUSH(); FT_FLUSH();
}

__global__ __launch_bounds__(256, 1) void fused(Params p) {
  extern __shared__ __attribute__((aligned(16))) char smem[];
  LAS char* lds = (LAS char*)smem;
  const int lane = threadIdx.x & 63, wave = __builtin_amdgcn_readfirstlane(threadIdx.x >> 6);
  const int x = blockIdx.x & 7, r = blockIdx.x >> 3;
  unsigned pf = 0;
  {
    unsigned long long pc; unsigned len;
    asm volatile("s_getpc_b64 %0\nLpf_here:\n\ts_mov_b32 %1, Lpf_end-Lpf_here" : "=s"(pc), "=s"(len));
    const unsigned off = (r * 4 + wave) * 1024 + lane * 16;
    const rsrc_t rs = mkrs((const void*)pc, len);
    v4u t;
    asm volatile("buffer_load_dwordx4 %0, %1, %2, 0 offen" : "=v"(t) : "v"(off), "s"(rs));
    if (r < 24) { body<true>(p, lds, x, r, wave, lane); }
    else { body<false>(p, lds, x, r, wave, lane); }
    asm volatile("s_waitcnt vmcnt(0)\nLpf_end:" : "+v"(t));
    pf = t.x;
  }
  asm volatile("" ::"v"(pf));
}

extern "C" void kernel_launch(void* const* d_in, const int* in_sizes, int n_in, void* d_out, int out_size, void* d_ws, size_t ws_size, hipStream_t stream) {
  Params p;
  p.q = (const u16*)d_in[0]; p.k = (const u16*)d_in[1]; p.v = (const u16*)d_in[2]; p.sk = (const u16*)d_in[3];
  p.h = (const u16*)d_in[4]; p.Dv = (const u16*)d_in[5]; p.w1 = (const u16*)d_in[7]; p.w2 = (const u16*)d_in[8]; p.gw = (const u16*)d_in[9];
  p.out = (u16*)d_out; p.ws = (char*)d_ws;
  (void)hipFuncSetAttribute((const void*)fused, hipFuncAttributeMaxDynamicSharedMemorySize, LDS_BYTES);
  fused<<<256, 256, LDS_BYTES, stream>>>(p);
}
```

```cpp
#include <hip/hip_runtime.h>
#include <type_traits>
#define SPIN_SLEEP 4
#define FSTR 64
#include <stdint.h>

typedef unsigned short u16;
typedef unsigned v4u __attribute__((ext_vector_type(4)));
typedef unsigned v2u __attribute__((ext_vector_type(2)));
typedef float f32x4 __attribute__((ext_vector_type(4)));
typedef float f32x2 __attribute__((ext_vector_type(2)));
typedef __bf16 bf16x8 __attribute__((ext_vector_type(8)));
typedef __bf16 bf16x2 __attribute__((ext_vector_type(2)));
typedef short v4s __attribute__((ext_vector_type(4)));
typedef short v8s __attribute__((ext_vector_type(8)));
#define GAS __attribute__((address_space(1)))
#define LAS __attribute__((address_space(3)))
#define DEV __device__ __forceinline__
typedef __amdgpu_buffer_rsrc_t rsrc_t;

constexpr int S = 2048, D = 640, F = 1536;

template <int N> DEV void waitv() { asm volatile("s_waitcnt vmcnt(%0)" ::"i"(N) : "memory"); }
template <int N> DEV void waitl() { asm volatile("s_waitcnt lgkmcnt(%0)" ::"i"(N) : "memory"); }
DEV void sbar() { asm volatile("s_barrier" ::: "memory"); }
#define SCHED() __builtin_amdgcn_sched_barrier(0)
DEV rsrc_t mkrs(const void* p, unsigned bytes) { return __builtin_amdgcn_make_buffer_rsrc((void*)p, 0, bytes, 0x00020000); }
DEV f32x4 mfma(v4u a, v4u b, f32x4 c) {
  return __builtin_amdgcn_mfma_f32_16x16x32_bf16(__builtin_bit_cast(bf16x8, a), __builtin_bit_cast(bf16x8, b), c, 0, 0, 0);
}
DEV v4u lds128(LAS const char* p) { return *(LAS const v4u*)p; }
DEV v2u lds64(LAS const char* p) { return *(LAS const v2u*)p; }
DEV v2u ldstr(LAS const char* p) { return __builtin_bit_cast(v2u, __builtin_amdgcn_ds_read_tr16_b64_v4i16((LAS v4s*)p)); }
DEV v4u cat(v2u a, v2u b) { return v4u{a.x, a.y, b.x, b.y}; }
DEV unsigned pk2(float a, float b) { bf16x2 r = __builtin_convertvector(f32x2{a, b}, bf16x2); return __builtin_bit_cast(unsigned, r); }
DEV float blo(unsigned d) { return __builtin_bit_cast(float, d << 16); }
DEV float bhi(unsigned d) { return __builtin_bit_cast(float, d & 0xffff0000u); }
DEV void setm0(unsigned v) { asm volatile("s_mov_b32 m0, %0\n\ts_nop 0" ::"s"(v)); }
template <int OFF, int NT = 0> DEV void dma16(unsigned voff, rsrc_t rs, unsigned soff) {
  if constexpr (NT) asm volatile("buffer_load_dwordx4 %0, %1, %2 offen offset:%3 nt lds" ::"v"(voff), "s"(rs), "s"(soff), "i"(OFF));
  else asm volatile("buffer_load_dwordx4 %0, %1, %2 offen offset:%3 lds" ::"v"(voff), "s"(rs), "s"(soff), "i"(OFF));
}
typedef float f32x16 __attribute__((ext_vector_type(16)));
DEV f32x16 mfma32(v4u a, v4u b, f32x16 c) {
  return __builtin_amdgcn_mfma_f32_32x32x16_bf16(__builtin_bit_cast(bf16x8, a), __builtin_bit_cast(bf16x8, b), c, 0, 0, 0);
}
template <int IMM> DEV void setm0_add(unsigned base) { asm volatile("s_add_u32 m0, %0, %1" ::"s"(base), "i"(IMM) : "scc"); }
template <int I, int N, class Fn> DEV void static_for(Fn&& f) { if constexpr (I < N) { f(std::integral_constant<int, I>{}); static_for<I + 1, N>(f); } }
DEV void mfma_ip(f32x4& c, const v4u& a, const v4u& b) { asm volatile("v_mfma_f32_16x16x32_bf16 %0, %1, %2, %0" : "+v"(c) : "v"(a), "v"(b)); }
DEV void mfma_ipA(f32x4& c, const v4u& a, const v4u& b) { asm volatile("v_mfma_f32_16x16x32_bf16 %0, %1, %2, %0" : "+v"(c) : "a"(a), "v"(b)); }
DEV void mfma_z(f32x4& c, const v4u& a, const v4u& b) { asm volatile("v_mfma_f32_16x16x32_bf16 %0, %1, %2, 0" : "=&v"(c) : "v"(a), "v"(b)); }
DEV void mfma_zA(f32x4& c, const v4u& a, const v4u& b) { asm volatile("v_mfma_f32_16x16x32_bf16 %0, %1, %2, 0" : "=&v"(c) : "a"(a), "v"(b)); }
template <int AUX> DEV void ldA(v4u& d, unsigned voff, rsrc_t rs, unsigned soff) {
  if constexpr (AUX) asm volatile("buffer_load_dwordx4 %0, %1, %2, %3 offen sc1" : "=a"(d) : "v"(voff), "s"(rs), "s"(soff));
  else asm volatile("buffer_load_dwordx4 %0, %1, %2, %3 offen" : "=a"(d) : "v"(voff), "s"(rs), "s"(soff));
}
template <int AUX> DEV void ldV(v4u& d, unsigned voff, rsrc_t rs, unsigned soff) {
  if constexpr (AUX == 2) asm volatile("buffer_load_dwordx4 %0, %1, %2, %3 offen nt" : "=v"(d) : "v"(voff), "s"(rs), "s"(soff));
  else if constexpr (AUX == 16) asm volatile("buffer_load_dwordx4 %0, %1, %2, %3 offen sc1" : "=v"(d) : "v"(voff), "s"(rs), "s"(soff));
  else asm volatile("buffer_load_dwordx4 %0, %1, %2, %3 offen" : "=v"(d) : "v"(voff), "s"(rs), "s"(soff));
}
DEV void nop8() { asm volatile("s_nop 7"); }

constexpr size_t WS_CTRL = 0;
constexpr size_t WS_KVT = 262144;
constexpr size_t WS_YT = WS_KVT + (size_t)D * S * 2;
constexpr size_t WS_W2T = WS_YT + (size_t)D * S * 2;
constexpr size_t WS_HF = WS_W2T + (size_t)8 * 48 * 5 * 1024;
constexpr size_t WS_G = WS_HF + (size_t)128 * 48 * 1024;
constexpr size_t WS_END = WS_G + S * 4;
constexpr unsigned C_EPOCH = 0, C_TMO = 256;
constexpr unsigned FS = FSTR;
constexpr unsigned C_FW = 4096;
constexpr unsigned C_FKV = C_FW + 64 * FS;
constexpr unsigned C_FH = C_FKV + 64 * FS;
constexpr unsigned C_FY = C_FH + 768 * FS;
constexpr unsigned C_BYTES = 262144;
static_assert(C_FY + 1024 * FS <= C_BYTES, "");

#define NSTAMP 16
#define STAMP_OFF (size_t)(32u << 20)
#define STAMP_DECL
#define STAMP(i)
#define STAMP_FLUSH()
#define FT_DECL
#define FT(i)
#define FT_FLUSH()

struct Params {
  const u16 *q, *k, *v, *sk, *h, *Dv, *w1, *w2, *gw;
  u16* out;
  char* ws;
};

constexpr int LDS_BYTES = 160 * 1024;
constexpr int SC1 = 16;
#define NT_X 0
#define NT_W1 0
#define NT_P 0
constexpr int AUXP = NT_P ? 2 : 0;
#define IC(n) std::integral_constant<int, (n)>{}

DEV void ld32_sc1(unsigned& v, unsigned voff, rsrc_t rs) { asm volatile("buffer_load_dword %0, %1, %2, 0 offen sc1" : "=v"(v) : "v"(voff), "s"(rs)); }
DEV void st32_sc1(unsigned v, unsigned voff, rsrc_t rs) { asm volatile("buffer_store_dword %0, %1, %2, 0 offen sc1" ::"v"(v), "v"(voff), "s"(rs)); }
DEV void poll_finish(unsigned v, unsigned voff, rsrc_t rsC, unsigned tag) {
  asm volatile("" : "+v"(v));
  if (__builtin_amdgcn_ballot_w64(v != tag) == 0) return;
#pragma unroll 1
  for (int spins = 0; spins < (1 << 15); ++spins) {
    __builtin_amdgcn_s_sleep(SPIN_SLEEP);
    ld32_sc1(v, voff, rsC);
    asm volatile("s_waitcnt vmcnt(0)" : "+v"(v)::"memory");
    if (__builtin_amdgcn_ballot_w64(v != tag) == 0) return;
  }
  st32_sc1(1u, C_TMO, rsC);
}

DEV void poll_finish2(unsigned a, unsigned oa, unsigned b, unsigned ob, rsrc_t rsC, unsigned tag) {
  asm volatile("" : "+v"(a), "+v"(b));
  if (__builtin_amdgcn_ballot_w64(a != tag || b != tag) == 0) return;
#pragma unroll 1
  for (int spins = 0; spins < (1 << 15); ++spins) {
    __builtin_amdgcn_s_sleep(SPIN_SLEEP);
    ld32_sc1(a, oa, rsC); ld32_sc1(b, ob, rsC);
    asm volatile("s_waitcnt vmcnt(0)" : "+v"(a), "+v"(b)::"memory");
    if (__builtin_amdgcn_ballot_w64(a != tag || b != tag) == 0) return;
  }
  st32_sc1(1u, C_TMO, rsC);
}

DEV void mfma32_ip(f32x16& c, const v4u& a, const v4u& b) { asm volatile("v_mfma_f32_32x32x16_bf16 %0, %1, %2, %0" : "+v"(c) : "v"(a), "v"(b)); }
DEV void mfma32_z(f32x16& c, const v4u& a, const v4u& b) { asm volatile("v_mfma_f32_32x32x16_bf16 %0, %1, %2, 0" : "=&v"(c) : "v"(a), "v"(b)); }

constexpr int KV_BASE = 32768, KV_GS = 1152, KV_V = 9 * KV_GS, KV_CG = 17 * KV_GS;
constexpr int CV_BASE = 32768;
constexpr int CV_HC = 4160;
constexpr int CV_U = 4 * CV_HC;
constexpr int CV_CH = CV_U + 4608;
constexpr int CV_EX = CV_BASE + 4 * CV_CH;
constexpr int G2_PART = 0;
constexpr int G2_Y = 81920;
constexpr int HW = 24;

template <int AUX> DEV void ldV2(v2u& d, unsigned voff, rsrc_t rs, unsigned soff) {
  if constexpr (AUX == 2) asm volatile("buffer_load_dwordx2 %0, %1, %2, %3 offen nt" : "=v"(d) : "v"(voff), "s"(rs), "s"(soff));
  else if constexpr (AUX == 16) asm volatile("buffer_load_dwordx2 %0, %1, %2, %3 offen sc1" : "=v"(d) : "v"(voff), "s"(rs), "s"(soff));
  else asm volatile("buffer_load_dwordx2 %0, %1, %2, %3 offen" : "=v"(d) : "v"(voff), "s"(rs), "s"(soff));
}
DEV void dma16s(unsigned voff, rsrc_t rs, unsigned soff, unsigned m0v) {
  if constexpr (NT_P) asm volatile("s_mov_b32 m0, %3\n\ts_nop 0\n\tbuffer_load_dwordx4 %0, %1, %2 offen nt lds" ::"v"(voff), "s"(rs), "s"(soff), "s"(m0v));
  else asm volatile("s_mov_b32 m0, %3\n\ts_nop 0\n\tbuffer_load_dwordx4 %0, %1, %2 offen lds" ::"v"(voff), "s"(rs), "s"(soff), "s"(m0v));
}
DEV float dot2(unsigned a, unsigned b, float c) { return __builtin_amdgcn_fdot2_f32_bf16(__builtin_bit_cast(bf16x2, a), __builtin_bit_cast(bf16x2, b), c, false); }
DEV unsigned mktag(unsigned& epoch) {
  asm volatile("" : "+v"(epoch));
  return __builtin_amdgcn_readfirstlane(epoch * 0x9E3779B1u + 0x7F4A7C15u);
}

template <int NR, class ST> DEV void kv_math(const v4u (&kk)[NR + 3], const v4u (&vv)[NR], const v4u (&ww)[4], ST&& st) {
#pragma unroll
  for (int cp = 0; cp < 4; ++cp) {
    f32x2 w[4];
    w[0] = f32x2{blo(ww[cp].x), blo(ww[cp].z)}; w[1] = f32x2{bhi(ww[cp].x), bhi(ww[cp].z)};
    w[2] = f32x2{blo(ww[cp].y), blo(ww[cp].w)}; w[3] = f32x2{bhi(ww[cp].y), bhi(ww[cp].w)};
    f32x2 kf[NR + 3];
#pragma unroll
    for (int i = 0; i < NR + 3; ++i) kf[i] = f32x2{blo(kk[i][cp]), bhi(kk[i][cp])};
    f32x2 r[NR];
#pragma unroll
    for (int i = 0; i < NR; ++i) {
      f32x2 a = kf[i + 3] * w[0];
      a = __builtin_elementwise_fma(kf[i + 2], w[1], a);
      a = __builtin_elementwise_fma(kf[i + 1], w[2], a);
      a = __builtin_elementwise_fma(kf[i], w[3], a);
      r[i] = a * f32x2{blo(vv[i][cp]), bhi(vv[i][cp])};
    }
    st(cp, r);
  }
}

#define Q0 7
#define ROT 0
#define PREP_FIRST 0
#define HPUB 90
#define WPRE_ 16
constexpr int WPRE = WPRE_;
constexpr int HW0 = 16, HWM = 24;
constexpr int PW_BASE = 131072, PW_SZ = 3072;
constexpr int g2_issued(int i) { int r = HW0; for (int s = 0; s < i; ++s) r += (r - s < HWM) ? ((r + 1 - s < HWM) ? 2 : 1) : 0; return r < 48 ? r : 48; }

template <int OFF, int AUX> DEV void ldVo(v4u& d, unsigned voff, rsrc_t rs, unsigned soff) {
  static_assert(AUX == 16, "");
  asm volatile("buffer_load_dwordx4 %0, %1, %2, %3 offen offset:%4 sc1" : "=v"(d) : "v"(voff), "s"(rs), "s"(soff), "i"(OFF));
}
template <int OFF> DEV void ldAo(v4u& d, unsigned voff, rsrc_t rs, unsigned soff) {
  asm volatile("buffer_load_dwordx4 %0, %1, %2, %3 offen offset:%4 sc1" : "=a"(d) : "v"(voff), "s"(rs), "s"(soff), "i"(OFF));
}

template <bool G1R>
DEV void body(const Params& p, LAS char* lds, const int x, const int r, const int wave, const int lane) {
  const int g = lane >> 4, q4 = (lane >> 2) & 3, p4 = lane & 3, c16 = lane & 15;
  STAMP_DECL; FT_DECL;
  STAMP(0); FT(13);
  const rsrc_t rsC = mkrs(p.ws + WS_CTRL, C_BYTES);
  unsigned epoch, tag;
  ld32_sc1(epoch, C_EPOCH, rsC);
  const int cl = G1R ? (wave >> 1) : wave, half = G1R ? (wave & 1) : 0;
  const int ch = (G1R ? 80 * x + 2 * r : 80 * x + 48 + 4 * (r - 24)) + cl;
  const unsigned cb = CV_BASE + cl * CV_CH;
  constexpr int NCHK = G1R ? 2 : 4;
  unsigned dv;
  { const rsrc_t rsD = mkrs(p.Dv, D * 2); asm volatile("buffer_load_ushort %0, %1, %2, 0 offen" : "=v"(dv) : "v"(ch * 2), "s"(rsD)); }
  const rsrc_t rsHh = mkrs(p.h, D * S * 2), rsU = mkrs(p.ws + WS_KVT, D * S * 2), rsY = mkrs(p.ws + WS_YT, D * S * 2);
  const rsrc_t rsW2 = mkrs(p.ws + WS_W2T, 8 * 48 * 5 * 1024), rsH = mkrs(p.ws + WS_HF, 128 * 48 * 1024);
#define MAP42 1
  const int rb = MAP42 ? 8 * (x >> 1) + (r & 7) : 4 * x + (r & 3), nt = MAP42 ? 4 * (x & 1) + (r >> 3) : r >> 2;
  const unsigned vl = lane * 16;
  const unsigned sW = (nt * 48 + 12 * wave) * 5 * 1024;
  const unsigned foW = C_FW + lane * FS, foKV = C_FKV + lane * FS;
  v4u W[12][5];
  auto ldW = [&](auto I) { constexpr int i = I.value; if constexpr (i >= 0 && i < 60) ldAo<(i & 3) * 1024>(W[i / 5][i % 5], vl, rsW2, sW + (i & ~3) * 1024); };
  v4u hc[NCHK], uc[NCHK]; v2u hp0;
  auto ld_h = [&]() {
    ldV2<AUXP>(hp0, half > 0 ? (unsigned)(ch * 4096 + half * NCHK * 1024 - 8) : 0x80000000u, rsHh, 0);
#pragma unroll
    for (int i = 0; i < NCHK; ++i) ldV<AUXP>(hc[i], ch * 4096 + ((half * NCHK + i) * 64 + lane) * 16, rsHh, 0);
  };
  auto ld_u = [&]() {
#pragma unroll
    for (int i = 0; i < NCHK; ++i) ldV<SC1>(uc[i], ch * 4096 + ((half * NCHK + i) * 64 + lane) * 16, rsU, 0);
  };
  auto fill_h = [&]() {
    asm volatile("" : "+v"(hp0));
#pragma unroll
    for (int i = 0; i < NCHK; ++i) asm volatile("" : "+v"(hc[i]));
    unsigned e0 = hp0.x, e1 = hp0.y;
#pragma unroll
    for (int i = 0; i < NCHK; ++i) {
      const int n = (half * NCHK + i) * 64 + lane;
      const v4u cur = hc[i];
      const v2u prv = v2u{(unsigned)__builtin_amdgcn_update_dpp((int)e0, (int)cur.z, 0x138, 0xf, 0xf, false), (unsigned)__builtin_amdgcn_update_dpp((int)e1, (int)cur.w, 0x138, 0xf, 0xf, false)};
      e0 = __builtin_amdgcn_readlane(cur.z, 63); e1 = __builtin_amdgcn_readlane(cur.w, 63);
      const unsigned om2 = __builtin_amdgcn_alignbit(prv.y, prv.x, 16), om1 = __builtin_amdgcn_alignbit(cur.x, prv.y, 16);
      const unsigned o0 = __builtin_amdgcn_alignbit(cur.y, cur.x, 16), o1 = __builtin_amdgcn_alignbit(cur.z, cur.y, 16), o2 = __builtin_amdgcn_alignbit(cur.w, cur.z, 16);
      LAS char* dst = lds + cb + (n + 2) * 16;
      *(LAS v4u*)(dst) = cur;
      *(LAS v4u*)(dst + CV_HC) = v4u{om1, o0, o1, o2};
      *(LAS v4u*)(dst + 2 * CV_HC) = v4u{prv.y, cur.x, cur.y, cur.z};
      *(LAS v4u*)(dst + 3 * CV_HC) = v4u{om2, om1, o0, o1};
    }
    if (half == 0) {
      if (lane < 8) *(LAS v4u*)(lds + cb + (lane >> 1) * CV_HC + (lane & 1) * 16) = v4u{0, 0, 0, 0};
      if (lane >= 32 && lane < 62) *(LAS v4u*)(lds + cb + CV_U + (lane - 32) * 16) = v4u{0, 0, 0, 0};
    }
  };
  auto fill_u = [&]() {
#pragma unroll
    for (int i = 0; i < NCHK; ++i) {
      asm volatile("" : "+v"(uc[i]));
      *(LAS v4u*)(lds + cb + CV_U + 480 + ((half * NCHK + i) * 64 + lane) * 16) = uc[i];
    }
  };
  unsigned fw;

  if constexpr (G1R) {
    const int sb = 4 * (x >> 1) + (r & 3), fs = 6 * (x & 1) + (r >> 2);
    f32x16 acc1[2][2];
    const rsrc_t rsX = mkrs(p.q, S * D * 2), rsW = mkrs(p.w1, D * F * 2);
    unsigned vX[4], vW[4];
#pragma unroll
    for (int ii = 0; ii < 4; ++ii) {
      vX[ii] = (32 * wave + 8 * ii + (lane >> 3)) * 1280 + (((lane & 7) ^ (4 * (ii & 1) + g)) << 4) - 1024 * ii;
      vW[ii] = (16 * wave + 4 * ii + g) * 3072 + (((((c16 >> 1) ^ (g << 1)) << 1) | (c16 & 1)) << 4) - 1024 * ii;
    }
    const unsigned sX0 = sb * 128 * 1280, sW0 = fs * 256;
    const unsigned mX = wave * 4096, mW = 16384 + wave * 4096;
    const int rot = ROT ? (2 * (r >> 2) + 3 * (r & 3)) % 10 : 0;
    auto kslice = [&](int t) { const int k = t + rot; return k >= 10 ? k - 10 : k; };
    unsigned sq;
    auto grp_begin = [&](auto Q) { constexpr int q = Q.value, t = q >> 1;
      if constexpr (q < 20) { sq = (q & 1) ? sW0 + kslice(t) * (64 * 3072) : sX0 + kslice(t) * 128; setm0_add<(t % 5) * 32768>((q & 1) ? mW : mX); } };
    auto grp_dma = [&](auto Q, auto I) { constexpr int q = Q.value, i = I.value;
      if constexpr (q < 20) { if constexpr (q & 1) dma16<1024 * i, NT_W1>(vW[i], rsW, sq); else dma16<1024 * i, NT_X>(vX[i], rsX, sq); } };
    FT(10);
    static_for<0, Q0>([&](auto Q) { grp_begin(Q); asm volatile("s_nop 0"); static_for<0, 4>([&](auto I) { grp_dma(Q, I); }); });
    FT(11);
    const int ws_ = wave & 1, wf = wave >> 1, h = lane >> 5, r32 = lane & 31;
    unsigned aX[3][4], aW[3][2];
#pragma unroll
    for (int w = 0; w < 3; ++w) {
#pragma unroll
      for (int ks = 0; ks < 4; ++ks) aX[w][ks] = w * 65536 + (64 * ws_ + r32) * 128 + (((2 * ks + h) ^ ((r32 >> 1) & 7)) << 4);
#pragma unroll
      for (int ft = 0; ft < 2; ++ft) aW[w][ft] = w * 65536 + 16384 + (8 * h + q4) * 256 + (((2 * (2 * wf + ft) + (g & 1)) ^ (q4 << 1)) << 5) + 8 * p4;
    }
    v4u A[2][2][2], B[2][2][2];
    v2u At;
    auto rd = [&](auto T, auto HF, auto SET, auto N) {
      constexpr int t = T.value, hf = HF.value, set = SET.value, n = N.value;
      if constexpr (t < 10) {
        constexpr int b = t % 5, w = b >> 1, bo = (b & 1) * 32768;
        constexpr int kk = n / 6, rr = n % 6, ks = 2 * hf + kk;
        if constexpr (rr == 0) B[set][kk][0] = lds128(lds + aX[w][ks] + bo);
        else if constexpr (rr == 3) B[set][kk][1] = lds128(lds + aX[w][ks] + bo + 32 * 128);
        else { constexpr int ft = rr / 3, jj = (rr % 3) - 1;
          v2u xx = ldstr(lds + aW[w][ft] + bo + ks * 4096 + jj * 1024);
          if constexpr (jj == 0) At = xx; else A[set][kk][ft] = cat(At, xx); }
      }
    };

    unsigned fk;
    waitv<4 * (Q0 - 2)>(); sbar();
    STAMP(1);
    static_for<0, 12>([&](auto N) { rd(IC(0), IC(0), IC(0), N); });
    SCHED();
    static_for<0, 10>([&](auto T) {
      constexpr int t = T.value;
      static_for<0, 2>([&](auto HF) {
        constexpr int hf = HF.value, hi = 2 * t + hf, q = Q0 + hi;
        static_for<0, 8>([&](auto M) {
          constexpr int m = M.value, kk = m >> 2, ft = (m >> 1) & 1, st = m & 1;
          if constexpr (t == 0 && hf == 0 && kk == 0) mfma32_z(acc1[ft][st], A[hf][kk][ft], B[hf][kk][st]);
          else mfma32_ip(acc1[ft][st], A[hf][kk][ft], B[hf][kk][st]);
          if constexpr (m < 6) {
            if constexpr (hf == 0) { rd(T, IC(1), IC(1), IC(2 * m)); rd(T, IC(1), IC(1), IC(2 * m + 1)); }
            else { rd(IC(t + 1), IC(0), IC(0), IC(2 * m)); rd(IC(t + 1), IC(0), IC(0), IC(2 * m + 1)); }
          }
          if constexpr (m == 0) grp_begin(IC(q));
          if constexpr (m & 1) grp_dma(IC(q), IC(m >> 1));
          SCHED();
        });
        if constexpr (hf == 0 && t < 9) {
          constexpr int lastq = (Q0 + 2 * t < 19) ? Q0 + 2 * t : 19;
          constexpr int younger = 4 * (lastq - (2 * t + 3) > 0 ? lastq - (2 * t + 3) : 0);
          waitv<younger>(); sbar(); SCHED();
        }
      });
      FT(t); SCHED();
    });
    asm volatile("s_nop 15" : "+v"(acc1[0][0]), "+v"(acc1[0][1]), "+v"(acc1[1][0]), "+v"(acc1[1][1]));
    sbar();
    STAMP(2);
    tag = mktag(epoch);
    ld_h();
    const unsigned vo = (g & 1) * (48 * 1024) + (c16 + 16 * h) * 16;
    static_for<0, 4>([&](auto Q) {
      constexpr int qd = Q.value, st = qd >> 1, ft = qd & 1;
      const f32x16 a = acc1[ft][st];
      unsigned o[8];
      static_for<0, 2>([&](auto G) {
        constexpr int gq = G.value;
        static_for<0, 4>([&](auto E) {
          constexpr int e = 8 * gq + 2 * E.value;
          const float r0 = a[e] * __builtin_amdgcn_rcpf(1.f + __builtin_amdgcn_exp2f(a[e] * -1.44269504088896f));
          const float r1 = a[e + 1] * __builtin_amdgcn_rcpf(1.f + __builtin_amdgcn_exp2f(a[e + 1] * -1.44269504088896f));
          o[e >> 1] = pk2(r0, r1);
        });
        SCHED();
      });
      const unsigned so = ((8 * sb + 4 * ws_ + 2 * st) * 48 + 4 * fs + 2 * wf + ft) * 1024;
      __builtin_amdgcn_raw_buffer_store_b128(v4u{o[0], o[1], o[4], o[5]}, rsH, vo, so, SC1);
      __builtin_amdgcn_raw_buffer_store_b128(v4u{o[2], o[3], o[6], o[7]}, rsH, vo + 512, so, SC1);
      SCHED();
      FT(32 + qd); SCHED();
    });
    ld32_sc1(fk, foKV, rsC); ld32_sc1(fw, foW, rsC);
    waitv<10>();
    fill_h();
    FT(54);
    waitv<0>();
    if (lane == 0) st32_sc1(tag, C_FH + ((sb * 2 + ws_) * 24 + fs * 2 + wf) * FS, rsC);
    FT(55);
    poll_finish(fk, foKV, rsC, tag);
    ld_u();
    FT(36);
    poll_finish(fw, foW, rsC, tag);
    FT(37);
    static_for<0, WPRE>([&](auto I) { ldW(I); });
    STAMP(3);
    waitv<WPRE>();
    fill_u();
    waitl<0>(); sbar();
    STAMP(4);
  } else {
    const int j = (r - 24) * 8 + x, rbk = j >> 1, cg0 = 5 * (j & 1);
    const rsrc_t rsK = mkrs(p.k, S * D * 2), rsV = mkrs(p.v, S * D * 2), rsS = mkrs(p.sk, D * 8), rsO = mkrs(p.ws + WS_KVT, D * S * 2);
    const unsigned vr = (lane >> 3) * 1280 + ((lane & 7) << 4), vr4 = vr ^ 64;
    auto dmaK = [&](int cgl, int G) {
      const int row = 64 * rbk - 8 + 8 * G;
      dma16s(((G >> 1) & 1) ? vr4 : vr, rsK, row >= 0 ? (unsigned)(row * 1280 + (cg0 + cgl) * 128) : 0x80000000u, KV_BASE + cgl * KV_CG + G * KV_GS);
    };
    auto dmaV = [&](int cgl, int G) {
      dma16s(((G >> 1) & 1) ? vr4 : vr, rsV, (64 * rbk + 8 * G) * 1280 + (cg0 + cgl) * 128, KV_BASE + cgl * KV_CG + KV_V + G * KV_GS);
    };
#pragma unroll
    for (int G = 0; G < 9; ++G) dmaK(wave, G);
#pragma unroll
    for (int G = 0; G < 8; ++G) dmaV(wave, G);
    const unsigned pw = PW_BASE + wave * PW_SZ;
    dma16s(lane < 32 ? (unsigned)((cg0 + wave) * 512 + lane * 16) : (lane < 40 ? (unsigned)((64 * (cg0 + 4) + 16 * wave) * 8 + (lane - 32) * 16) : 0x80000000u), rsS, 0, pw);
    dmaK(4, wave); dmaK(4, wave + 4); dmaV(4, wave); dmaV(4, wave + 4);
    if (wave == 0) dmaK(4, 8);
    const int cc = lane >> 3, rg = lane & 7;
    FT(10);
    waitv<4>();
    STAMP(1);
    {
      const unsigned kb = KV_BASE + wave * KV_CG;
      const unsigned a0 = kb + rg * KV_GS + ((cc ^ (4 * ((rg >> 1) & 1))) << 4), a1 = kb + (rg + 1) * KV_GS + ((cc ^ (4 * (((rg + 1) >> 1) & 1))) << 4);
      v4u kk[11], vv[8], ww[4];
#pragma unroll
      for (int i = 0; i < 4; ++i) ww[i] = lds128(lds + pw + cc * 64 + i * 16);
#pragma unroll
      for (int i = 0; i < 11; ++i) kk[i] = i < 3 ? lds128(lds + a0 + (5 + i) * 128) : lds128(lds + a1 + (i - 3) * 128);
#pragma unroll
      for (int i = 0; i < 8; ++i) vv[i] = lds128(lds + a0 + KV_V + i * 128);
      const unsigned ob = (64 * (cg0 + wave) + 8 * cc) * 4096 + (64 * rbk + 8 * rg) * 2;
      kv_math<8>(kk, vv, ww, [&](int cp, const f32x2 (&rr)[8]) {
#pragma unroll
        for (int c = 0; c < 2; ++c) {
          v4u o{pk2(rr[0][c], rr[1][c]), pk2(rr[2][c], rr[3][c]), pk2(rr[4][c], rr[5][c]), pk2(rr[6][c], rr[7][c])};
          __builtin_amdgcn_raw_buffer_store_b128(o, rsO, ob + (2 * cp + c) * 4096, 0, SC1);
        }
      });
    }
    const int id = 4 * j + wave, pk = id / 5, blk = id % 5;
    const rsrc_t rsWs = mkrs(p.w2, F * D * 2);
    const unsigned ldsb = wave * 8192;
#pragma unroll
    for (int i = 0; i < 8; ++i) {
      const int row = 4 * i + g;
      dma16s((32 * pk + row) * 1280 + blk * 256 + (((((c16 >> 1) ^ (row & 7)) << 1) | (c16 & 1)) << 4), rsWs, id < 240 ? 0u : 0x80000000u, ldsb + i * 1024);
    }
    waitv<16>(); sbar();
    STAMP(2);
    {
      const int cc4 = 2 * wave + (lane >> 5), rp = lane & 31;
      const unsigned kb = KV_BASE + 4 * KV_CG;
      v4u kk[5], vv[2], w4[4];
#pragma unroll
      for (int i = 0; i < 4; ++i) w4[i] = lds128(lds + pw + 512 + (lane >> 5) * 64 + i * 16);
#pragma unroll
      for (int i = 0; i < 5; ++i) { const int row = 2 * rp + 5 + i; kk[i] = lds128(lds + kb + (row >> 3) * KV_GS + (row & 7) * 128 + ((cc4 ^ (4 * ((row >> 4) & 1))) << 4)); }
#pragma unroll
      for (int i = 0; i < 2; ++i) { const int row = 2 * rp + i; vv[i] = lds128(lds + kb + KV_V + (row >> 3) * KV_GS + (row & 7) * 128 + ((cc4 ^ (4 * ((row >> 4) & 1))) << 4)); }
      const unsigned ob = (64 * (cg0 + 4) + 8 * cc4) * 4096 + (64 * rbk + 2 * rp) * 2;
      kv_math<2>(kk, vv, w4, [&](int cp, const f32x2 (&rr)[2]) {
#pragma unroll
        for (int c = 0; c < 2; ++c) __builtin_amdgcn_raw_buffer_store_b32(pk2(rr[0][c], rr[1][c]), rsO, ob + (2 * cp + c) * 4096, 0, SC1);
      });
    }
    FT(11);
    waitv<8>();
    {
      const rsrc_t rsWo = mkrs(p.ws + WS_W2T, 8 * 48 * 5 * 1024);
      const int r0 = 4 * g + q4;
#pragma unroll
      for (int i = 0; i < 8; ++i) {
        const unsigned a = ldsb + r0 * 256 + ((i ^ (r0 & 7)) << 5) + 8 * p4;
        const v4u fr = cat(ldstr(lds + a), ldstr(lds + a + 4096));
        const int ctg = 8 * blk + i, ntt = ctg / 5, ct = ctg % 5;
        __builtin_amdgcn_raw_buffer_store_b128(fr, rsWo, vl, id < 240 ? (unsigned)(((ntt * 48 + pk) * 5 + ct) * 1024) : 0x80000000u, SC1);
      }
    }
    FT(0);
    waitv<8>();
    waitl<0>(); sbar();
    tag = mktag(epoch);
    if (threadIdx.x == 0) st32_sc1(tag, C_FKV + j * FS, rsC);
    STAMP(3);
    ld_h();
    waitv<5>(); sbar();
    if (threadIdx.x == 0) st32_sc1(tag, C_FW + j * FS, rsC);
    unsigned fk;
    ld32_sc1(fk, foKV, rsC);
    FT(3);
    waitv<1>();
    waitv<0>();
    fill_h();
    FT(2);
    poll_finish(fk, foKV, rsC, tag);
    FT(4);
    ld_u();
    const rsrc_t rsX = mkrs(p.q, S * D * 2), rsG = mkrs(p.gw, D * 2), rsGo = mkrs(p.ws + WS_G, S * 4);
    const int grow = 256 * x + 32 * (r - 24) + 8 * wave + (lane >> 3), part = lane & 7;
    v4u xx[10];
    dma16s(lane * 16, rsG, 0, pw + 1024); dma16s(1024 + lane * 16, rsG, 0, pw + 2048);
#pragma unroll
    for (int i = 0; i < 10; ++i) ldV<0>(xx[i], grow * 1280 + i * 128 + part * 16, rsX, 0);
    waitv<12>();
    fill_u();
    ld32_sc1(fw, foW, rsC);
    waitv<1>();
    {
      float a0 = 0.f, a1 = 0.f;
#pragma unroll
      for (int i = 0; i < 10; ++i) {
        asm volatile("" : "+v"(xx[i]));
        const v4u gg = lds128(lds + pw + 1024 + (i * 8 + part) * 16);
        a0 = dot2(xx[i].x, gg.x, a0); a1 = dot2(xx[i].y, gg.y, a1); a0 = dot2(xx[i].z, gg.z, a0); a1 = dot2(xx[i].w, gg.w, a1);
      }
      float a = a0 + a1;
      a += __shfl_xor(a, 1); a += __shfl_xor(a, 2); a += __shfl_xor(a, 4);
      const float gt = __builtin_amdgcn_rcpf(1.f + __builtin_amdgcn_exp2f(a * -1.44269504088896f));
      if (part == 0) __builtin_amdgcn_raw_buffer_store_b32(__builtin_bit_cast(unsigned, gt), rsGo, grow * 4, 0, SC1);
    }
    FT(5);
    waitl<0>();
    STAMP(4);
  }
  STAMP(5);
  constexpr int NZ = G1R ? 4 : 8;
  constexpr int W0 = G1R ? WPRE : 0, WG0 = G1R ? 0 : 14;
  auto wcount = [](int gap) constexpr { return gap < WG0 ? 0 : (G1R ? ((gap - WG0) * (60 - WPRE)) / 140 + 1 : (gap - WG0) / 4 + 1); };
  const unsigned foH = C_FH + (((rb >> 1) * 2 + (rb & 1)) * 24 + (lane < 24 ? lane : 0)) * FS;
  unsigned fh;
  constexpr int GPOLL = NZ * 36 - 12;
  f32x4 acc[8];
  {
    const unsigned aA = cb + q4 * CV_HC + 56 - 16 * g + 8 * p4 + 256 * half;
    const unsigned aB = cb + CV_U + 448 + 32 * c16 + 16 * g - 256 * half - 64 * (NZ - 1);
    v4u A[8], B[8];
#pragma unroll
    for (int i = 0; i < 8; ++i) {
      B[7 - i] = lds128(lds + aB + 64 * (NZ - 1) + 512 * (7 - i));
      A[i] = cat(ldstr(lds + aA + 512 * i + 8), ldstr(lds + aA + 512 * i));
    }
    SCHED();
    static_for<0, NZ>([&](auto Z) {
      constexpr int z = Z.value;
      static_for<0, 8>([&](auto I) {
        constexpr int i = I.value;
        static_for<0, 8 - i>([&](auto DD) {
          constexpr int d = 7 - i - DD.value;
          constexpr int gap = z * 36 + (i * (17 - i)) / 2 + DD.value;
          if constexpr (z == 0 && i == 0) mfma_z(acc[i + d], A[i], B[d]); else mfma_ip(acc[i + d], A[i], B[d]);
          if constexpr (d == 7 - i && z + 1 < NZ) B[7 - i] = lds128(lds + aB + 64 * (NZ - 2 - z) + 512 * (7 - i));
          if constexpr (!G1R && gap == WG0 - 2) { waitv<0>(); poll_finish(fw, foW, rsC, tag); }
          if constexpr (wcount(gap) > wcount(gap - 1)) ldW(IC(W0 + wcount(gap) - 1));
          if constexpr (gap == GPOLL) ld32_sc1(fh, foH, rsC);
          SCHED();
        });
        if constexpr (i == 7) { FT(40 + z); SCHED(); }
        if constexpr (z + 1 < NZ) { A[i] = cat(ldstr(lds + aA + 64 * (z + 1) + 512 * i + 8), ldstr(lds + aA + 64 * (z + 1) + 512 * i)); SCHED(); }
      });
    });
  }
  static_assert(W0 + wcount(NZ * 36 - 1) >= 60, "all W2 fragments issued");
  STAMP(6);
  constexpr int WAFT = 60 - (W0 + wcount(GPOLL));
  waitv<(WAFT > 0 ? WAFT : 0)>();
  FT(48);
  poll_finish(fh, foH, rsC, tag);
  FT(49);
  v4u Hf[HWM];
  const unsigned sH = (4 * rb * 48 + 12 * wave) * 1024;
  auto ldH = [&](auto N) { constexpr int n = N.value; if constexpr (n < 48) ldVo<(n & 3) * 1024, SC1>(Hf[n % HWM], vl, rsH, sH + ((n / 12) * 48 + ((n % 12) & ~3)) * 1024); };
  static_for<0, HW0>([&](auto I) { ldH(I); });
  FT(14);
  asm volatile("s_nop 7" : "+v"(acc[0]), "+v"(acc[1]), "+v"(acc[2]), "+v"(acc[3]), "+v"(acc[4]), "+v"(acc[5]), "+v"(acc[6]), "+v"(acc[7]));
  SCHED();
  int ctb = 0;
  if constexpr (G1R) {
    LAS char* ex = lds + CV_EX + cl * 8192;
    f32x4 keep[4];
#pragma unroll
    for (int i = 0; i < 4; ++i) {
      f32x4 lo = acc[i], hi = acc[4 + i];
      asm volatile("" : "+v"(lo), "+v"(hi));
      *(LAS f32x4*)(ex + half * 4096 + i * 1024 + lane * 16) = half == 0 ? hi : lo;
      keep[i] = half == 0 ? lo : hi;
    }
    waitl<0>(); sbar();
#pragma unroll
    for (int i = 0; i < 4; ++i) acc[i] = keep[i] + *(LAS f32x4*)(ex + (1 - half) * 4096 + i * 1024 + lane * 16);
    ctb = 4 * half;
  }
  FT(51);
  constexpr int NT = G1R ? 4 : 8, NYS = NT / 2;
  {
    asm volatile("" : "+v"(dv));
    const float Dc = blo(dv);
#pragma unroll
    for (int i = 0; i < NT; i += 2) {
      unsigned w0[2][2];
#pragma unroll
      for (int e = 0; e < 2; ++e) {
        const int ct = ctb + i + e;
        const f32x4 a = acc[i + e];
        const v2u uu = lds64(lds + cb + CV_U + 480 + 512 * ct + 32 * c16 + 8 * g);
        w0[e][0] = pk2(fmaf(blo(uu.x), Dc, a[0]), fmaf(bhi(uu.x), Dc, a[1]));
        w0[e][1] = pk2(fmaf(blo(uu.y), Dc, a[2]), fmaf(bhi(uu.y), Dc, a[3]));
      }
      auto s0 = __builtin_amdgcn_permlane16_swap(w0[0][0], w0[1][0], false, false);
      auto s1 = __builtin_amdgcn_permlane16_swap(w0[0][1], w0[1][1], false, false);
      const int ct = ctb + i + (g & 1);
      __builtin_amdgcn_raw_buffer_store_b128(v4u{s0[0], s1[0], s0[1], s1[1]}, rsY, ch * 4096 + 512 * ct + 32 * c16 + 8 * (g & 2), 0, SC1);
    }
  }
  FT(52);
  waitl<0>(); sbar();
  STAMP(7);
  f32x4 ac2[2][5];
  const unsigned foY0 = C_FY + (nt * 128 + lane) * FS, foY1 = foY0 + 64 * FS;
  const unsigned foG = C_FY + ((rb >> 2) * 128 + 96 + (lane & 31)) * FS;
  unsigned fy0, fy1, fg, gtu;
  v4u yv[3]; v2u qq[5];
  const int s = 64 * rb + 16 * wave + c16;
#define SF_ 5
  constexpr int SF = SF_;
  constexpr int SP = 25;
  constexpr int SY = 40;
  static_assert(g2_issued(SP) == 48 && SY >= SP + 8, "");
  auto extras_at = [](int st) constexpr { return (st == SF ? 1 : 0) + ((st >= SP && st < SP + 8) ? 1 : 0) + (st == SY ? 4 : 0); };
  static_for<0, 48>([&](auto I) {
    constexpr int i = I.value, rt = i / 12, k = i % 12, set = rt & 1;
    constexpr int younger = [&]() constexpr {
      int y = g2_issued(i) - (i + 1);
      if (i < HW0) y += NYS;
      int si = -1; while (i >= HW0 && g2_issued(si + 1) <= i) ++si;
      for (int st = (si < 0 ? 0 : si); st < i; ++st) y += extras_at(st);
      return y; }();
    waitv<(younger < 63 ? younger : 63)>();
    static_for<0, 5>([&](auto C) { constexpr int ct = C.value;
      if constexpr (k == 0) mfma_zA(ac2[set][ct], W[k][ct], Hf[i % HWM]); else mfma_ipA(ac2[set][ct], W[k][ct], Hf[i % HWM]);
      if constexpr (rt > 0 && k >= 1 && k <= 5 && ct == 3) *(LAS f32x4*)(lds + G2_PART + (((rt - 1) * 4 + wave) * 5 + (k - 1)) * 1024 + lane * 16) = ac2[set ^ 1][k - 1];
      if constexpr (ct == 1) { static_for<g2_issued(i), g2_issued(i + 1)>([&](auto N) { ldH(N); }); }
    });
    if constexpr (i == SF) {
      waitv<g2_issued(SF + 1) - HW0>();
      if (lane == 0) st32_sc1(tag, C_FY + ((x * 32 + r) * 4 + wave) * FS, rsC);
    }
    if constexpr (i == SP) ld32_sc1(fy0, foY0, rsC);
    if constexpr (i == SP + 1) ld32_sc1(fy1, foY1, rsC);
    if constexpr (i == SP + 2) ld32_sc1(fg, foG, rsC);
    if constexpr (i >= SP + 3 && i < SP + 8) { const rsrc_t rsQ = mkrs(p.q, S * D * 2); ldV2<AUXP>(qq[i - SP - 3], (s * D + 80 * nt + 16 * (i - SP - 3) + 4 * g) * 2, rsQ, 0); }
    if constexpr (i == SY) {
      waitv<5>();
      poll_finish2(fy0, foY0, fy1, foY1, rsC, tag);
      poll_finish(fg, foG, rsC, tag);
      { const rsrc_t rsG = mkrs(p.ws + WS_G, S * 4); ld32_sc1(gtu, s * 4, rsG); }
#pragma unroll
      for (int u = 0; u < 3; ++u) {
        const int idx = u * 256 + wave * 64 + lane, d = idx >> 3, cch = idx & 7;
        ldV<SC1>(yv[u], idx < 640 ? (unsigned)((80 * nt + d) * 4096 + rb * 128 + cch * 16) : 0x80000000u, rsY, 0);
      }
    }
    if constexpr (i % 4 == 3) { FT(16 + i / 4); }
    SCHED();
  });
  STAMP(9);
  asm volatile("s_nop 7" : "+v"(ac2[1][0]), "+v"(ac2[1][1]), "+v"(ac2[1][2]), "+v"(ac2[1][3]), "+v"(ac2[1][4]));
#pragma unroll
  for (int ct = 0; ct < 5; ++ct) *(LAS f32x4*)(lds + G2_PART + ((3 * 4 + wave) * 5 + ct) * 1024 + lane * 16) = ac2[1][ct];
  unsigned fe[16];
  const bool bump = blockIdx.x == 0 && wave == 0;
  if (bump) {
#pragma unroll
    for (int i = 0; i < 16; ++i) ld32_sc1(fe[i], C_FY + (i * 64 + lane) * FS, rsC);
  }
  waitv<0>();
#pragma unroll
  for (int u = 0; u < 3; ++u) {
    asm volatile("" : "+v"(yv[u]));
    const int idx = u * 256 + wave * 64 + lane, d = idx >> 3, cch = idx & 7;
    if (idx < 640) { *(LAS v2u*)(lds + G2_Y + d * 136 + cch * 16) = v2u{yv[u].x, yv[u].y}; *(LAS v2u*)(lds + G2_Y + d * 136 + cch * 16 + 8) = v2u{yv[u].z, yv[u].w}; }
  }
  asm volatile("" : "+v"(gtu));
#pragma unroll
  for (int ct = 0; ct < 5; ++ct) asm volatile("" : "+v"(qq[ct]));
  waitl<0>(); sbar();
  STAMP(10);
  const float gt = __builtin_bit_cast(float, gtu);
#pragma unroll
  for (int ct = 0; ct < 5; ++ct) {
    f32x4 b = *(LAS f32x4*)(lds + G2_PART + ((wave * 4 + 0) * 5 + ct) * 1024 + lane * 16);
#pragma unroll
    for (int w = 1; w < 4; ++w) b += *(LAS f32x4*)(lds + G2_PART + ((wave * 4 + w) * 5 + ct) * 1024 + lane * 16);
    const v2u yy = ldstr(lds + G2_Y + (16 * ct + 4 * g + q4) * 136 + (16 * wave + 4 * p4) * 2);
    const float o0 = gt * (blo(qq[ct].x) * blo(yy.x)) + (1.f - gt) * b[0];
    const float o1 = gt * (bhi(qq[ct].x) * bhi(yy.x)) + (1.f - gt) * b[1];
    const float o2 = gt * (blo(qq[ct].y) * blo(yy.y)) + (1.f - gt) * b[2];
    const float o3 = gt * (bhi(qq[ct].y) * bhi(yy.y)) + (1.f - gt) * b[3];
    *(GAS v2u*)(p.out + s * D + 80 * nt + 16 * ct + 4 * g) = v2u{pk2(o0, o1), pk2(o2, o3)};
  }
  if (bump) {
    unsigned bad = 0;
#pragma unroll
    for (int e = 0; e < 16; ++e) { asm volatile("" : "+v"(fe[e])); bad |= fe[e] ^ tag; }
    if (__builtin_amdgcn_ballot_w64(bad != 0) != 0) {
#pragma unroll 1
      for (int e = 0; e < 16; ++e) { const unsigned fo = C_FY + (e * 64 + lane) * FS; unsigned f; ld32_sc1(f, fo, rsC); waitv<0>(); poll_finish(f, fo, rsC, tag); }
    }
    if (lane == 0) st32_sc1(tag, C_EPOCH, rsC);
  }
  STAMP(11);
  STAMP_FLUSH(); FT_FLUSH();
}

__global__ __launch_bounds__(256, 1) void fused(Params p) {
  extern __shared__ __attribute__((aligned(16))) char smem[];
  LAS char* lds = (LAS char*)smem;
  const int lane = threadIdx.x & 63, wave = __builtin_amdgcn_readfirstlane(threadIdx.x >> 6);
  const int x = blockIdx.x & 7, rp_ = blockIdx.x >> 3, r = PREP_FIRST ? (rp_ < 8 ? rp_ + 24 : rp_ - 8) : rp_;
  unsigned pf = 0;
  {
    unsigned long long pc; unsigned len;
    asm volatile("s_getpc_b64 %0\nLpf_here:\n\ts_mov_b32 %1, Lpf_end-Lpf_here" : "=s"(pc), "=s"(len));
    const unsigned off = (rp_ * 4 + wave) * 1024 + lane * 16;
    const rsrc_t rs = mkrs((const void*)pc, len);
    v4u t;
    asm volatile("buffer_load_dwordx4 %0, %1, %2, 0 offen" : "=v"(t) : "v"(off), "s"(rs));
    if (r < 24) { body<true>(p, lds, x, r, wave, lane); }
    else { body<false>(p, lds, x, r, wave, lane); }
    asm volatile("s_waitcnt vmcnt(0)\nLpf_end:" : "+v"(t));
    pf = t.x;
  }
  asm volatile("" ::"v"(pf));
}

extern "C" void kernel_launch(void* const* d_in, const int* in_sizes, int n_in, void* d_out, int out_size, void* d_ws, size_t ws_size, hipStream_t stream) {
  Params p;
  p.q = (const u16*)d_in[0]; p.k = (const u16*)d_in[1]; p.v = (const u16*)d_in[2]; p.sk = (const u16*)d_in[3];
  p.h = (const u16*)d_in[4]; p.Dv = (const u16*)d_in[5]; p.w1 = (const u16*)d_in[7]; p.w2 = (const u16*)d_in[8]; p.gw = (const u16*)d_in[9];
  p.out = (u16*)d_out; p.ws = (char*)d_ws + ((ws_size - WS_END) & ~(size_t)0xFFFF);
  (void)hipFuncSetAttribute((const void*)fused, hipFuncAttributeMaxDynamicSharedMemorySize, LDS_BYTES);
  fused<<<256, 256, LDS_BYTES, stream>>>(p);
}
```

```cpp
#include <hip/hip_runtime.h>
#include <type_traits>
#define SPIN_SLEEP 4
#define FSTR 64
#include <stdint.h>

typedef unsigned short u16;
typedef unsigned v4u __attribute__((ext_vector_type(4)));
typedef unsigned v2u __attribute__((ext_vector_type(2)));
typedef float f32x4 __attribute__((ext_vector_type(4)));
typedef float f32x2 __attribute__((ext_vector_type(2)));
typedef __bf16 bf16x8 __attribute__((ext_vector_type(8)));
typedef __bf16 bf16x2 __attribute__((ext_vector_type(2)));
typedef short v4s __attribute__((ext_vector_type(4)));
typedef short v8s __attribute__((ext_vector_type(8)));
#define GAS __attribute__((address_space(1)))
#define LAS __attribute__((address_space(3)))
#define DEV __device__ __forceinline__
typedef __amdgpu_buffer_rsrc_t rsrc_t;

constexpr int S = 2048, D = 640, F = 1536;

template <int N> DEV void waitv() { asm volatile("s_waitcnt vmcnt(%0)" ::"i"(N) : "memory"); }
template <int N> DEV void waitl() { asm volatile("s_waitcnt lgkmcnt(%0)" ::"i"(N) : "memory"); }
DEV void sbar() { asm volatile("s_barrier" ::: "memory"); }
#define SCHED() __builtin_amdgcn_sched_barrier(0)
DEV rsrc_t mkrs(const void* p, unsigned bytes) { return __builtin_amdgcn_make_buffer_rsrc((void*)p, 0, bytes, 0x00020000); }
DEV f32x4 mfma(v4u a, v4u b, f32x4 c) {
  return __builtin_amdgcn_mfma_f32_16x16x32_bf16(__builtin_bit_cast(bf16x8, a), __builtin_bit_cast(bf16x8, b), c, 0, 0, 0);
}
DEV v4u lds128(LAS const char* p) { return *(LAS const v4u*)p; }
DEV v2u lds64(LAS const char* p) { return *(LAS const v2u*)p; }
DEV v2u ldstr(LAS const char* p) { return __builtin_bit_cast(v2u, __builtin_amdgcn_ds_read_tr16_b64_v4i16((LAS v4s*)p)); }
DEV v4u cat(v2u a, v2u b) { return v4u{a.x, a.y, b.x, b.y}; }
DEV unsigned pk2(float a, float b) { bf16x2 r = __builtin_convertvector(f32x2{a, b}, bf16x2); return __builtin_bit_cast(unsigned, r); }
DEV float blo(unsigned d) { return __builtin_bit_cast(float, d << 16); }
DEV float bhi(unsigned d) { return __builtin_bit_cast(float, d & 0xffff0000u); }
DEV void setm0(unsigned v) { asm volatile("s_mov_b32 m0, %0\n\ts_nop 0" ::"s"(v)); }
template <int OFF, int NT = 0> DEV void dma16(unsigned voff, rsrc_t rs, unsigned soff) {
  if constexpr (NT) asm volatile("buffer_load_dwordx4 %0, %1, %2 offen offset:%3 nt lds" ::"v"(voff), "s"(rs), "s"(soff), "i"(OFF));
  else asm volatile("buffer_load_dwordx4 %0, %1, %2 offen offset:%3 lds" ::"v"(voff), "s"(rs), "s"(soff), "i"(OFF));
}
typedef float f32x16 __attribute__((ext_vector_type(16)));
DEV f32x16 mfma32(v4u a, v4u b, f32x16 c) {
  return __builtin_amdgcn_mfma_f32_32x32x16_bf16(__builtin_bit_cast(bf16x8, a), __builtin_bit_cast(bf16x8, b), c, 0, 0, 0);
}
template <int IMM> DEV void setm0_add(unsigned base) { asm volatile("s_add_u32 m0, %0, %1" ::"s"(base), "i"(IMM) : "scc"); }
template <int I, int N, class Fn> DEV void static_for(Fn&& f) { if constexpr (I < N) { f(std::integral_constant<int, I>{}); static_for<I + 1, N>(f); } }
DEV void mfma_ip(f32x4& c, const v4u& a, const v4u& b) { asm volatile("v_mfma_f32_16x16x32_bf16 %0, %1, %2, %0" : "+v"(c) : "v"(a), "v"(b)); }
DEV void mfma_ipA(f32x4& c, const v4u& a, const v4u& b) { asm volatile("v_mfma_f32_16x16x32_bf16 %0, %1, %2, %0" : "+v"(c) : "a"(a), "v"(b)); }
DEV void mfma_z(f32x4& c, const v4u& a, const v4u& b) { asm volatile("v_mfma_f32_16x16x32_bf16 %0, %1, %2, 0" : "=&v"(c) : "v"(a), "v"(b)); }
DEV void mfma_zA(f32x4& c, const v4u& a, const v4u& b) { asm volatile("v_mfma_f32_16x16x32_bf16 %0, %1, %2, 0" : "=&v"(c) : "a"(a), "v"(b)); }
template <int AUX> DEV void ldA(v4u& d, unsigned voff, rsrc_t rs, unsigned soff) {
  if constexpr (AUX) asm volatile("buffer_load_dwordx4 %0, %1, %2, %3 offen sc1" : "=a"(d) : "v"(voff), "s"(rs), "s"(soff));
  else asm volatile("buffer_load_dwordx4 %0, %1, %2, %3 offen" : "=a"(d) : "v"(voff), "s"(rs), "s"(soff));
}
template <int AUX> DEV void ldV(v4u& d, unsigned voff, rsrc_t rs, unsigned soff) {
  if constexpr (AUX == 2) asm volatile("buffer_load_dwordx4 %0, %1, %2, %3 offen nt" : "=v"(d) : "v"(voff), "s"(rs), "s"(soff));
  else if constexpr (AUX == 16) asm volatile("buffer_load_dwordx4 %0, %1, %2, %3 offen sc1" : "=v"(d) : "v"(voff), "s"(rs), "s"(soff));
  else asm volatile("buffer_load_dwordx4 %0, %1, %2, %3 offen" : "=v"(d) : "v"(voff), "s"(rs), "s"(soff));
}
DEV void nop8() { asm volatile("s_nop 7"); }

constexpr size_t WS_CTRL = 0;
constexpr size_t WS_KVT = 262144;
constexpr size_t WS_YT = WS_KVT + (size_t)D * S * 2;
constexpr size_t WS_W2T = WS_YT + (size_t)D * S * 2;
constexpr size_t WS_HF = WS_W2T + (size_t)8 * 48 * 5 * 1024;
constexpr size_t WS_G = WS_HF + (size_t)128 * 48 * 1024;
constexpr size_t WS_END = WS_G + S * 4;
constexpr unsigned C_EPOCH = 0, C_TMO = 256;
constexpr unsigned FS = FSTR;
constexpr unsigned C_FW = 4096;
constexpr unsigned C_FKV = C_FW + 64 * FS;
constexpr unsigned C_FH = C_FKV + 64 * FS;
constexpr unsigned C_FY = C_FH + 768 * FS;
constexpr unsigned C_BYTES = 262144;
static_assert(C_FY + 1024 * FS <= C_BYTES, "");

#define NSTAMP 16
#define STAMP_OFF (size_t)(32u << 20)
#define STAMP_DECL
#define STAMP(i)
#define STAMP_FLUSH()
#define FT_DECL
#define FT(i)
#define FT_FLUSH()

struct Params {
  const u16 *q, *k, *v, *sk, *h, *Dv, *w1, *w2, *gw;
  u16* out;
  char* ws;
};

constexpr int LDS_BYTES = 160 * 1024;
constexpr int SC1 = 16;
#define NT_X 0
#define NT_W1 0
#define NT_P 0
constexpr int AUXP = NT_P ? 2 : 0;
#define IC(n) std::integral_constant<int, (n)>{}

DEV void ld32_sc1(unsigned& v, unsigned voff, rsrc_t rs) { asm volatile("buffer_load_dword %0, %1, %2, 0 offen sc1" : "=v"(v) : "v"(voff), "s"(rs)); }
DEV void st32_sc1(unsigned v, unsigned voff, rsrc_t rs) { asm volatile("buffer_store_dword %0, %1, %2, 0 offen sc1" ::"v"(v), "v"(voff), "s"(rs)); }
DEV void poll_finish(unsigned v, unsigned voff, rsrc_t rsC, unsigned tag) {
  asm volatile("" : "+v"(v));
  if (__builtin_amdgcn_ballot_w64(v != tag) == 0) return;
#pragma unroll 1
  for (int spins = 0; spins < (1 << 15); ++spins) {
    __builtin_amdgcn_s_sleep(SPIN_SLEEP);
    ld32_sc1(v, voff, rsC);
    asm volatile("s_waitcnt vmcnt(0)" : "+v"(v)::"memory");
    if (__builtin_amdgcn_ballot_w64(v != tag) == 0) return;
  }
  st32_sc1(1u, C_TMO, rsC);
}

DEV void poll_finish2(unsigned a, unsigned oa, unsigned b, unsigned ob, rsrc_t rsC, unsigned tag) {
  asm volatile("" : "+v"(a), "+v"(b));
  if (__builtin_amdgcn_ballot_w64(a != tag || b != tag) == 0) return;
#pragma unroll 1
  for (int spins = 0; spins < (1 << 15); ++spins) {
    __builtin_amdgcn_s_sleep(SPIN_SLEEP);
    ld32_sc1(a, oa, rsC); ld32_sc1(b, ob, rsC);
    asm volatile("s_waitcnt vmcnt(0)" : "+v"(a), "+v"(b)::"memory");
    if (__builtin_amdgcn_ballot_w64(a != tag || b != tag) == 0) return;
  }
  st32_sc1(1u, C_TMO, rsC);
}

DEV void mfma32_ip(f32x16& c, const v4u& a, const v4u& b) { asm volatile("v_mfma_f32_32x32x16_bf16 %0, %1, %2, %0" : "+v"(c) : "v"(a), "v"(b)); }
DEV void mfma32_z(f32x16& c, const v4u& a, const v4u& b) { asm volatile("v_mfma_f32_32x32x16_bf16 %0, %1, %2, 0" : "=&v"(c) : "v"(a), "v"(b)); }

constexpr int KV_BASE = 32768, KV_GS = 1152, KV_V = 9 * KV_GS, KV_CG = 17 * KV_GS;
constexpr int CV_BASE = 32768;
constexpr int CV_HC = 4160;
constexpr int CV_U = 4 * CV_HC;
constexpr int CV_CH = CV_U + 4608;
constexpr int CV_EX = CV_BASE + 4 * CV_CH;
constexpr int G2_PART = 0;
constexpr int G2_Y = 81920;
constexpr int HW = 24;

template <int AUX> DEV void ldV2(v2u& d, unsigned voff, rsrc_t rs, unsigned soff) {
  if constexpr (AUX == 2) asm volatile("buffer_load_dwordx2 %0, %1, %2, %3 offen nt" : "=v"(d) : "v"(voff), "s"(rs), "s"(soff));
  else if constexpr (AUX == 16) asm volatile("buffer_load_dwordx2 %0, %1, %2, %3 offen sc1" : "=v"(d) : "v"(voff), "s"(rs), "s"(soff));
  else asm volatile("buffer_load_dwordx2 %0, %1, %2, %3 offen" : "=v"(d) : "v"(voff), "s"(rs), "s"(soff));
}
DEV void dma16s(unsigned voff, rsrc_t rs, unsigned soff, unsigned m0v) {
  if constexpr (NT_P) asm volatile("s_mov_b32 m0, %3\n\ts_nop 0\n\tbuffer_load_dwordx4 %0, %1, %2 offen nt lds" ::"v"(voff), "s"(rs), "s"(soff), "s"(m0v));
  else asm volatile("s_mov_b32 m0, %3\n\ts_nop 0\n\tbuffer_load_dwordx4 %0, %1, %2 offen lds" ::"v"(voff), "s"(rs), "s"(soff), "s"(m0v));
}
DEV float dot2(unsigned a, unsigned b, float c) { return __builtin_amdgcn_fdot2_f32_bf16(__builtin_bit_cast(bf16x2, a), __builtin_bit_cast(bf16x2, b), c, false); }
DEV unsigned mktag(unsigned& epoch) {
  asm volatile("" : "+v"(epoch));
  return __builtin_amdgcn_readfirstlane(epoch * 0x9E3779B1u + 0x7F4A7C15u);
}

template <int NR, class ST> DEV void kv_math(const v4u (&kk)[NR + 3], const v4u (&vv)[NR], const v4u (&ww)[4], ST&& st) {
#pragma unroll
  for (int cp = 0; cp < 4; ++cp) {
    f32x2 w[4];
    w[0] = f32x2{blo(ww[cp].x), blo(ww[cp].z)}; w[1] = f32x2{bhi(ww[cp].x), bhi(ww[cp].z)};
    w[2] = f32x2{blo(ww[cp].y), blo(ww[cp].w)}; w[3] = f32x2{bhi(ww[cp].y), bhi(ww[cp].w)};
    f32x2 kf[NR + 3];
#pragma unroll
    for (int i = 0; i < NR + 3; ++i) kf[i] = f32x2{blo(kk[i][cp]), bhi(kk[i][cp])};
    f32x2 r[NR];
#pragma unroll
    for (int i = 0; i < NR; ++i) {
      f32x2 a = kf[i + 3] * w[0];
      a = __builtin_elementwise_fma(kf[i + 2], w[1], a);
      a = __builtin_elementwise_fma(kf[i + 1], w[2], a);
      a = __builtin_elementwise_fma(kf[i], w[3], a);
      r[i] = a * f32x2{blo(vv[i][cp]), bhi(vv[i][cp])};
    }
    st(cp, r);
  }
}

#define Q0 7
#define ROT 0
#define PREP_FIRST 0
#define HPUB 90
#define WPRE_ 16
constexpr int WPRE = WPRE_;
constexpr int HW0 = 16, HWM = 24;
constexpr int PW_BASE = 131072, PW_SZ = 3072;
constexpr int g2_issued(int i) { int r = HW0; for (int s = 0; s < i; ++s) r += (r - s < HWM) ? ((r + 1 - s < HWM) ? 2 : 1) : 0; return r < 48 ? r : 48; }

template <int OFF, int AUX> DEV void ldVo(v4u& d, unsigned voff, rsrc_t rs, unsigned soff) {
  static_assert(AUX == 16, "");
  asm volatile("buffer_load_dwordx4 %0, %1, %2, %3 offen offset:%4 sc1" : "=v"(d) : "v"(voff), "s"(rs), "s"(soff), "i"(OFF));
}
template <int OFF> DEV void ldAo(v4u& d, unsigned voff, rsrc_t rs, unsigned soff) {
  asm volatile("buffer_load_dwordx4 %0, %1, %2, %3 offen offset:%4 sc1" : "=a"(d) : "v"(voff), "s"(rs), "s"(soff), "i"(OFF));
}

template <bool G1R>
DEV void body(const Params& p, LAS char* lds, const int x, const int r, const int wave, const int lane) {
  const int g = lane >> 4, q4 = (lane >> 2) & 3, p4 = lane & 3, c16 = lane & 15;
  STAMP_DECL; FT_DECL;
  STAMP(0); FT(13);
  const rsrc_t rsC = mkrs(p.ws + WS_CTRL, C_BYTES);
  unsigned epoch, tag;
  ld32_sc1(epoch, C_EPOCH, rsC);
  const int cl = G1R ? (wave >> 1) : wave, half = G1R ? (wave & 1) : 0;
  const int ch = (G1R ? 80 * x + 2 * r : 80 * x + 48 + 4 * (r - 24)) + cl;
  const unsigned cb = CV_BASE + cl * CV_CH;
  constexpr int NCHK = G1R ? 2 : 4;
  unsigned dv;
  { const rsrc_t rsD = mkrs(p.Dv, D * 2); asm volatile("buffer_load_ushort %0, %1, %2, 0 offen" : "=v"(dv) : "v"(ch * 2), "s"(rsD)); }
  const rsrc_t rsHh = mkrs(p.h, D * S * 2), rsU = mkrs(p.ws + WS_KVT, D * S * 2), rsY = mkrs(p.ws + WS_YT, D * S * 2);
  const rsrc_t rsW2 = mkrs(p.ws + WS_W2T, 8 * 48 * 5 * 1024), rsH = mkrs(p.ws + WS_HF, 128 * 48 * 1024);
#define MAP42 1
  const int rb = MAP42 ? 8 * (x >> 1) + (r & 7) : 4 * x + (r & 3), nt = MAP42 ? 4 * (x & 1) + (r >> 3) : r >> 2;
  const unsigned vl = lane * 16;
  const unsigned sW = (nt * 48 + 12 * wave) * 5 * 1024;
  const unsigned foW = C_FW + lane * FS, foKV = C_FKV + lane * FS;
  v4u W[12][5];
  auto ldW = [&](auto I) { constexpr int i = I.value; if constexpr (i >= 0 && i < 60) ldAo<(i & 3) * 1024>(W[i / 5][i % 5], vl, rsW2, sW + (i & ~3) * 1024); };
  v4u hc[NCHK], uc[NCHK]; v2u hp0;
  auto ld_h = [&]() {
    ldV2<AUXP>(hp0, half > 0 ? (unsigned)(ch * 4096 + half * NCHK * 1024 - 8) : 0x80000000u, rsHh, 0);
#pragma unroll
    for (int i = 0; i < NCHK; ++i) ldV<AUXP>(hc[i], ch * 4096 + ((half * NCHK + i) * 64 + lane) * 16, rsHh, 0);
  };
  auto ld_u = [&]() {
#pragma unroll
    for (int i = 0; i < NCHK; ++i) ldV<SC1>(uc[i], ch * 4096 + ((half * NCHK + i) * 64 + lane) * 16, rsU, 0);
  };
  auto fill_h = [&]() {
    asm volatile("" : "+v"(hp0));
#pragma unroll
    for (int i = 0; i < NCHK; ++i) asm volatile("" : "+v"(hc[i]));
    unsigned e0 = hp0.x, e1 = hp0.y;
#pragma unroll
    for (int i = 0; i < NCHK; ++i) {
      const int n = (half * NCHK + i) * 64 + lane;
      const v4u cur = hc[i];
      const v2u prv = v2u{(unsigned)__builtin_amdgcn_update_dpp((int)e0, (int)cur.z, 0x138, 0xf, 0xf, false), (unsigned)__builtin_amdgcn_update_dpp((int)e1, (int)cur.w, 0x138, 0xf, 0xf, false)};
      e0 = __builtin_amdgcn_readlane(cur.z, 63); e1 = __builtin_amdgcn_readlane(cur.w, 63);
      const unsigned om2 = __builtin_amdgcn_alignbit(prv.y, prv.x, 16), om1 = __builtin_amdgcn_alignbit(cur.x, prv.y, 16);
      const unsigned o0 = __builtin_amdgcn_alignbit(cur.y, cur.x, 16), o1 = __builtin_amdgcn_alignbit(cur.z, cur.y, 16), o2 = __builtin_amdgcn_alignbit(cur.w, cur.z, 16);
      LAS char* dst = lds + cb + (n + 2) * 16;
      *(LAS v4u*)(dst) = cur;
      *(LAS v4u*)(dst + CV_HC) = v4u{om1, o0, o1, o2};
      *(LAS v4u*)(dst + 2 * CV_HC) = v4u{prv.y, cur.x, cur.y, cur.z};
      *(LAS v4u*)(dst + 3 * CV_HC) = v4u{om2, om1, o0, o1};
    }
    if (half == 0) {
      if (lane < 8) *(LAS v4u*)(lds + cb + (lane >> 1) * CV_HC + (lane & 1) * 16) = v4u{0, 0, 0, 0};
      if (lane >= 32 && lane < 62) *(LAS v4u*)(lds + cb + CV_U + (lane - 32) * 16) = v4u{0, 0, 0, 0};
    }
  };
  auto fill_u = [&]() {
#pragma unroll
    for (int i = 0; i < NCHK; ++i) {
      asm volatile("" : "+v"(uc[i]));
      *(LAS v4u*)(lds + cb + CV_U + 480 + ((half * NCHK + i) * 64 + lane) * 16) = uc[i];
    }
  };
  unsigned fw;

  if constexpr (G1R) {
    const int sb = 4 * (x >> 1) + (r & 3), fs = 6 * (x & 1) + (r >> 2);
    f32x16 acc1[2][2];
    const rsrc_t rsX = mkrs(p.q, S * D * 2), rsW = mkrs(p.w1, D * F * 2);
    unsigned vX[4], vW[4];
#pragma unroll
    for (int ii = 0; ii < 4; ++ii) {
      vX[ii] = (32 * wave + 8 * ii + (lane >> 3)) * 1280 + (((lane & 7) ^ (4 * (ii & 1) + g)) << 4) - 1024 * ii;
      vW[ii] = (16 * wave + 4 * ii + g) * 3072 + (((((c16 >> 1) ^ (g << 1)) << 1) | (c16 & 1)) << 4) - 1024 * ii;
    }
    const unsigned sX0 = sb * 128 * 1280, sW0 = fs * 256;
    const unsigned mX = wave * 4096, mW = 16384 + wave * 4096;
    const int rot = ROT ? (2 * (r >> 2) + 3 * (r & 3)) % 10 : 0;
    auto kslice = [&](int t) { const int k = t + rot; return k >= 10 ? k - 10 : k; };
    unsigned sq;
    auto grp_begin = [&](auto Q) { constexpr int q = Q.value, t = q >> 1;
      if constexpr (q < 20) { sq = (q & 1) ? sW0 + kslice(t) * (64 * 3072) : sX0 + kslice(t) * 128; setm0_add<(t % 5) * 32768>((q & 1) ? mW : mX); } };
    auto grp_dma = [&](auto Q, auto I) { constexpr int q = Q.value, i = I.value;
      if constexpr (q < 20) { if constexpr (q & 1) dma16<1024 * i, NT_W1>(vW[i], rsW, sq); else dma16<1024 * i, NT_X>(vX[i], rsX, sq); } };
    FT(10);
    static_for<0, Q0>([&](auto Q) { grp_begin(Q); asm volatile("s_nop 0"); static_for<0, 4>([&](auto I) { grp_dma(Q, I); }); });
    FT(11);
    const int ws_ = wave & 1, wf = wave >> 1, h = lane >> 5, r32 = lane & 31;
    unsigned aX[3][4], aW[3][2];
#pragma unroll
    for (int w = 0; w < 3; ++w) {
#pragma unroll
      for (int ks = 0; ks < 4; ++ks) aX[w][ks] = w * 65536 + (64 * ws_ + r32) * 128 + (((2 * ks + h) ^ ((r32 >> 1) & 7)) << 4);
#pragma unroll
      for (int ft = 0; ft < 2; ++ft) aW[w][ft] = w * 65536 + 16384 + (8 * h + q4) * 256 + (((2 * (2 * wf + ft) + (g & 1)) ^ (q4 << 1)) << 5) + 8 * p4;
    }
    v4u A[2][2][2], B[2][2][2];
    v2u At;
    auto rd = [&](auto T, auto HF, auto SET, auto N) {
      constexpr int t = T.value, hf = HF.value, set = SET.value, n = N.value;
      if constexpr (t < 10) {
        constexpr int b = t % 5, w = b >> 1, bo = (b & 1) * 32768;
        constexpr int kk = n / 6, rr = n % 6, ks = 2 * hf + kk;
        if constexpr (rr == 0) B[set][kk][0] = lds128(lds + aX[w][ks] + bo);
        else if constexpr (rr == 3) B[set][kk][1] = lds128(lds + aX[w][ks] + bo + 32 * 128);
        else { constexpr int ft = rr / 3, jj = (rr % 3) - 1;
          v2u xx = ldstr(lds + aW[w][ft] + bo + ks * 4096 + jj * 1024);
          if constexpr (jj == 0) At = xx; else A[set][kk][ft] = cat(At, xx); }
      }
    };

    unsigned fk;
    waitv<4 * (Q0 - 2)>(); sbar();
    STAMP(1);
    static_for<0, 12>([&](auto N) { rd(IC(0), IC(0), IC(0), N); });
    SCHED();
    static_for<0, 10>([&](auto T) {
      constexpr int t = T.value;
      static_for<0, 2>([&](auto HF) {
        constexpr int hf = HF.value, hi = 2 * t + hf, q = Q0 + hi;
        static_for<0, 8>([&](auto M) {
          constexpr int m = M.value, kk = m >> 2, ft = (m >> 1) & 1, st = m & 1;
          if constexpr (t == 0 && hf == 0 && kk == 0) mfma32_z(acc1[ft][st], A[hf][kk][ft], B[hf][kk][st]);
          else mfma32_ip(acc1[ft][st], A[hf][kk][ft], B[hf][kk][st]);
          if constexpr (m < 6) {
            if constexpr (hf == 0) { rd(T, IC(1), IC(1), IC(2 * m)); rd(T, IC(1), IC(1), IC(2 * m + 1)); }
            else { rd(IC(t + 1), IC(0), IC(0), IC(2 * m)); rd(IC(t + 1), IC(0), IC(0), IC(2 * m + 1)); }
          }
          if constexpr (m == 0) grp_begin(IC(q));
          if constexpr (m & 1) grp_dma(IC(q), IC(m >> 1));
          SCHED();
        });
        if constexpr (hf == 0 && t < 9) {
          constexpr int lastq = (Q0 + 2 * t < 19) ? Q0 + 2 * t : 19;
          constexpr int younger = 4 * (lastq - (2 * t + 3) > 0 ? lastq - (2 * t + 3) : 0);
          waitv<younger>(); sbar(); SCHED();
        }
      });
      FT(t); SCHED();
    });
    asm volatile("s_nop 15" : "+v"(acc1[0][0]), "+v"(acc1[0][1]), "+v"(acc1[1][0]), "+v"(acc1[1][1]));
    sbar();
    STAMP(2);
    tag = mktag(epoch);
    ld_h();
    const unsigned vo = (g & 1) * (48 * 1024) + (c16 + 16 * h) * 16;
    static_for<0, 4>([&](auto Q) {
      constexpr int qd = Q.value, st = qd >> 1, ft = qd & 1;
      const f32x16 a = acc1[ft][st];
      unsigned o[8];
      static_for<0, 2>([&](auto G) {
        constexpr int gq = G.value;
        static_for<0, 4>([&](auto E) {
          constexpr int e = 8 * gq + 2 * E.value;
          const float r0 = a[e] * __builtin_amdgcn_rcpf(1.f + __builtin_amdgcn_exp2f(a[e] * -1.44269504088896f));
          const float r1 = a[e + 1] * __builtin_amdgcn_rcpf(1.f + __builtin_amdgcn_exp2f(a[e + 1] * -1.44269504088896f));
          o[e >> 1] = pk2(r0, r1);
        });
        SCHED();
      });
      const unsigned so = ((8 * sb + 4 * ws_ + 2 * st) * 48 + 4 * fs + 2 * wf + ft) * 1024;
      __builtin_amdgcn_raw_buffer_store_b128(v4u{o[0], o[1], o[4], o[5]}, rsH, vo, so, SC1);
      __builtin_amdgcn_raw_buffer_store_b128(v4u{o[2], o[3], o[6], o[7]}, rsH, vo + 512, so, SC1);
      SCHED();
      FT(32 + qd); SCHED();
    });
    ld32_sc1(fk, foKV, rsC); ld32_sc1(fw, foW, rsC);
    waitv<10>();
    fill_h();
    FT(54);
    waitv<0>();
    if (lane == 0) st32_sc1(tag, C_FH + ((sb * 2 + ws_) * 24 + fs * 2 + wf) * FS, rsC);
    FT(55);
    poll_finish(fk, foKV, rsC, tag);
    ld_u();
    FT(36);
    poll_finish(fw, foW, rsC, tag);
    FT(37);
    static_for<0, WPRE>([&](auto I) { ldW(I); });
    STAMP(3);
    waitv<WPRE>();
    fill_u();
    waitl<0>(); sbar();
    STAMP(4);
  } else {
    const int j = (r - 24) * 8 + x, rbk = j >> 1, cg0 = 5 * (j & 1);
    const rsrc_t rsK = mkrs(p.k, S * D * 2), rsV = mkrs(p.v, S * D * 2), rsS = mkrs(p.sk, D * 8), rsO = mkrs(p.ws + WS_KVT, D * S * 2);
    const unsigned vr = (lane >> 3) * 1280 + ((lane & 7) << 4), vr4 = vr ^ 64;
    auto dmaK = [&](int cgl, int G) {
      const int row = 64 * rbk - 8 + 8 * G;
      dma16s(((G >> 1) & 1) ? vr4 : vr, rsK, row >= 0 ? (unsigned)(row * 1280 + (cg0 + cgl) * 128) : 0x80000000u, KV_BASE + cgl * KV_CG + G * KV_GS);
    };
    auto dmaV = [&](int cgl, int G) {
      dma16s(((G >> 1) & 1) ? vr4 : vr, rsV, (64 * rbk + 8 * G) * 1280 + (cg0 + cgl) * 128, KV_BASE + cgl * KV_CG + KV_V + G * KV_GS);
    };
#pragma unroll
    for (int G = 0; G < 9; ++G) dmaK(wave, G);
#pragma unroll
    for (int G = 0; G < 8; ++G) dmaV(wave, G);
    const unsigned pw = PW_BASE + wave * PW_SZ;
    dma16s(lane < 32 ? (unsigned)((cg0 + wave) * 512 + lane * 16) : (lane < 40 ? (unsigned)((64 * (cg0 + 4) + 16 * wave) * 8 + (lane - 32) * 16) : 0x80000000u), rsS, 0, pw);
    dmaK(4, wave); dmaK(4, wave + 4); dmaV(4, wave); dmaV(4, wave + 4);
    if (wave == 0) dmaK(4, 8);
    const int cc = lane >> 3, rg = lane & 7;
    FT(10);
    waitv<4>();
    STAMP(1);
    {
      const unsigned kb = KV_BASE + wave * KV_CG;
      const unsigned a0 = kb + rg * KV_GS + ((cc ^ (4 * ((rg >> 1) & 1))) << 4), a1 = kb + (rg + 1) * KV_GS + ((cc ^ (4 * (((rg + 1) >> 1) & 1))) << 4);
      v4u kk[11], vv[8], ww[4];
#pragma unroll
      for (int i = 0; i < 4; ++i) ww[i] = lds128(lds + pw + cc * 64 + i * 16);
#pragma unroll
      for (int i = 0; i < 11; ++i) kk[i] = i < 3 ? lds128(lds + a0 + (5 + i) * 128) : lds128(lds + a1 + (i - 3) * 128);
#pragma unroll
      for (int i = 0; i < 8; ++i) vv[i] = lds128(lds + a0 + KV_V + i * 128);
      const unsigned ob = (64 * (cg0 + wave) + 8 * cc) * 4096 + (64 * rbk + 8 * rg) * 2;
      kv_math<8>(kk, vv, ww, [&](int cp, const f32x2 (&rr)[8]) {
#pragma unroll
        for (int c = 0; c < 2; ++c) {
          v4u o{pk2(rr[0][c], rr[1][c]), pk2(rr[2][c], rr[3][c]), pk2(rr[4][c], rr[5][c]), pk2(rr[6][c], rr[7][c])};
          __builtin_amdgcn_raw_buffer_store_b128(o, rsO, ob + (2 * cp + c) * 4096, 0, SC1);
        }
      });
    }
    const int id = 4 * j + wave, pk = id / 5, blk = id % 5;
    const rsrc_t rsWs = mkrs(p.w2, F * D * 2);
    const unsigned ldsb = wave * 8192;
#pragma unroll
    for (int i = 0; i < 8; ++i) {
      const int row = 4 * i + g;
      dma16s((32 * pk + row) * 1280 + blk * 256 + (((((c16 >> 1) ^ (row & 7)) << 1) | (c16 & 1)) << 4), rsWs, id < 240 ? 0u : 0x80000000u, ldsb + i * 1024);
    }
    waitv<16>(); sbar();
    STAMP(2);
    {
      const int cc4 = 2 * wave + (lane >> 5), rp = lane & 31;
      const unsigned kb = KV_BASE + 4 * KV_CG;
      v4u kk[5], vv[2], w4[4];
#pragma unroll
      for (int i = 0; i < 4; ++i) w4[i] = lds128(lds + pw + 512 + (lane >> 5) * 64 + i * 16);
#pragma unroll
      for (int i = 0; i < 5; ++i) { const int row = 2 * rp + 5 + i; kk[i] = lds128(lds + kb + (row >> 3) * KV_GS + (row & 7) * 128 + ((cc4 ^ (4 * ((row >> 4) & 1))) << 4)); }
#pragma unroll
      for (int i = 0; i < 2; ++i) { const int row = 2 * rp + i; vv[i] = lds128(lds + kb + KV_V + (row >> 3) * KV_GS + (row & 7) * 128 + ((cc4 ^ (4 * ((row >> 4) & 1))) << 4)); }
      const unsigned ob = (64 * (cg0 + 4) + 8 * cc4) * 4096 + (64 * rbk + 2 * rp) * 2;
      kv_math<2>(kk, vv, w4, [&](int cp, const f32x2 (&rr)[2]) {
#pragma unroll
        for (int c = 0; c < 2; ++c) __builtin_amdgcn_raw_buffer_store_b32(pk2(rr[0][c], rr[1][c]), rsO, ob + (2 * cp + c) * 4096, 0, SC1);
      });
    }
    FT(11);
    waitv<8>();
    {
      const rsrc_t rsWo = mkrs(p.ws + WS_W2T, 8 * 48 * 5 * 1024);
      const int r0 = 4 * g + q4;
#pragma unroll
      for (int i = 0; i < 8; ++i) {
        const unsigned a = ldsb + r0 * 256 + ((i ^ (r0 & 7)) << 5) + 8 * p4;
        const v4u fr = cat(ldstr(lds + a), ldstr(lds + a + 4096));
        const int ctg = 8 * blk + i, ntt = ctg / 5, ct = ctg % 5;
        __builtin_amdgcn_raw_buffer_store_b128(fr, rsWo, vl, id < 240 ? (unsigned)(((ntt * 48 + pk) * 5 + ct) * 1024) : 0x80000000u, SC1);
      }
    }
    FT(0);
    waitv<8>();
    waitl<0>(); sbar();
    tag = mktag(epoch);
    if (threadIdx.x == 0) st32_sc1(tag, C_FKV + j * FS, rsC);
    STAMP(3);
    ld_h();
    waitv<5>(); sbar();
    if (threadIdx.x == 0) st32_sc1(tag, C_FW + j * FS, rsC);
    unsigned fk;
    ld32_sc1(fk, foKV, rsC);
    FT(3);
    waitv<1>();
    waitv<0>();
    fill_h();
    FT(2);
    poll_finish(fk, foKV, rsC, tag);
    FT(4);
    ld_u();
    const rsrc_t rsX = mkrs(p.q, S * D * 2), rsG = mkrs(p.gw, D * 2), rsGo = mkrs(p.ws + WS_G, S * 4);
    const int grow = 256 * x + 32 * (r - 24) + 8 * wave + (lane >> 3), part = lane & 7;
    v4u xx[10];
    dma16s(lane * 16, rsG, 0, pw + 1024); dma16s(1024 + lane * 16, rsG, 0, pw + 2048);
#pragma unroll
    for (int i = 0; i < 10; ++i) ldV<0>(xx[i], grow * 1280 + i * 128 + part * 16, rsX, 0);
    waitv<12>();
    fill_u();
    ld32_sc1(fw, foW, rsC);
    waitv<1>();
    {
      float a0 = 0.f, a1 = 0.f;
#pragma unroll
      for (int i = 0; i < 10; ++i) {
        asm volatile("" : "+v"(xx[i]));
        const v4u gg = lds128(lds + pw + 1024 + (i * 8 + part) * 16);
        a0 = dot2(xx[i].x, gg.x, a0); a1 = dot2(xx[i].y, gg.y, a1); a0 = dot2(xx[i].z, gg.z, a0); a1 = dot2(xx[i].w, gg.w, a1);
      }
      float a = a0 + a1;
      a += __shfl_xor(a, 1); a += __shfl_xor(a, 2); a += __shfl_xor(a, 4);
      const float gt = __builtin_amdgcn_rcpf(1.f + __builtin_amdgcn_exp2f(a * -1.44269504088896f));
      if (part == 0) __builtin_amdgcn_raw_buffer_store_b32(__builtin_bit_cast(unsigned, gt), rsGo, grow * 4, 0, SC1);
    }
    FT(5);
    waitl<0>();
    STAMP(4);
  }
  STAMP(5);
  constexpr int NZ = G1R ? 4 : 8;
  constexpr int W0 = G1R ? WPRE : 0, WG0 = G1R ? 0 : 14;
  auto wcount = [](int gap) constexpr { return gap < WG0 ? 0 : (G1R ? ((gap - WG0) * (60 - WPRE)) / 140 + 1 : (gap - WG0) / 4 + 1); };
  const unsigned foH = C_FH + (((rb >> 1) * 2 + (rb & 1)) * 24 + (lane < 24 ? lane : 0)) * FS;
  unsigned fh;
  constexpr int GPOLL = NZ * 36 - 12;
  f32x4 acc[8];
  {
    const unsigned aA = cb + q4 * CV_HC + 56 - 16 * g + 8 * p4 + 256 * half;
    const unsigned aB = cb + CV_U + 448 + 32 * c16 + 16 * g - 256 * half - 64 * (NZ - 1);
    v4u A[8], B[8];
#pragma unroll
    for (int i = 0; i < 8; ++i) {
      B[7 - i] = lds128(lds + aB + 64 * (NZ - 1) + 512 * (7 - i));
      A[i] = cat(ldstr(lds + aA + 512 * i + 8), ldstr(lds + aA + 512 * i));
    }
    SCHED();
    static_for<0, NZ>([&](auto Z) {
      constexpr int z = Z.value;
      static_for<0, 8>([&](auto I) {
        constexpr int i = I.value;
        static_for<0, 8 - i>([&](auto DD) {
          constexpr int d = 7 - i - DD.value;
          constexpr int gap = z * 36 + (i * (17 - i)) / 2 + DD.value;
          if constexpr (z == 0 && i == 0) mfma_z(acc[i + d], A[i], B[d]); else mfma_ip(acc[i + d], A[i], B[d]);
          if constexpr (d == 7 - i && z + 1 < NZ) B[7 - i] = lds128(lds + aB + 64 * (NZ - 2 - z) + 512 * (7 - i));
          if constexpr (!G1R && gap == WG0 - 2) { waitv<0>(); poll_finish(fw, foW, rsC, tag); }
          if constexpr (wcount(gap) > wcount(gap - 1)) ldW(IC(W0 + wcount(gap) - 1));
          if constexpr (gap == GPOLL) ld32_sc1(fh, foH, rsC);
          SCHED();
        });
        if constexpr (i == 7) { FT(40 + z); SCHED(); }
        if constexpr (z + 1 < NZ) { A[i] = cat(ldstr(lds + aA + 64 * (z + 1) + 512 * i + 8), ldstr(lds + aA + 64 * (z + 1) + 512 * i)); SCHED(); }
      });
    });
  }
  static_assert(W0 + wcount(NZ * 36 - 1) >= 60, "all W2 fragments issued");
  STAMP(6);
  constexpr int WAFT = 60 - (W0 + wcount(GPOLL));
  waitv<(WAFT > 0 ? WAFT : 0)>();
  FT(48);
  poll_finish(fh, foH, rsC, tag);
  FT(49);
  v4u Hf[HWM];
  const unsigned sH = (4 * rb * 48 + 12 * wave) * 1024;
  auto ldH = [&](auto N) { constexpr int n = N.value; if constexpr (n < 48) ldVo<(n & 3) * 1024, SC1>(Hf[n % HWM], vl, rsH, sH + ((n / 12) * 48 + ((n % 12) & ~3)) * 1024); };
  static_for<0, HW0>([&](auto I) { ldH(I); });
  FT(14);
  asm volatile("s_nop 7" : "+v"(acc[0]), "+v"(acc[1]), "+v"(acc[2]), "+v"(acc[3]), "+v"(acc[4]), "+v"(acc[5]), "+v"(acc[6]), "+v"(acc[7]));
  SCHED();
  int ctb = 0;
  if constexpr (G1R) {
    LAS char* ex = lds + CV_EX + cl * 8192;
    f32x4 keep[4];
#pragma unroll
    for (int i = 0; i < 4; ++i) {
      f32x4 lo = acc[i], hi = acc[4 + i];
      asm volatile("" : "+v"(lo), "+v"(hi));
      *(LAS f32x4*)(ex + half * 4096 + i * 1024 + lane * 16) = half == 0 ? hi : lo;
      keep[i] = half == 0 ? lo : hi;
    }
    waitl<0>(); sbar();
#pragma unroll
    for (int i = 0; i < 4; ++i) acc[i] = keep[i] + *(LAS f32x4*)(ex + (1 - half) * 4096 + i * 1024 + lane * 16);
    ctb = 4 * half;
  }
  FT(51);
  constexpr int NT = G1R ? 4 : 8, NYS = NT / 2;
  {
    asm volatile("" : "+v"(dv));
    const float Dc = blo(dv);
#pragma unroll
    for (int i = 0; i < NT; i += 2) {
      unsigned w0[2][2];
#pragma unroll
      for (int e = 0; e < 2; ++e) {
        const int ct = ctb + i + e;
        const f32x4 a = acc[i + e];
        const v2u uu = lds64(lds + cb + CV_U + 480 + 512 * ct + 32 * c16 + 8 * g);
        w0[e][0] = pk2(fmaf(blo(uu.x), Dc, a[0]), fmaf(bhi(uu.x), Dc, a[1]));
        w0[e][1] = pk2(fmaf(blo(uu.y), Dc, a[2]), fmaf(bhi(uu.y), Dc, a[3]));
      }
      auto s0 = __builtin_amdgcn_permlane16_swap(w0[0][0], w0[1][0], false, false);
      auto s1 = __builtin_amdgcn_permlane16_swap(w0[0][1], w0[1][1], false, false);
      const int ct = ctb + i + (g & 1);
      __builtin_amdgcn_raw_buffer_store_b128(v4u{s0[0], s1[0], s0[1], s1[1]}, rsY, ch * 4096 + 512 * ct + 32 * c16 + 8 * (g & 2), 0, SC1);
    }
  }
  FT(52);
  waitl<0>(); sbar();
  STAMP(7);
  f32x4 ac2[2][5];
  const unsigned foY0 = C_FY + (nt * 128 + lane) * FS, foY1 = foY0 + 64 * FS;
  const unsigned foG = C_FY + ((rb >> 2) * 128 + 96 + (lane & 31)) * FS;
  unsigned fy0, fy1, fg, gtu;
  v4u yv[3]; v2u qq[5];
  const int s = 64 * rb + 16 * wave + c16;
#define SF_ 5
  constexpr int SF = SF_;
  constexpr int SP = 25;
  constexpr int SY = 40;
  static_assert(g2_issued(SP) == 48 && SY >= SP + 8, "");
  auto extras_at = [](int st) constexpr { return (st == SF ? 1 : 0) + ((st >= SP && st < SP + 8) ? 1 : 0) + (st == SY ? 4 : 0); };
  static_for<0, 48>([&](auto I) {
    constexpr int i = I.value, rt = i / 12, k = i % 12, set = rt & 1;
    constexpr int younger = [&]() constexpr {
      int y = g2_issued(i) - (i + 1);
      if (i < HW0) y += NYS;
      int si = -1; while (i >= HW0 && g2_issued(si + 1) <= i) ++si;
      for (int st = (si < 0 ? 0 : si); st < i; ++st) y += extras_at(st);
      return y; }();
    waitv<(younger < 63 ? younger : 63)>();
    static_for<0, 5>([&](auto C) { constexpr int ct = C.value;
      if constexpr (k == 0) mfma_zA(ac2[set][ct], W[k][ct], Hf[i % HWM]); else mfma_ipA(ac2[set][ct], W[k][ct], Hf[i % HWM]);
      if constexpr (rt > 0 && k >= 1 && k <= 5 && ct == 3) *(LAS f32x4*)(lds + G2_PART + (((rt - 1) * 4 + wave) * 5 + (k - 1)) * 1024 + lane * 16) = ac2[set ^ 1][k - 1];
      if constexpr (ct == 1) { static_for<g2_issued(i), g2_issued(i + 1)>([&](auto N) { ldH(N); }); }
    });
    if constexpr (i == SF) {
      waitv<g2_issued(SF + 1) - HW0>();
      if (lane == 0) st32_sc1(tag, C_FY + ((x * 32 + r) * 4 + wave) * FS, rsC);
    }
    if constexpr (i == SP) ld32_sc1(fy0, foY0, rsC);
    if constexpr (i == SP + 1) ld32_sc1(fy1, foY1, rsC);
    if constexpr (i == SP + 2) ld32_sc1(fg, foG, rsC);
    if constexpr (i >= SP + 3 && i < SP + 8) { const rsrc_t rsQ = mkrs(p.q, S * D * 2); ldV2<AUXP>(qq[i - SP - 3], (s * D + 80 * nt + 16 * (i - SP - 3) + 4 * g) * 2, rsQ, 0); }
    if constexpr (i == SY) {
      waitv<5>();
      poll_finish2(fy0, foY0, fy1, foY1, rsC, tag);
      poll_finish(fg, foG, rsC, tag);
      { const rsrc_t rsG = mkrs(p.ws + WS_G, S * 4); ld32_sc1(gtu, s * 4, rsG); }
#pragma unroll
      for (int u = 0; u < 3; ++u) {
        const int idx = u * 256 + wave * 64 + lane, d = idx >> 3, cch = idx & 7;
        ldV<SC1>(yv[u], idx < 640 ? (unsigned)((80 * nt + d) * 4096 + rb * 128 + cch * 16) : 0x80000000u, rsY, 0);
      }
    }
    if constexpr (i % 4 == 3) { FT(16 + i / 4); }
    SCHED();
  });
  STAMP(9);
  asm volatile("s_nop 7" : "+v"(ac2[1][0]), "+v"(ac2[1][1]), "+v"(ac2[1][2]), "+v"(ac2[1][3]), "+v"(ac2[1][4]));
#pragma unroll
  for (int ct = 0; ct < 5; ++ct) *(LAS f32x4*)(lds + G2_PART + ((3 * 4 + wave) * 5 + ct) * 1024 + lane * 16) = ac2[1][ct];
  unsigned fe[16];
  const bool bump = blockIdx.x == 0 && wave == 0;
  if (bump) {
#pragma unroll
    for (int i = 0; i < 16; ++i) ld32_sc1(fe[i], C_FY + (i * 64 + lane) * FS, rsC);
  }
  waitv<0>();
#pragma unroll
  for (int u = 0; u < 3; ++u) {
    asm volatile("" : "+v"(yv[u]));
    const int idx = u * 256 + wave * 64 + lane, d = idx >> 3, cch = idx & 7;
    if (idx < 640) { *(LAS v2u*)(lds + G2_Y + d * 136 + cch * 16) = v2u{yv[u].x, yv[u].y}; *(LAS v2u*)(lds + G2_Y + d * 136 + cch * 16 + 8) = v2u{yv[u].z, yv[u].w}; }
  }
  asm volatile("" : "+v"(gtu));
#pragma unroll
  for (int ct = 0; ct < 5; ++ct) asm volatile("" : "+v"(qq[ct]));
  waitl<0>(); sbar();
  STAMP(10);
  const float gt = __builtin_bit_cast(float, gtu);
#pragma unroll
  for (int ct = 0; ct < 5; ++ct) {
    f32x4 b = *(LAS f32x4*)(lds + G2_PART + ((wave * 4 + 0) * 5 + ct) * 1024 + lane * 16);
#pragma unroll
    for (int w = 1; w < 4; ++w) b += *(LAS f32x4*)(lds + G2_PART + ((wave * 4 + w) * 5 + ct) * 1024 + lane * 16);
    const v2u yy = ldstr(lds + G2_Y + (16 * ct + 4 * g + q4) * 136 + (16 * wave + 4 * p4) * 2);
    const float o0 = gt * (blo(qq[ct].x) * blo(yy.x)) + (1.f - gt) * b[0];
    const float o1 = gt * (bhi(qq[ct].x) * bhi(yy.x)) + (1.f - gt) * b[1];
    const float o2 = gt * (blo(qq[ct].y) * blo(yy.y)) + (1.f - gt) * b[2];
    const float o3 = gt * (bhi(qq[ct].y) * bhi(yy.y)) + (1.f - gt) * b[3];
    *(GAS v2u*)(p.out + s * D + 80 * nt + 16 * ct + 4 * g) = v2u{pk2(o0, o1), pk2(o2, o3)};
  }
  if (bump) {
    unsigned bad = 0;
#pragma unroll
    for (int e = 0; e < 16; ++e) { asm volatile("" : "+v"(fe[e])); bad |= fe[e] ^ tag; }
    if (__builtin_amdgcn_ballot_w64(bad != 0) != 0) {
#pragma unroll 1
      for (int e = 0; e < 16; ++e) { const unsigned fo = C_FY + (e * 64 + lane) * FS; unsigned f; ld32_sc1(f, fo, rsC); waitv<0>(); poll_finish(f, fo, rsC, tag); }
    }
    if (lane == 0) st32_sc1(tag, C_EPOCH, rsC);
  }
  STAMP(11);
  STAMP_FLUSH(); FT_FLUSH();
}

__global__ __launch_bounds__(256, 1) void fused(Params p) {
  unsigned long long e_r0, e_r1;
  asm volatile("s_memrealtime %0" : "=s"(e_r0));
  { const char* w_ = p.ws; asm volatile("s_waitcnt lgkmcnt(0)\n\ts_memrealtime %0\n\ts_waitcnt lgkmcnt(0)" : "=s"(e_r1) : "s"(w_), "s"(e_r0)); }
  extern __shared__ __attribute__((aligned(16))) char smem[];
  LAS char* lds = (LAS char*)smem;
  const int lane = threadIdx.x & 63, wave = __builtin_amdgcn_readfirstlane(threadIdx.x >> 6);
  const int x = blockIdx.x & 7, rp_ = blockIdx.x >> 3, r = PREP_FIRST ? (rp_ < 8 ? rp_ + 24 : rp_ - 8) : rp_;
  unsigned pf = 0;
  {
    unsigned long long pc; unsigned len;
    asm volatile("s_getpc_b64 %0\nLpf_here:\n\ts_mov_b32 %1, Lpf_end-Lpf_here" : "=s"(pc), "=s"(len));
    const unsigned off = (rp_ * 4 + wave) * 1024 + lane * 16;
    const rsrc_t rs = mkrs((const void*)pc, len);
    v4u t;
    asm volatile("buffer_load_dwordx4 %0, %1, %2, 0 offen" : "=v"(t) : "v"(off), "s"(rs));
    if (r < 24) { body<true>(p, lds, x, r, wave, lane); }
    else { body<false>(p, lds, x, r, wave, lane); }
    asm volatile("s_waitcnt vmcnt(0)\nLpf_end:" : "+v"(t));
    pf = t.x;
  }
  asm volatile("" ::"v"(pf));
  if (blockIdx.x == 5) { const unsigned long long d_ = (e_r1 - e_r0) * 0, e0 = __builtin_amdgcn_s_memrealtime(); while (__builtin_amdgcn_s_memrealtime() - e0 < d_) __builtin_amdgcn_s_sleep(1); }
}

extern "C" void kernel_launch(void* const* d_in, const int* in_sizes, int n_in, void* d_out, int out_size, void* d_ws, size_t ws_size, hipStream_t stream) {
  Params p;
  p.q = (const u16*)d_in[0]; p.k = (const u16*)d_in[1]; p.v = (const u16*)d_in[2]; p.sk = (const u16*)d_in[3];
  p.h = (const u16*)d_in[4]; p.Dv = (const u16*)d_in[5]; p.w1 = (const u16*)d_in[7]; p.w2 = (const u16*)d_in[8]; p.gw = (const u16*)d_in[9];
  p.out = (u16*)d_out; p.ws = (char*)d_ws;
  (void)hipFuncSetAttribute((const void*)fused, hipFuncAttributeMaxDynamicSharedMemorySize, LDS_BYTES);
  fused<<<256, 256, LDS_BYTES, stream>>>(p);
}
```

```cpp
#include <hip/hip_runtime.h>
#include <type_traits>
#define SPIN_SLEEP 4
#define FSTR 64
#include <stdint.h>

typedef unsigned short u16;
typedef unsigned v4u __attribute__((ext_vector_type(4)));
typedef unsigned v2u __attribute__((ext_vector_type(2)));
typedef float f32x4 __attribute__((ext_vector_type(4)));
typedef float f32x2 __attribute__((ext_vector_type(2)));
typedef __bf16 bf16x8 __attribute__((ext_vector_type(8)));
typedef __bf16 bf16x2 __attribute__((ext_vector_type(2)));
typedef short v4s __attribute__((ext_vector_type(4)));
typedef short v8s __attribute__((ext_vector_type(8)));
#define GAS __attribute__((address_space(1)))
#define LAS __attribute__((address_space(3)))
#define DEV __device__ __forceinline__
typedef __amdgpu_buffer_rsrc_t rsrc_t;

constexpr int S = 2048, D = 640, F = 1536;

template <int N> DEV void waitv() { asm volatile("s_waitcnt vmcnt(%0)" ::"i"(N) : "memory"); }
template <int N> DEV void waitl() { asm volatile("s_waitcnt lgkmcnt(%0)" ::"i"(N) : "memory"); }
DEV void sbar() { asm volatile("s_barrier" ::: "memory"); }
#define SCHED() __builtin_amdgcn_sched_barrier(0)
DEV rsrc_t mkrs(const void* p, unsigned bytes) { return __builtin_amdgcn_make_buffer_rsrc((void*)p, 0, bytes, 0x00020000); }
DEV f32x4 mfma(v4u a, v4u b, f32x4 c) {
  return __builtin_amdgcn_mfma_f32_16x16x32_bf16(__builtin_bit_cast(bf16x8, a), __builtin_bit_cast(bf16x8, b), c, 0, 0, 0);
}
DEV v4u lds128(LAS const char* p) { return *(LAS const v4u*)p; }
DEV v2u lds64(LAS const char* p) { return *(LAS const v2u*)p; }
DEV v2u ldstr(LAS const char* p) { return __builtin_bit_cast(v2u, __builtin_amdgcn_ds_read_tr16_b64_v4i16((LAS v4s*)p)); }
DEV v4u cat(v2u a, v2u b) { return v4u{a.x, a.y, b.x, b.y}; }
DEV unsigned pk2(float a, float b) { bf16x2 r = __builtin_convertvector(f32x2{a, b}, bf16x2); return __builtin_bit_cast(unsigned, r); }
DEV float blo(unsigned d) { return __builtin_bit_cast(float, d << 16); }
DEV float bhi(unsigned d) { return __builtin_bit_cast(float, d & 0xffff0000u); }
DEV void setm0(unsigned v) { asm volatile("s_mov_b32 m0, %0\n\ts_nop 0" ::"s"(v)); }
template <int OFF, int NT = 0> DEV void dma16(unsigned voff, rsrc_t rs, unsigned soff) {
  if constexpr (NT) asm volatile("buffer_load_dwordx4 %0, %1, %2 offen offset:%3 nt lds" ::"v"(voff), "s"(rs), "s"(soff), "i"(OFF));
  else asm volatile("buffer_load_dwordx4 %0, %1, %2 offen offset:%3 lds" ::"v"(voff), "s"(rs), "s"(soff), "i"(OFF));
}
typedef float f32x16 __attribute__((ext_vector_type(16)));
DEV f32x16 mfma32(v4u a, v4u b, f32x16 c) {
  return __builtin_amdgcn_mfma_f32_32x32x16_bf16(__builtin_bit_cast(bf16x8, a), __builtin_bit_cast(bf16x8, b), c, 0, 0, 0);
}
template <int IMM> DEV void setm0_add(unsigned base) { asm volatile("s_add_u32 m0, %0, %1" ::"s"(base), "i"(IMM) : "scc"); }
template <int I, int N, class Fn> DEV void static_for(Fn&& f) { if constexpr (I < N) { f(std::integral_constant<int, I>{}); static_for<I + 1, N>(f); } }
DEV void mfma_ip(f32x4& c, const v4u& a, const v4u& b) { asm volatile("v_mfma_f32_16x16x32_bf16 %0, %1, %2, %0" : "+v"(c) : "v"(a), "v"(b)); }
DEV void mfma_ipA(f32x4& c, const v4u& a, const v4u& b) { asm volatile("v_mfma_f32_16x16x32_bf16 %0, %1, %2, %0" : "+v"(c) : "a"(a), "v"(b)); }
DEV void mfma_z(f32x4& c, const v4u& a, const v4u& b) { asm volatile("v_mfma_f32_16x16x32_bf16 %0, %1, %2, 0" : "=&v"(c) : "v"(a), "v"(b)); }
DEV void mfma_zA(f32x4& c, const v4u& a, const v4u& b) { asm volatile("v_mfma_f32_16x16x32_bf16 %0, %1, %2, 0" : "=&v"(c) : "a"(a), "v"(b)); }
template <int AUX> DEV void ldA(v4u& d, unsigned voff, rsrc_t rs, unsigned soff) {
  if constexpr (AUX) asm volatile("buffer_load_dwordx4 %0, %1, %2, %3 offen sc1" : "=a"(d) : "v"(voff), "s"(rs), "s"(soff));
  else asm volatile("buffer_load_dwordx4 %0, %1, %2, %3 offen" : "=a"(d) : "v"(voff), "s"(rs), "s"(soff));
}
template <int AUX> DEV void ldV(v4u& d, unsigned voff, rsrc_t rs, unsigned soff) {
  if constexpr (AUX == 2) asm volatile("buffer_load_dwordx4 %0, %1, %2, %3 offen nt" : "=v"(d) : "v"(voff), "s"(rs), "s"(soff));
  else if constexpr (AUX == 16) asm volatile("buffer_load_dwordx4 %0, %1, %2, %3 offen sc1" : "=v"(d) : "v"(voff), "s"(rs), "s"(soff));
  else asm volatile("buffer_load_dwordx4 %0, %1, %2, %3 offen" : "=v"(d) : "v"(voff), "s"(rs), "s"(soff));
}
DEV void nop8() { asm volatile("s_nop 7"); }

constexpr size_t WS_CTRL = 0;
constexpr size_t WS_KVT = 262144;
constexpr size_t WS_YT = WS_KVT + (size_t)D * S * 2;
constexpr size_t WS_W2T = WS_YT + (size_t)D * S * 2;
constexpr size_t WS_HF = WS_W2T + (size_t)8 * 48 * 5 * 1024;
constexpr size_t WS_G = WS_HF + (size_t)128 * 48 * 1024;
constexpr size_t WS_END = WS_G + S * 4;
constexpr unsigned C_EPOCH = 0, C_TMO = 256;
constexpr unsigned FS = FSTR;
constexpr unsigned C_FW = 4096;
constexpr unsigned C_FKV = C_FW + 64 * FS;
constexpr unsigned C_FH = C_FKV + 64 * FS;
constexpr unsigned C_FY = C_FH + 768 * FS;
constexpr unsigned C_BYTES = 262144;
static_assert(C_FY + 1024 * FS <= C_BYTES, "");

#define NSTAMP 16
#define STAMP_OFF (size_t)(32u << 20)
#define STAMP_DECL
#define STAMP(i)
#define STAMP_FLUSH()
#define FT_DECL
#define FT(i)
#define FT_FLUSH()

struct Params {
  const u16 *q, *k, *v, *sk, *h, *Dv, *w1, *w2, *gw;
  u16* out;
  char* ws;
};

constexpr int LDS_BYTES = 160 * 1024;
constexpr int SC1 = 16;
#define NT_X 0
#define NT_W1 0
#define NT_P 0
constexpr int AUXP = NT_P ? 2 : 0;
#define IC(n) std::integral_constant<int, (n)>{}

DEV void ld32_sc1(unsigned& v, unsigned voff, rsrc_t rs) { asm volatile("buffer_load_dword %0, %1, %2, 0 offen sc1" : "=v"(v) : "v"(voff), "s"(rs)); }
DEV void st32_sc1(unsigned v, unsigned voff, rsrc_t rs) { asm volatile("buffer_store_dword %0, %1, %2, 0 offen sc1" ::"v"(v), "v"(voff), "s"(rs)); }
DEV void poll_finish(unsigned v, unsigned voff, rsrc_t rsC, unsigned tag) {
  asm volatile("" : "+v"(v));
  if (__builtin_amdgcn_ballot_w64(v != tag) == 0) return;
#pragma unroll 1
  for (int spins = 0; spins < (1 << 15); ++spins) {
    __builtin_amdgcn_s_sleep(SPIN_SLEEP);
    ld32_sc1(v, voff, rsC);
    asm volatile("s_waitcnt vmcnt(0)" : "+v"(v)::"memory");
    if (__builtin_amdgcn_ballot_w64(v != tag) == 0) return;
  }
  st32_sc1(1u, C_TMO, rsC);
}

DEV void poll_finish2(unsigned a, unsigned oa, unsigned b, unsigned ob, rsrc_t rsC, unsigned tag) {
  asm volatile("" : "+v"(a), "+v"(b));
  if (__builtin_amdgcn_ballot_w64(a != tag || b != tag) == 0) return;
#pragma unroll 1
  for (int spins = 0; spins < (1 << 15); ++spins) {
    __builtin_amdgcn_s_sleep(SPIN_SLEEP);
    ld32_sc1(a, oa, rsC); ld32_sc1(b, ob, rsC);
    asm volatile("s_waitcnt vmcnt(0)" : "+v"(a), "+v"(b)::"memory");
    if (__builtin_amdgcn_ballot_w64(a != tag || b != tag) == 0) return;
  }
  st32_sc1(1u, C_TMO, rsC);
}

DEV void mfma32_ip(f32x16& c, const v4u& a, const v4u& b) { asm volatile("v_mfma_f32_32x32x16_bf16 %0, %1, %2, %0" : "+v"(c) : "v"(a), "v"(b)); }
DEV void mfma32_z(f32x16& c, const v4u& a, const v4u& b) { asm volatile("v_mfma_f32_32x32x16_bf16 %0, %1, %2, 0" : "=&v"(c) : "v"(a), "v"(b)); }

constexpr int KV_BASE = 32768, KV_GS = 1152, KV_V = 9 * KV_GS, KV_CG = 17 * KV_GS;
constexpr int CV_BASE = 32768;
constexpr int CV_HC = 4160;
constexpr int CV_U = 4 * CV_HC;
constexpr int CV_CH = CV_U + 4608;
constexpr int CV_EX = CV_BASE + 4 * CV_CH;
constexpr int G2_PART = 0;
constexpr int G2_Y = 81920;
constexpr int HW = 24;

template <int AUX> DEV void ldV2(v2u& d, unsigned voff, rsrc_t rs, unsigned soff) {
  if constexpr (AUX == 2) asm volatile("buffer_load_dwordx2 %0, %1, %2, %3 offen nt" : "=v"(d) : "v"(voff), "s"(rs), "s"(soff));
  else if constexpr (AUX == 16) asm volatile("buffer_load_dwordx2 %0, %1, %2, %3 offen sc1" : "=v"(d) : "v"(voff), "s"(rs), "s"(soff));
  else asm volatile("buffer_load_dwordx2 %0, %1, %2, %3 offen" : "=v"(d) : "v"(voff), "s"(rs), "s"(soff));
}
DEV void dma16s(unsigned voff, rsrc_t rs, unsigned soff, unsigned m0v) {
  if constexpr (NT_P) asm volatile("s_mov_b32 m0, %3\n\ts_nop 0\n\tbuffer_load_dwordx4 %0, %1, %2 offen nt lds" ::"v"(voff), "s"(rs), "s"(soff), "s"(m0v));
  else asm volatile("s_mov_b32 m0, %3\n\ts_nop 0\n\tbuffer_load_dwordx4 %0, %1, %2 offen lds" ::"v"(voff), "s"(rs), "s"(soff), "s"(m0v));
}
DEV float dot2(unsigned a, unsigned b, float c) { return __builtin_amdgcn_fdot2_f32_bf16(__builtin_bit_cast(bf16x2, a), __builtin_bit_cast(bf16x2, b), c, false); }
DEV unsigned mktag(unsigned& epoch) {
  asm volatile("" : "+v"(epoch));
  return __builtin_amdgcn_readfirstlane(epoch * 0x9E3779B1u + 0x7F4A7C15u);
}

template <int NR, class ST> DEV void kv_math(const v4u (&kk)[NR + 3], const v4u (&vv)[NR], const v4u (&ww)[4], ST&& st) {
#pragma unroll
  for (int cp = 0; cp < 4; ++cp) {
    f32x2 w[4];
    w[0] = f32x2{blo(ww[cp].x), blo(ww[cp].z)}; w[1] = f32x2{bhi(ww[cp].x), bhi(ww[cp].z)};
    w[2] = f32x2{blo(ww[cp].y), blo(ww[cp].w)}; w[3] = f32x2{bhi(ww[cp].y), bhi(ww[cp].w)};
    f32x2 kf[NR + 3];
#pragma unroll
    for (int i = 0; i < NR + 3; ++i) kf[i] = f32x2{blo(kk[i][cp]), bhi(kk[i][cp])};
    f32x2 r[NR];
#pragma unroll
    for (int i = 0; i < NR; ++i) {
      f32x2 a = kf[i + 3] * w[0];
      a = __builtin_elementwise_fma(kf[i + 2], w[1], a);
      a = __builtin_elementwise_fma(kf[i + 1], w[2], a);
      a = __builtin_elementwise_fma(kf[i], w[3], a);
      r[i] = a * f32x2{blo(vv[i][cp]), bhi(vv[i][cp])};
    }
    st(cp, r);
  }
}

#define Q0 7
#define ROT 0
#define PREP_FIRST 0
#define HPUB 90
#define WPRE_ 16
constexpr int WPRE = WPRE_;
constexpr int HW0 = 16, HWM = 24;
constexpr int PW_BASE = 131072, PW_SZ = 3072;
constexpr int g2_issued(int i) { int r = HW0; for (int s = 0; s < i; ++s) r += (r - s < HWM) ? ((r + 1 - s < HWM) ? 2 : 1) : 0; return r < 48 ? r : 48; }

template <int OFF, int AUX> DEV void ldVo(v4u& d, unsigned voff, rsrc_t rs, unsigned soff) {
  static_assert(AUX == 16, "");
  asm volatile("buffer_load_dwordx4 %0, %1, %2, %3 offen offset:%4 sc1" : "=v"(d) : "v"(voff), "s"(rs), "s"(soff), "i"(OFF));
}
template <int OFF> DEV void ldAo(v4u& d, unsigned voff, rsrc_t rs, unsigned soff) {
  asm volatile("buffer_load_dwordx4 %0, %1, %2, %3 offen offset:%4 sc1" : "=a"(d) : "v"(voff), "s"(rs), "s"(soff), "i"(OFF));
}

template <bool G1R>
DEV void body(const Params& p, LAS char* lds, const int x, const int r, const int wave, const int lane) {
  const int g = lane >> 4, q4 = (lane >> 2) & 3, p4 = lane & 3, c16 = lane & 15;
  STAMP_DECL; FT_DECL;
  STAMP(0); FT(13);
  const rsrc_t rsC = mkrs(p.ws + WS_CTRL, C_BYTES);
  unsigned epoch, tag;
  ld32_sc1(epoch, C_EPOCH, rsC);
  const int cl = G1R ? (wave >> 1) : wave, half = G1R ? (wave & 1) : 0;
  const int ch = (G1R ? 80 * x + 2 * r : 80 * x + 48 + 4 * (r - 24)) + cl;
  const unsigned cb = CV_BASE + cl * CV_CH;
  constexpr int NCHK = G1R ? 2 : 4;
  unsigned dv;
  { const rsrc_t rsD = mkrs(p.Dv, D * 2); asm volatile("buffer_load_ushort %0, %1, %2, 0 offen" : "=v"(dv) : "v"(ch * 2), "s"(rsD)); }
  const rsrc_t rsHh = mkrs(p.h, D * S * 2), rsU = mkrs(p.ws + WS_KVT, D * S * 2), rsY = mkrs(p.ws + WS_YT, D * S * 2);
  const rsrc_t rsW2 = mkrs(p.ws + WS_W2T, 8 * 48 * 5 * 1024), rsH = mkrs(p.ws + WS_HF, 128 * 48 * 1024);
#define MAP42 1
  const int rb = MAP42 ? 8 * (x >> 1) + (r & 7) : 4 * x + (r & 3), nt = MAP42 ? 4 * (x & 1) + (r >> 3) : r >> 2;
  const unsigned vl = lane * 16;
  const unsigned sW = (nt * 48 + 12 * wave) * 5 * 1024;
  const unsigned foW = C_FW + lane * FS, foKV = C_FKV + lane * FS;
  v4u W[12][5];
  auto ldW = [&](auto I) { constexpr int i = I.value; if constexpr (i >= 0 && i < 60) ldAo<(i & 3) * 1024>(W[i / 5][i % 5], vl, rsW2, sW + (i & ~3) * 1024); };
  v4u hc[NCHK], uc[NCHK]; v2u hp0;
  auto ld_h = [&]() {
    ldV2<AUXP>(hp0, half > 0 ? (unsigned)(ch * 4096 + half * NCHK * 1024 - 8) : 0x80000000u, rsHh, 0);
#pragma unroll
    for (int i = 0; i < NCHK; ++i) ldV<AUXP>(hc[i], ch * 4096 + ((half * NCHK + i) * 64 + lane) * 16, rsHh, 0);
  };
  auto ld_u = [&]() {
#pragma unroll
    for (int i = 0; i < NCHK; ++i) ldV<SC1>(uc[i], ch * 4096 + ((half * NCHK + i) * 64 + lane) * 16, rsU, 0);
  };
  auto fill_h = [&]() {
    asm volatile("" : "+v"(hp0));
#pragma unroll
    for (int i = 0; i < NCHK; ++i) asm volatile("" : "+v"(hc[i]));
    unsigned e0 = hp0.x, e1 = hp0.y;
#pragma unroll
    for (int i = 0; i < NCHK; ++i) {
      const int n = (half * NCHK + i) * 64 + lane;
      const v4u cur = hc[i];
      const v2u prv = v2u{(unsigned)__builtin_amdgcn_update_dpp((int)e0, (int)cur.z, 0x138, 0xf, 0xf, false), (unsigned)__builtin_amdgcn_update_dpp((int)e1, (int)cur.w, 0x138, 0xf, 0xf, false)};
      e0 = __builtin_amdgcn_readlane(cur.z, 63); e1 = __builtin_amdgcn_readlane(cur.w, 63);
      const unsigned om2 = __builtin_amdgcn_alignbit(prv.y, prv.x, 16), om1 = __builtin_amdgcn_alignbit(cur.x, prv.y, 16);
      const unsigned o0 = __builtin_amdgcn_alignbit(cur.y, cur.x, 16), o1 = __builtin_amdgcn_alignbit(cur.z, cur.y, 16), o2 = __builtin_amdgcn_alignbit(cur.w, cur.z, 16);
      LAS char* dst = lds + cb + (n + 2) * 16;
      *(LAS v4u*)(dst) = cur;
      *(LAS v4u*)(dst + CV_HC) = v4u{om1, o0, o1, o2};
      *(LAS v4u*)(dst + 2 * CV_HC) = v4u{prv.y, cur.x, cur.y, cur.z};
      *(LAS v4u*)(dst + 3 * CV_HC) = v4u{om2, om1, o0, o1};
    }
    if (half == 0) {
      if (lane < 8) *(LAS v4u*)(lds + cb + (lane >> 1) * CV_HC + (lane & 1) * 16) = v4u{0, 0, 0, 0};
      if (lane >= 32 && lane < 62) *(LAS v4u*)(lds + cb + CV_U + (lane - 32) * 16) = v4u{0, 0, 0, 0};
    }
  };
  auto fill_u = [&]() {
#pragma unroll
    for (int i = 0; i < NCHK; ++i) {
      asm volatile("" : "+v"(uc[i]));
      *(LAS v4u*)(lds + cb + CV_U + 480 + ((half * NCHK + i) * 64 + lane) * 16) = uc[i];
    }
  };
  unsigned fw;

  if constexpr (G1R) {
    const int sb = 4 * (x >> 1) + (r & 3), fs = 6 * (x & 1) + (r >> 2);
    f32x16 acc1[2][2];
    const rsrc_t rsX = mkrs(p.q, S * D * 2), rsW = mkrs(p.w1, D * F * 2);
    unsigned vX[4], vW[4];
#pragma unroll
    for (int ii = 0; ii < 4; ++ii) {
      vX[ii] = (32 * wave + 8 * ii + (lane >> 3)) * 1280 + (((lane & 7) ^ (4 * (ii & 1) + g)) << 4) - 1024 * ii;
      vW[ii] = (16 * wave + 4 * ii + g) * 3072 + (((((c16 >> 1) ^ (g << 1)) << 1) | (c16 & 1)) << 4) - 1024 * ii;
    }
    const unsigned sX0 = sb * 128 * 1280, sW0 = fs * 256;
    const unsigned mX = wave * 4096, mW = 16384 + wave * 4096;
    const int rot = ROT ? (2 * (r >> 2) + 3 * (r & 3)) % 10 : 0;
    auto kslice = [&](int t) { const int k = t + rot; return k >= 10 ? k - 10 : k; };
    unsigned sq;
    auto grp_begin = [&](auto Q) { constexpr int q = Q.value, t = q >> 1;
      if constexpr (q < 20) { sq = (q & 1) ? sW0 + kslice(t) * (64 * 3072) : sX0 + kslice(t) * 128; setm0_add<(t % 5) * 32768>((q & 1) ? mW : mX); } };
    auto grp_dma = [&](auto Q, auto I) { constexpr int q = Q.value, i = I.value;
      if constexpr (q < 20) { if constexpr (q & 1) dma16<1024 * i, NT_W1>(vW[i], rsW, sq); else dma16<1024 * i, NT_X>(vX[i], rsX, sq); } };
    FT(10);
    static_for<0, Q0>([&](auto Q) { grp_begin(Q); asm volatile("s_nop 0"); static_for<0, 4>([&](auto I) { grp_dma(Q, I); }); });
    FT(11);
    const int ws_ = wave & 1, wf = wave >> 1, h = lane >> 5, r32 = lane & 31;
    unsigned aX[3][4], aW[3][2];
#pragma unroll
    for (int w = 0; w < 3; ++w) {
#pragma unroll
      for (int ks = 0; ks < 4; ++ks) aX[w][ks] = w * 65536 + (64 * ws_ + r32) * 128 + (((2 * ks + h) ^ ((r32 >> 1) & 7)) << 4);
#pragma unroll
      for (int ft = 0; ft < 2; ++ft) aW[w][ft] = w * 65536 + 16384 + (8 * h + q4) * 256 + (((2 * (2 * wf + ft) + (g & 1)) ^ (q4 << 1)) << 5) + 8 * p4;
    }
    v4u A[2][2][2], B[2][2][2];
    v2u At;
    auto rd = [&](auto T, auto HF, auto SET, auto N) {
      constexpr int t = T.value, hf = HF.value, set = SET.value, n = N.value;
      if constexpr (t < 10) {
        constexpr int b = t % 5, w = b >> 1, bo = (b & 1) * 32768;
        constexpr int kk = n / 6, rr = n % 6, ks = 2 * hf + kk;
        if constexpr (rr == 0) B[set][kk][0] = lds128(lds + aX[w][ks] + bo);
        else if constexpr (rr == 3) B[set][kk][1] = lds128(lds + aX[w][ks] + bo + 32 * 128);
        else { constexpr int ft = rr / 3, jj = (rr % 3) - 1;
          v2u xx = ldstr(lds + aW[w][ft] + bo + ks * 4096 + jj * 1024);
          if constexpr (jj == 0) At = xx; else A[set][kk][ft] = cat(At, xx); }
      }
    };

    unsigned fk;
    waitv<4 * (Q0 - 2)>(); sbar();
    STAMP(1);
    static_for<0, 12>([&](auto N) { rd(IC(0), IC(0), IC(0), N); });
    SCHED();
    static_for<0, 10>([&](auto T) {
      constexpr int t = T.value;
      static_for<0, 2>([&](auto HF) {
        constexpr int hf = HF.value, hi = 2 * t + hf, q = Q0 + hi;
        static_for<0, 8>([&](auto M) {
          constexpr int m = M.value, kk = m >> 2, ft = (m >> 1) & 1, st = m & 1;
          if constexpr (t == 0 && hf == 0 && kk == 0) mfma32_z(acc1[ft][st], A[hf][kk][ft], B[hf][kk][st]);
          else mfma32_ip(acc1[ft][st], A[hf][kk][ft], B[hf][kk][st]);
          if constexpr (m < 6) {
            if constexpr (hf == 0) { rd(T, IC(1), IC(1), IC(2 * m)); rd(T, IC(1), IC(1), IC(2 * m + 1)); }
            else { rd(IC(t + 1), IC(0), IC(0), IC(2 * m)); rd(IC(t + 1), IC(0), IC(0), IC(2 * m + 1)); }
          }
          if constexpr (m == 0) grp_begin(IC(q));
          if constexpr (m & 1) grp_dma(IC(q), IC(m >> 1));
          SCHED();
        });
        if constexpr (hf == 0 && t < 9) {
          constexpr int lastq = (Q0 + 2 * t < 19) ? Q0 + 2 * t : 19;
          constexpr int younger = 4 * (lastq - (2 * t + 3) > 0 ? lastq - (2 * t + 3) : 0);
          waitv<younger>(); sbar(); SCHED();
        }
      });
      FT(t); SCHED();
    });
    asm volatile("s_nop 15" : "+v"(acc1[0][0]), "+v"(acc1[0][1]), "+v"(acc1[1][0]), "+v"(acc1[1][1]));
    sbar();
    STAMP(2);
    tag = mktag(epoch);
    ld_h();
    const unsigned vo = (g & 1) * (48 * 1024) + (c16 + 16 * h) * 16;
    static_for<0, 4>([&](auto Q) {
      constexpr int qd = Q.value, st = qd >> 1, ft = qd & 1;
      const f32x16 a = acc1[ft][st];
      unsigned o[8];
      static_for<0, 2>([&](auto G) {
        constexpr int gq = G.value;
        static_for<0, 4>([&](auto E) {
          constexpr int e = 8 * gq + 2 * E.value;
          const float r0 = a[e] * __builtin_amdgcn_rcpf(1.f + __builtin_amdgcn_exp2f(a[e] * -1.44269504088896f));
          const float r1 = a[e + 1] * __builtin_amdgcn_rcpf(1.f + __builtin_amdgcn_exp2f(a[e + 1] * -1.44269504088896f));
          o[e >> 1] = pk2(r0, r1);
        });
        SCHED();
      });
      const unsigned so = ((8 * sb + 4 * ws_ + 2 * st) * 48 + 4 * fs + 2 * wf + ft) * 1024;
      __builtin_amdgcn_raw_buffer_store_b128(v4u{o[0], o[1], o[4], o[5]}, rsH, vo, so, SC1);
      __builtin_amdgcn_raw_buffer_store_b128(v4u{o[2], o[3], o[6], o[7]}, rsH, vo + 512, so, SC1);
      SCHED();
      FT(32 + qd); SCHED();
    });
    ld32_sc1(fk, foKV, rsC); ld32_sc1(fw, foW, rsC);
    waitv<10>();
    fill_h();
    FT(54);
    waitv<0>();
    if (lane == 0) st32_sc1(tag, C_FH + ((sb * 2 + ws_) * 24 + fs * 2 + wf) * FS, rsC);
    FT(55);
    poll_finish(fk, foKV, rsC, tag);
    ld_u();
    FT(36);
    poll_finish(fw, foW, rsC, tag);
    FT(37);
    static_for<0, WPRE>([&](auto I) { ldW(I); });
    STAMP(3);
    waitv<WPRE>();
    fill_u();
    waitl<0>(); sbar();
    STAMP(4);
  } else {
    const int j = (r - 24) * 8 + x, rbk = j >> 1, cg0 = 5 * (j & 1);
    const rsrc_t rsK = mkrs(p.k, S * D * 2), rsV = mkrs(p.v, S * D * 2), rsS = mkrs(p.sk, D * 8), rsO = mkrs(p.ws + WS_KVT, D * S * 2);
    const unsigned vr = (lane >> 3) * 1280 + ((lane & 7) << 4), vr4 = vr ^ 64;
    auto dmaK = [&](int cgl, int G) {
      const int row = 64 * rbk - 8 + 8 * G;
      dma16s(((G >> 1) & 1) ? vr4 : vr, rsK, row >= 0 ? (unsigned)(row * 1280 + (cg0 + cgl) * 128) : 0x80000000u, KV_BASE + cgl * KV_CG + G * KV_GS);
    };
    auto dmaV = [&](int cgl, int G) {
      dma16s(((G >> 1) & 1) ? vr4 : vr, rsV, (64 * rbk + 8 * G) * 1280 + (cg0 + cgl) * 128, KV_BASE + cgl * KV_CG + KV_V + G * KV_GS);
    };
#pragma unroll
    for (int G = 0; G < 9; ++G) dmaK(wave, G);
#pragma unroll
    for (int G = 0; G < 8; ++G) dmaV(wave, G);
    const unsigned pw = PW_BASE + wave * PW_SZ;
    dma16s(lane < 32 ? (unsigned)((cg0 + wave) * 512 + lane * 16) : (lane < 40 ? (unsigned)((64 * (cg0 + 4) + 16 * wave) * 8 + (lane - 32) * 16) : 0x80000000u), rsS, 0, pw);
    dmaK(4, wave); dmaK(4, wave + 4); dmaV(4, wave); dmaV(4, wave + 4);
    if (wave == 0) dmaK(4, 8);
    const int cc = lane >> 3, rg = lane & 7;
    FT(10);
    waitv<4>();
    STAMP(1);
    {
      const unsigned kb = KV_BASE + wave * KV_CG;
      const unsigned a0 = kb + rg * KV_GS + ((cc ^ (4 * ((rg >> 1) & 1))) << 4), a1 = kb + (rg + 1) * KV_GS + ((cc ^ (4 * (((rg + 1) >> 1) & 1))) << 4);
      v4u kk[11], vv[8], ww[4];
#pragma unroll
      for (int i = 0; i < 4; ++i) ww[i] = lds128(lds + pw + cc * 64 + i * 16);
#pragma unroll
      for (int i = 0; i < 11; ++i) kk[i] = i < 3 ? lds128(lds + a0 + (5 + i) * 128) : lds128(lds + a1 + (i - 3) * 128);
#pragma unroll
      for (int i = 0; i < 8; ++i) vv[i] = lds128(lds + a0 + KV_V + i * 128);
      const unsigned ob = (64 * (cg0 + wave) + 8 * cc) * 4096 + (64 * rbk + 8 * rg) * 2;
      kv_math<8>(kk, vv, ww, [&](int cp, const f32x2 (&rr)[8]) {
#pragma unroll
        for (int c = 0; c < 2; ++c) {
          v4u o{pk2(rr[0][c], rr[1][c]), pk2(rr[2][c], rr[3][c]), pk2(rr[4][c], rr[5][c]), pk2(rr[6][c], rr[7][c])};
          __builtin_amdgcn_raw_buffer_store_b128(o, rsO, ob + (2 * cp + c) * 4096, 0, SC1);
        }
      });
    }
    const int id = 4 * j + wave, pk = id / 5, blk = id % 5;
    const rsrc_t rsWs = mkrs(p.w2, F * D * 2);
    const unsigned ldsb = wave * 8192;
#pragma unroll
    for (int i = 0; i < 8; ++i) {
      const int row = 4 * i + g;
      dma16s((32 * pk + row) * 1280 + blk * 256 + (((((c16 >> 1) ^ (row & 7)) << 1) | (c16 & 1)) << 4), rsWs, id < 240 ? 0u : 0x80000000u, ldsb + i * 1024);
    }
    waitv<16>(); sbar();
    STAMP(2);
    {
      const int cc4 = 2 * wave + (lane >> 5), rp = lane & 31;
      const unsigned kb = KV_BASE + 4 * KV_CG;
      v4u kk[5], vv[2], w4[4];
#pragma unroll
      for (int i = 0; i < 4; ++i) w4[i] = lds128(lds + pw + 512 + (lane >> 5) * 64 + i * 16);
#pragma unroll
      for (int i = 0; i < 5; ++i) { const int row = 2 * rp + 5 + i; kk[i] = lds128(lds + kb + (row >> 3) * KV_GS + (row & 7) * 128 + ((cc4 ^ (4 * ((row >> 4) & 1))) << 4)); }
#pragma unroll
      for (int i = 0; i < 2; ++i) { const int row = 2 * rp + i; vv[i] = lds128(lds + kb + KV_V + (row >> 3) * KV_GS + (row & 7) * 128 + ((cc4 ^ (4 * ((row >> 4) & 1))) << 4)); }
      const unsigned ob = (64 * (cg0 + 4) + 8 * cc4) * 4096 + (64 * rbk + 2 * rp) * 2;
      kv_math<2>(kk, vv, w4, [&](int cp, const f32x2 (&rr)[2]) {
#pragma unroll
        for (int c = 0; c < 2; ++c) __builtin_amdgcn_raw_buffer_store_b32(pk2(rr[0][c], rr[1][c]), rsO, ob + (2 * cp + c) * 4096, 0, SC1);
      });
    }
    FT(11);
    waitv<8>();
    {
      const rsrc_t rsWo = mkrs(p.ws + WS_W2T, 8 * 48 * 5 * 1024);
      const int r0 = 4 * g + q4;
#pragma unroll
      for (int i = 0; i < 8; ++i) {
        const unsigned a = ldsb + r0 * 256 + ((i ^ (r0 & 7)) << 5) + 8 * p4;
        const v4u fr = cat(ldstr(lds + a), ldstr(lds + a + 4096));
        const int ctg = 8 * blk + i, ntt = ctg / 5, ct = ctg % 5;
        __builtin_amdgcn_raw_buffer_store_b128(fr, rsWo, vl, id < 240 ? (unsigned)(((ntt * 48 + pk) * 5 + ct) * 1024) : 0x80000000u, SC1);
      }
    }
    FT(0);
    waitv<8>();
    waitl<0>(); sbar();
    tag = mktag(epoch);
    if (threadIdx.x == 0) st32_sc1(tag, C_FKV + j * FS, rsC);
    STAMP(3);
    ld_h();
    waitv<5>(); sbar();
    if (threadIdx.x == 0) st32_sc1(tag, C_FW + j * FS, rsC);
    unsigned fk;
    ld32_sc1(fk, foKV, rsC);
    FT(3);
    waitv<1>();
    waitv<0>();
    fill_h();
    FT(2);
    poll_finish(fk, foKV, rsC, tag);
    FT(4);
    ld_u();
    const rsrc_t rsX = mkrs(p.q, S * D * 2), rsG = mkrs(p.gw, D * 2), rsGo = mkrs(p.ws + WS_G, S * 4);
    const int grow = 256 * x + 32 * (r - 24) + 8 * wave + (lane >> 3), part = lane & 7;
    v4u xx[10];
    dma16s(lane * 16, rsG, 0, pw + 1024); dma16s(1024 + lane * 16, rsG, 0, pw + 2048);
#pragma unroll
    for (int i = 0; i < 10; ++i) ldV<0>(xx[i], grow * 1280 + i * 128 + part * 16, rsX, 0);
    waitv<12>();
    fill_u();
    ld32_sc1(fw, foW, rsC);
    waitv<1>();
    {
      float a0 = 0.f, a1 = 0.f;
#pragma unroll
      for (int i = 0; i < 10; ++i) {
        asm volatile("" : "+v"(xx[i]));
        const v4u gg = lds128(lds + pw + 1024 + (i * 8 + part) * 16);
        a0 = dot2(xx[i].x, gg.x, a0); a1 = dot2(xx[i].y, gg.y, a1); a0 = dot2(xx[i].z, gg.z, a0); a1 = dot2(xx[i].w, gg.w, a1);
      }
      float a = a0 + a1;
      a += __shfl_xor(a, 1); a += __shfl_xor(a, 2); a += __shfl_xor(a, 4);
      const float gt = __builtin_amdgcn_rcpf(1.f + __builtin_amdgcn_exp2f(a * -1.44269504088896f));
      if (part == 0) __builtin_amdgcn_raw_buffer_store_b32(__builtin_bit_cast(unsigned, gt), rsGo, grow * 4, 0, SC1);
    }
    FT(5);
    waitl<0>();
    STAMP(4);
  }
  STAMP(5);
  constexpr int NZ = G1R ? 4 : 8;
  constexpr int W0 = G1R ? WPRE : 0, WG0 = G1R ? 0 : 14;
  auto wcount = [](int gap) constexpr { return gap < WG0 ? 0 : (G1R ? ((gap - WG0) * (60 - WPRE)) / 140 + 1 : (gap - WG0) / 4 + 1); };
  const unsigned foH = C_FH + (((rb >> 1) * 2 + (rb & 1)) * 24 + (lane < 24 ? lane : 0)) * FS;
  unsigned fh;
  constexpr int GPOLL = NZ * 36 - 12;
  f32x4 acc[8];
  {
    const unsigned aA = cb + q4 * CV_HC + 56 - 16 * g + 8 * p4 + 256 * half;
    const unsigned aB = cb + CV_U + 448 + 32 * c16 + 16 * g - 256 * half - 64 * (NZ - 1);
    v4u A[8], B[8];
#pragma unroll
    for (int i = 0; i < 8; ++i) {
      B[7 - i] = lds128(lds + aB + 64 * (NZ - 1) + 512 * (7 - i));
      A[i] = cat(ldstr(lds + aA + 512 * i + 8), ldstr(lds + aA + 512 * i));
    }
    SCHED();
    static_for<0, NZ>([&](auto Z) {
      constexpr int z = Z.value;
      static_for<0, 8>([&](auto I) {
        constexpr int i = I.value;
        static_for<0, 8 - i>([&](auto DD) {
          constexpr int d = 7 - i - DD.value;
          constexpr int gap = z * 36 + (i * (17 - i)) / 2 + DD.value;
          if constexpr (z == 0 && i == 0) mfma_z(acc[i + d], A[i], B[d]); else mfma_ip(acc[i + d], A[i], B[d]);
          if constexpr (d == 7 - i && z + 1 < NZ) B[7 - i] = lds128(lds + aB + 64 * (NZ - 2 - z) + 512 * (7 - i));
          if constexpr (!G1R && gap == WG0 - 2) { waitv<0>(); poll_finish(fw, foW, rsC, tag); }
          if constexpr (wcount(gap) > wcount(gap - 1)) ldW(IC(W0 + wcount(gap) - 1));
          if constexpr (gap == GPOLL) ld32_sc1(fh, foH, rsC);
          SCHED();
        });
        if constexpr (i == 7) { FT(40 + z); SCHED(); }
        if constexpr (z + 1 < NZ) { A[i] = cat(ldstr(lds + aA + 64 * (z + 1) + 512 * i + 8), ldstr(lds + aA + 64 * (z + 1) + 512 * i)); SCHED(); }
      });
    });
  }
  static_assert(W0 + wcount(NZ * 36 - 1) >= 60, "all W2 fragments issued");
  STAMP(6);
  constexpr int WAFT = 60 - (W0 + wcount(GPOLL));
  waitv<(WAFT > 0 ? WAFT : 0)>();
  FT(48);
  poll_finish(fh, foH, rsC, tag);
  FT(49);
  v4u Hf[HWM];
  const unsigned sH = (4 * rb * 48 + 12 * wave) * 1024;
  auto ldH = [&](auto N) { constexpr int n = N.value; if constexpr (n < 48) ldVo<(n & 3) * 1024, SC1>(Hf[n % HWM], vl, rsH, sH + ((n / 12) * 48 + ((n % 12) & ~3)) * 1024); };
  static_for<0, HW0>([&](auto I) { ldH(I); });
  FT(14);
  asm volatile("s_nop 7" : "+v"(acc[0]), "+v"(acc[1]), "+v"(acc[2]), "+v"(acc[3]), "+v"(acc[4]), "+v"(acc[5]), "+v"(acc[6]), "+v"(acc[7]));
  SCHED();
  int ctb = 0;
  if constexpr (G1R) {
    LAS char* ex = lds + CV_EX + cl * 8192;
    f32x4 keep[4];
#pragma unroll
    for (int i = 0; i < 4; ++i) {
      f32x4 lo = acc[i], hi = acc[4 + i];
      asm volatile("" : "+v"(lo), "+v"(hi));
      *(LAS f32x4*)(ex + half * 4096 + i * 1024 + lane * 16) = half == 0 ? hi : lo;
      keep[i] = half == 0 ? lo : hi;
    }
    waitl<0>(); sbar();
#pragma unroll
    for (int i = 0; i < 4; ++i) acc[i] = keep[i] + *(LAS f32x4*)(ex + (1 - half) * 4096 + i * 1024 + lane * 16);
    ctb = 4 * half;
  }
  FT(51);
  constexpr int NT = G1R ? 4 : 8, NYS = NT / 2;
  {
    asm volatile("" : "+v"(dv));
    const float Dc = blo(dv);
#pragma unroll
    for (int i = 0; i < NT; i += 2) {
      unsigned w0[2][2];
#pragma unroll
      for (int e = 0; e < 2; ++e) {
        const int ct = ctb + i + e;
        const f32x4 a = acc[i + e];
        const v2u uu = lds64(lds + cb + CV_U + 480 + 512 * ct + 32 * c16 + 8 * g);
        w0[e][0] = pk2(fmaf(blo(uu.x), Dc, a[0]), fmaf(bhi(uu.x), Dc, a[1]));
        w0[e][1] = pk2(fmaf(blo(uu.y), Dc, a[2]), fmaf(bhi(uu.y), Dc, a[3]));
      }
      auto s0 = __builtin_amdgcn_permlane16_swap(w0[0][0], w0[1][0], false, false);
      auto s1 = __builtin_amdgcn_permlane16_swap(w0[0][1], w0[1][1], false, false);
      const int ct = ctb + i + (g & 1);
      __builtin_amdgcn_raw_buffer_store_b128(v4u{s0[0], s1[0], s0[1], s1[1]}, rsY, ch * 4096 + 512 * ct + 32 * c16 + 8 * (g & 2), 0, SC1);
    }
  }
  FT(52);
  waitl<0>(); sbar();
  STAMP(7);
  f32x4 ac2[2][5];
  const unsigned foY0 = C_FY + (nt * 128 + lane) * FS, foY1 = foY0 + 64 * FS;
  const unsigned foG = C_FY + ((rb >> 2) * 128 + 96 + (lane & 31)) * FS;
  unsigned fy0, fy1, fg, gtu;
  v4u yv[3]; v2u qq[5];
  const int s = 64 * rb + 16 * wave + c16;
#define SF_ 5
  constexpr int SF = SF_;
  constexpr int SP = 25;
  constexpr int SY = 40;
  static_assert(g2_issued(SP) == 48 && SY >= SP + 8, "");
  auto extras_at = [](int st) constexpr { return (st == SF ? 1 : 0) + ((st >= SP && st < SP + 8) ? 1 : 0) + (st == SY ? 4 : 0); };
  static_for<0, 48>([&](auto I) {
    constexpr int i = I.value, rt = i / 12, k = i % 12, set = rt & 1;
    constexpr int younger = [&]() constexpr {
      int y = g2_issued(i) - (i + 1);
      if (i < HW0) y += NYS;
      int si = -1; while (i >= HW0 && g2_issued(si + 1) <= i) ++si;
      for (int st = (si < 0 ? 0 : si); st < i; ++st) y += extras_at(st);
      return y; }();
    waitv<(younger < 63 ? younger : 63)>();
    static_for<0, 5>([&](auto C) { constexpr int ct = C.value;
      if constexpr (k == 0) mfma_zA(ac2[set][ct], W[k][ct], Hf[i % HWM]); else mfma_ipA(ac2[set][ct], W[k][ct], Hf[i % HWM]);
      if constexpr (rt > 0 && k >= 1 && k <= 5 && ct == 3) *(LAS f32x4*)(lds + G2_PART + (((rt - 1) * 4 + wave) * 5 + (k - 1)) * 1024 + lane * 16) = ac2[set ^ 1][k - 1];
      if constexpr (ct == 1) { static_for<g2_issued(i), g2_issued(i + 1)>([&](auto N) { ldH(N); }); }
    });
    if constexpr (i == SF) {
      waitv<g2_issued(SF + 1) - HW0>();
      if (lane == 0) st32_sc1(tag, C_FY + ((x * 32 + r) * 4 + wave) * FS, rsC);
    }
    if constexpr (i == SP) ld32_sc1(fy0, foY0, rsC);
    if constexpr (i == SP + 1) ld32_sc1(fy1, foY1, rsC);
    if constexpr (i == SP + 2) ld32_sc1(fg, foG, rsC);
    if constexpr (i >= SP + 3 && i < SP + 8) { const rsrc_t rsQ = mkrs(p.q, S * D * 2); ldV2<AUXP>(qq[i - SP - 3], (s * D + 80 * nt + 16 * (i - SP - 3) + 4 * g) * 2, rsQ, 0); }
    if constexpr (i == SY) {
      waitv<5>();
      poll_finish2(fy0, foY0, fy1, foY1, rsC, tag);
      poll_finish(fg, foG, rsC, tag);
      { const rsrc_t rsG = mkrs(p.ws + WS_G, S * 4); ld32_sc1(gtu, s * 4, rsG); }
#pragma unroll
      for (int u = 0; u < 3; ++u) {
        const int idx = u * 256 + wave * 64 + lane, d = idx >> 3, cch = idx & 7;
        ldV<SC1>(yv[u], idx < 640 ? (unsigned)((80 * nt + d) * 4096 + rb * 128 + cch * 16) : 0x80000000u, rsY, 0);
      }
    }
    if constexpr (i % 4 == 3) { FT(16 + i / 4); }
    SCHED();
  });
  STAMP(9);
  asm volatile("s_nop 7" : "+v"(ac2[1][0]), "+v"(ac2[1][1]), "+v"(ac2[1][2]), "+v"(ac2[1][3]), "+v"(ac2[1][4]));
#pragma unroll
  for (int ct = 0; ct < 5; ++ct) *(LAS f32x4*)(lds + G2_PART + ((3 * 4 + wave) * 5 + ct) * 1024 + lane * 16) = ac2[1][ct];
  unsigned fe[16];
  const bool bump = blockIdx.x == 0 && wave == 0;
  if (bump) {
#pragma unroll
    for (int i = 0; i < 16; ++i) ld32_sc1(fe[i], C_FY + (i * 64 + lane) * FS, rsC);
  }
  waitv<0>();
#pragma unroll
  for (int u = 0; u < 3; ++u) {
    asm volatile("" : "+v"(yv[u]));
    const int idx = u * 256 + wave * 64 + lane, d = idx >> 3, cch = idx & 7;
    if (idx < 640) { *(LAS v2u*)(lds + G2_Y + d * 136 + cch * 16) = v2u{yv[u].x, yv[u].y}; *(LAS v2u*)(lds + G2_Y + d * 136 + cch * 16 + 8) = v2u{yv[u].z, yv[u].w}; }
  }
  asm volatile("" : "+v"(gtu));
#pragma unroll
  for (int ct = 0; ct < 5; ++ct) asm volatile("" : "+v"(qq[ct]));
  waitl<0>(); sbar();
  STAMP(10);
  const float gt = __builtin_bit_cast(float, gtu);
#pragma unroll
  for (int ct = 0; ct < 5; ++ct) {
    f32x4 b = *(LAS f32x4*)(lds + G2_PART + ((wave * 4 + 0) * 5 + ct) * 1024 + lane * 16);
#pragma unroll
    for (int w = 1; w < 4; ++w) b += *(LAS f32x4*)(lds + G2_PART + ((wave * 4 + w) * 5 + ct) * 1024 + lane * 16);
    const v2u yy = ldstr(lds + G2_Y + (16 * ct + 4 * g + q4) * 136 + (16 * wave + 4 * p4) * 2);
    const float o0 = gt * (blo(qq[ct].x) * blo(yy.x)) + (1.f - gt) * b[0];
    const float o1 = gt * (bhi(qq[ct].x) * bhi(yy.x)) + (1.f - gt) * b[1];
    const float o2 = gt * (blo(qq[ct].y) * blo(yy.y)) + (1.f - gt) * b[2];
    const float o3 = gt * (bhi(qq[ct].y) * bhi(yy.y)) + (1.f - gt) * b[3];
    *(GAS v2u*)(p.out + s * D + 80 * nt + 16 * ct + 4 * g) = v2u{pk2(o0, o1), pk2(o2, o3)};
  }
  if (bump) {
    unsigned bad = 0;
#pragma unroll
    for (int e = 0; e < 16; ++e) { asm volatile("" : "+v"(fe[e])); bad |= fe[e] ^ tag; }
    if (__builtin_amdgcn_ballot_w64(bad != 0) != 0) {
#pragma unroll 1
      for (int e = 0; e < 16; ++e) { const unsigned fo = C_FY + (e * 64 + lane) * FS; unsigned f; ld32_sc1(f, fo, rsC); waitv<0>(); poll_finish(f, fo, rsC, tag); }
    }
    if (lane == 0) st32_sc1(tag, C_EPOCH, rsC);
  }
  STAMP(11);
  STAMP_FLUSH(); FT_FLUSH();
}

__global__ __launch_bounds__(256, 1) void fused(Params p) {
  extern __shared__ __attribute__((aligned(16))) char smem[];
  LAS char* lds = (LAS char*)smem;
  const int lane = threadIdx.x & 63, wave = __builtin_amdgcn_readfirstlane(threadIdx.x >> 6);
  const int x = blockIdx.x & 7, rp_ = blockIdx.x >> 3, r = PREP_FIRST ? (rp_ < 8 ? rp_ + 24 : rp_ - 8) : rp_;
  unsigned pf = 0;
  {
    unsigned long long pc; unsigned len;
    asm volatile("s_getpc_b64 %0\nLpf_here:\n\ts_mov_b32 %1, Lpf_end-Lpf_here" : "=s"(pc), "=s"(len));
    const unsigned off = (rp_ * 4 + wave) * 1024 + lane * 16;
    const rsrc_t rs = mkrs((const void*)pc, len);
    v4u t;
    asm volatile("buffer_load_dwordx4 %0, %1, %2, 0 offen" : "=v"(t) : "v"(off), "s"(rs));
    if (r < 24) { body<true>(p, lds, x, r, wave, lane); }
    else { body<false>(p, lds, x, r, wave, lane); }
    asm volatile("s_waitcnt vmcnt(0)\nLpf_end:" : "+v"(t));
    pf = t.x;
  }
  asm volatile("" ::"v"(pf));
}

template <int MODE> __global__ __launch_bounds__(256) void k_ten(char* ws) {
  const unsigned long long r0 = __builtin_amdgcn_s_memrealtime();
  const rsrc_t rs = mkrs(ws + (64u << 20), 64u << 20);
  if (MODE == 1) { v4u a{0, 0, 0, 0}; for (int i = 0; i < 32; ++i) { v4u t = __builtin_amdgcn_raw_buffer_load_b128(rs, (blockIdx.x * 32 + i) * 4096 + threadIdx.x * 16, 0, 0); a ^= t; } asm volatile("" :: "v"(a)); }
  if (MODE == 2) for (int i = 0; i < 32; ++i) __builtin_amdgcn_raw_buffer_store_b128(v4u{1, 2, 3, 4}, rs, (blockIdx.x * 32 + i) * 4096 + threadIdx.x * 16, 0, 0);
  if (MODE == 3) for (int i = 0; i < 32; ++i) __builtin_amdgcn_raw_buffer_store_b128(v4u{1, 2, 3, 4}, rs, (blockIdx.x * 32 + i) * 4096 + threadIdx.x * 16, 0, 16);
  while (__builtin_amdgcn_s_memrealtime() - r0 < 1500) __builtin_amdgcn_s_sleep(1);
}
extern "C" void kernel_launch(void* const* d_in, const int* in_sizes, int n_in, void* d_out, int out_size, void* d_ws, size_t ws_size, hipStream_t stream) {
  Params p;
  p.q = (const u16*)d_in[0]; p.k = (const u16*)d_in[1]; p.v = (const u16*)d_in[2]; p.sk = (const u16*)d_in[3];
  p.h = (const u16*)d_in[4]; p.Dv = (const u16*)d_in[5]; p.w1 = (const u16*)d_in[7]; p.w2 = (const u16*)d_in[8]; p.gw = (const u16*)d_in[9];
  p.out = (u16*)d_out; p.ws = (char*)d_ws;
  (void)hipFuncSetAttribute((const void*)fused, hipFuncAttributeMaxDynamicSharedMemorySize, LDS_BYTES);
  fused<<<256, 256, LDS_BYTES, stream>>>(p);
  k_ten<0><<<256, 256, 0, stream>>>((char*)d_ws);
}
```

```cpp
#include <hip/hip_runtime.h>
#include <type_traits>
#define SPIN_SLEEP 4
#define FSTR 64
#include <stdint.h>

typedef unsigned short u16;
typedef unsigned v4u __attribute__((ext_vector_type(4)));
typedef unsigned v2u __attribute__((ext_vector_type(2)));
typedef float f32x4 __attribute__((ext_vector_type(4)));
typedef float f32x2 __attribute__((ext_vector_type(2)));
typedef __bf16 bf16x8 __attribute__((ext_vector_type(8)));
typedef __bf16 bf16x2 __attribute__((ext_vector_type(2)));
typedef short v4s __attribute__((ext_vector_type(4)));
typedef short v8s __attribute__((ext_vector_type(8)));
#define GAS __attribute__((address_space(1)))
#define LAS __attribute__((address_space(3)))
#define DEV __device__ __forceinline__
typedef __amdgpu_buffer_rsrc_t rsrc_t;

constexpr int S = 2048, D = 640, F = 1536;

template <int N> DEV void waitv() { asm volatile("s_waitcnt vmcnt(%0)" ::"i"(N) : "memory"); }
template <int N> DEV void waitl() { asm volatile("s_waitcnt lgkmcnt(%0)" ::"i"(N) : "memory"); }
DEV void sbar() { asm volatile("s_barrier" ::: "memory"); }
#define SCHED() __builtin_amdgcn_sched_barrier(0)
DEV rsrc_t mkrs(const void* p, unsigned bytes) { return __builtin_amdgcn_make_buffer_rsrc((void*)p, 0, bytes, 0x00020000); }
DEV f32x4 mfma(v4u a, v4u b, f32x4 c) {
  return __builtin_amdgcn_mfma_f32_16x16x32_bf16(__builtin_bit_cast(bf16x8, a), __builtin_bit_cast(bf16x8, b), c, 0, 0, 0);
}
DEV v4u lds128(LAS const char* p) { return *(LAS const v4u*)p; }
DEV v2u lds64(LAS const char* p) { return *(LAS const v2u*)p; }
DEV v2u ldstr(LAS const char* p) { return __builtin_bit_cast(v2u, __builtin_amdgcn_ds_read_tr16_b64_v4i16((LAS v4s*)p)); }
DEV v4u cat(v2u a, v2u b) { return v4u{a.x, a.y, b.x, b.y}; }
DEV unsigned pk2(float a, float b) { bf16x2 r = __builtin_convertvector(f32x2{a, b}, bf16x2); return __builtin_bit_cast(unsigned, r); }
DEV float blo(unsigned d) { return __builtin_bit_cast(float, d << 16); }
DEV float bhi(unsigned d) { return __builtin_bit_cast(float, d & 0xffff0000u); }
DEV void setm0(unsigned v) { asm volatile("s_mov_b32 m0, %0\n\ts_nop 0" ::"s"(v)); }
template <int OFF, int NT = 0> DEV void dma16(unsigned voff, rsrc_t rs, unsigned soff) {
  if constexpr (NT) asm volatile("buffer_load_dwordx4 %0, %1, %2 offen offset:%3 nt lds" ::"v"(voff), "s"(rs), "s"(soff), "i"(OFF));
  else asm volatile("buffer_load_dwordx4 %0, %1, %2 offen offset:%3 lds" ::"v"(voff), "s"(rs), "s"(soff), "i"(OFF));
}
typedef float f32x16 __attribute__((ext_vector_type(16)));
DEV f32x16 mfma32(v4u a, v4u b, f32x16 c) {
  return __builtin_amdgcn_mfma_f32_32x32x16_bf16(__builtin_bit_cast(bf16x8, a), __builtin_bit_cast(bf16x8, b), c, 0, 0, 0);
}
template <int IMM> DEV void setm0_add(unsigned base) { asm volatile("s_add_u32 m0, %0, %1" ::"s"(base), "i"(IMM) : "scc"); }
template <int I, int N, class Fn> DEV void static_for(Fn&& f) { if constexpr (I < N) { f(std::integral_constant<int, I>{}); static_for<I + 1, N>(f); } }
DEV void mfma_ip(f32x4& c, const v4u& a, const v4u& b) { asm volatile("v_mfma_f32_16x16x32_bf16 %0, %1, %2, %0" : "+v"(c) : "v"(a), "v"(b)); }
DEV void mfma_ipA(f32x4& c, const v4u& a, const v4u& b) { asm volatile("v_mfma_f32_16x16x32_bf16 %0, %1, %2, %0" : "+v"(c) : "a"(a), "v"(b)); }
DEV void mfma_z(f32x4& c, const v4u& a, const v4u& b) { asm volatile("v_mfma_f32_16x16x32_bf16 %0, %1, %2, 0" : "=&v"(c) : "v"(a), "v"(b)); }
DEV void mfma_zA(f32x4& c, const v4u& a, const v4u& b) { asm volatile("v_mfma_f32_16x16x32_bf16 %0, %1, %2, 0" : "=&v"(c) : "a"(a), "v"(b)); }
template <int AUX> DEV void ldA(v4u& d, unsigned voff, rsrc_t rs, unsigned soff) {
  if constexpr (AUX) asm volatile("buffer_load_dwordx4 %0, %1, %2, %3 offen sc1" : "=a"(d) : "v"(voff), "s"(rs), "s"(soff));
  else asm volatile("buffer_load_dwordx4 %0, %1, %2, %3 offen" : "=a"(d) : "v"(voff), "s"(rs), "s"(soff));
}
template <int AUX> DEV void ldV(v4u& d, unsigned voff, rsrc_t rs, unsigned soff) {
  if constexpr (AUX == 2) asm volatile("buffer_load_dwordx4 %0, %1, %2, %3 offen nt" : "=v"(d) : "v"(voff), "s"(rs), "s"(soff));
  else if constexpr (AUX == 16) asm volatile("buffer_load_dwordx4 %0, %1, %2, %3 offen sc1" : "=v"(d) : "v"(voff), "s"(rs), "s"(soff));
  else asm volatile("buffer_load_dwordx4 %0, %1, %2, %3 offen" : "=v"(d) : "v"(voff), "s"(rs), "s"(soff));
}
DEV void nop8() { asm volatile("s_nop 7"); }

constexpr size_t WS_CTRL = 0;
constexpr size_t WS_KVT = 262144;
constexpr size_t WS_YT = WS_KVT + (size_t)D * S * 2;
constexpr size_t WS_W2T = WS_YT + (size_t)D * S * 2;
constexpr size_t WS_HF = WS_W2T + (size_t)8 * 48 * 5 * 1024;
constexpr size_t WS_G = WS_HF + (size_t)128 * 48 * 1024;
constexpr size_t WS_END = WS_G + S * 4;
constexpr unsigned C_EPOCH = 0, C_TMO = 256;
constexpr unsigned FS = FSTR;
constexpr unsigned C_FW = 4096;
constexpr unsigned C_FKV = C_FW + 64 * FS;
constexpr unsigned C_FH = C_FKV + 64 * FS;
constexpr unsigned C_FY = C_FH + 768 * FS;
constexpr unsigned C_BYTES = 262144;
static_assert(C_FY + 1024 * FS <= C_BYTES, "");

#define NSTAMP 16
#define STAMP_OFF (size_t)(32u << 20)
#define STAMP_DECL
#define STAMP(i)
#define STAMP_FLUSH()
#define FT_DECL
#define FT(i)
#define FT_FLUSH()

struct Params {
  const u16 *q, *k, *v, *sk, *h, *Dv, *w1, *w2, *gw;
  u16* out;
  char* ws;
};

constexpr int LDS_BYTES = 160 * 1024;
constexpr int SC1 = 16;
#define NT_X 0
#define NT_W1 0
#define NT_P 0
constexpr int AUXP = NT_P ? 2 : 0;
#define IC(n) std::integral_constant<int, (n)>{}

DEV void ld32_sc1(unsigned& v, unsigned voff, rsrc_t rs) { asm volatile("buffer_load_dword %0, %1, %2, 0 offen sc1" : "=v"(v) : "v"(voff), "s"(rs)); }
DEV void st32_sc1(unsigned v, unsigned voff, rsrc_t rs) { asm volatile("buffer_store_dword %0, %1, %2, 0 offen sc1" ::"v"(v), "v"(voff), "s"(rs)); }
DEV void poll_finish(unsigned v, unsigned voff, rsrc_t rsC, unsigned tag) {
  asm volatile("" : "+v"(v));
  if (__builtin_amdgcn_ballot_w64(v != tag) == 0) return;
#pragma unroll 1
  for (int spins = 0; spins < (1 << 15); ++spins) {
    __builtin_amdgcn_s_sleep(SPIN_SLEEP);
    ld32_sc1(v, voff, rsC);
    asm volatile("s_waitcnt vmcnt(0)" : "+v"(v)::"memory");
    if (__builtin_amdgcn_ballot_w64(v != tag) == 0) return;
  }
  st32_sc1(1u, C_TMO, rsC);
}

DEV void poll_finish2(unsigned a, unsigned oa, unsigned b, unsigned ob, rsrc_t rsC, unsigned tag) {
  asm volatile("" : "+v"(a), "+v"(b));
  if (__builtin_amdgcn_ballot_w64(a != tag || b != tag) == 0) return;
#pragma unroll 1
  for (int spins = 0; spins < (1 << 15); ++spins) {
    __builtin_amdgcn_s_sleep(SPIN_SLEEP);
    ld32_sc1(a, oa, rsC); ld32_sc1(b, ob, rsC);
    asm volatile("s_waitcnt vmcnt(0)" : "+v"(a), "+v"(b)::"memory");
    if (__builtin_amdgcn_ballot_w64(a != tag || b != tag) == 0) return;
  }
  st32_sc1(1u, C_TMO, rsC);
}

DEV void mfma32_ip(f32x16& c, const v4u& a, const v4u& b) { asm volatile("v_mfma_f32_32x32x16_bf16 %0, %1, %2, %0" : "+v"(c) : "v"(a), "v"(b)); }
DEV void mfma32_z(f32x16& c, const v4u& a, const v4u& b) { asm volatile("v_mfma_f32_32x32x16_bf16 %0, %1, %2, 0" : "=&v"(c) : "v"(a), "v"(b)); }

constexpr int KV_BASE = 32768, KV_GS = 1152, KV_V = 9 * KV_GS, KV_CG = 17 * KV_GS;
constexpr int CV_BASE = 32768;
constexpr int CV_HC = 4160;
constexpr int CV_U = 4 * CV_HC;
constexpr int CV_CH = CV_U + 4608;
constexpr int CV_EX = CV_BASE + 4 * CV_CH;
constexpr int G2_PART = 0;
constexpr int G2_Y = 81920;
constexpr int HW = 24;

template <int AUX> DEV void ldV2(v2u& d, unsigned voff, rsrc_t rs, unsigned soff) {
  if constexpr (AUX == 2) asm volatile("buffer_load_dwordx2 %0, %1, %2, %3 offen nt" : "=v"(d) : "v"(voff), "s"(rs), "s"(soff));
  else if constexpr (AUX == 16) asm volatile("buffer_load_dwordx2 %0, %1, %2, %3 offen sc1" : "=v"(d) : "v"(voff), "s"(rs), "s"(soff));
  else asm volatile("buffer_load_dwordx2 %0, %1, %2, %3 offen" : "=v"(d) : "v"(voff), "s"(rs), "s"(soff));
}
DEV void dma16s(unsigned voff, rsrc_t rs, unsigned soff, unsigned m0v) {
  if constexpr (NT_P) asm volatile("s_mov_b32 m0, %3\n\ts_nop 0\n\tbuffer_load_dwordx4 %0, %1, %2 offen nt lds" ::"v"(voff), "s"(rs), "s"(soff), "s"(m0v));
  else asm volatile("s_mov_b32 m0, %3\n\ts_nop 0\n\tbuffer_load_dwordx4 %0, %1, %2 offen lds" ::"v"(voff), "s"(rs), "s"(soff), "s"(m0v));
}
DEV float dot2(unsigned a, unsigned b, float c) { return __builtin_amdgcn_fdot2_f32_bf16(__builtin_bit_cast(bf16x2, a), __builtin_bit_cast(bf16x2, b), c, false); }
DEV unsigned mktag(unsigned& epoch) {
  asm volatile("" : "+v"(epoch));
  return __builtin_amdgcn_readfirstlane(epoch * 0x9E3779B1u + 0x7F4A7C15u);
}

template <int NR, class ST> DEV void kv_math(const v4u (&kk)[NR + 3], const v4u (&vv)[NR], const v4u (&ww)[4], ST&& st) {
#pragma unroll
  for (int cp = 0; cp < 4; ++cp) {
    f32x2 w[4];
    w[0] = f32x2{blo(ww[cp].x), blo(ww[cp].z)}; w[1] = f32x2{bhi(ww[cp].x), bhi(ww[cp].z)};
    w[2] = f32x2{blo(ww[cp].y), blo(ww[cp].w)}; w[3] = f32x2{bhi(ww[cp].y), bhi(ww[cp].w)};
    f32x2 kf[NR + 3];
#pragma unroll
    for (int i = 0; i < NR + 3; ++i) kf[i] = f32x2{blo(kk[i][cp]), bhi(kk[i][cp])};
    f32x2 r[NR];
#pragma unroll
    for (int i = 0; i < NR; ++i) {
      f32x2 a = kf[i + 3] * w[0];
      a = __builtin_elementwise_fma(kf[i + 2], w[1], a);
      a = __builtin_elementwise_fma(kf[i + 1], w[2], a);
      a = __builtin_elementwise_fma(kf[i], w[3], a);
      r[i] = a * f32x2{blo(vv[i][cp]), bhi(vv[i][cp])};
    }
    st(cp, r);
  }
}

#define Q0 7
#define ROT 0
#define PREP_FIRST 0
#define HPUB 90
#define WPRE_ 16
constexpr int WPRE = WPRE_;
constexpr int HW0 = 16, HWM = 24;
constexpr int PW_BASE = 131072, PW_SZ = 3072;
constexpr int g2_issued(int i) { int r = HW0; for (int s = 0; s < i; ++s) r += (r - s < HWM) ? ((r + 1 - s < HWM) ? 2 : 1) : 0; return r < 48 ? r : 48; }

template <int OFF, int AUX> DEV void ldVo(v4u& d, unsigned voff, rsrc_t rs, unsigned soff) {
  static_assert(AUX == 16, "");
  asm volatile("buffer_load_dwordx4 %0, %1, %2, %3 offen offset:%4 sc1" : "=v"(d) : "v"(voff), "s"(rs), "s"(soff), "i"(OFF));
}
template <int OFF> DEV void ldAo(v4u& d, unsigned voff, rsrc_t rs, unsigned soff) {
  asm volatile("buffer_load_dwordx4 %0, %1, %2, %3 offen offset:%4 sc1" : "=a"(d) : "v"(voff), "s"(rs), "s"(soff), "i"(OFF));
}

template <bool G1R>
DEV void body(const Params& p, LAS char* lds, const int x, const int r, const int wave, const int lane) {
  const int g = lane >> 4, q4 = (lane >> 2) & 3, p4 = lane & 3, c16 = lane & 15;
  STAMP_DECL; FT_DECL;
  STAMP(0); FT(13);
  const rsrc_t rsC = mkrs(p.ws + WS_CTRL, C_BYTES);
  unsigned epoch, tag;
  ld32_sc1(epoch, C_EPOCH, rsC);
  const int cl = G1R ? (wave >> 1) : wave, half = G1R ? (wave & 1) : 0;
  const int ch = (G1R ? 80 * x + 2 * r : 80 * x + 48 + 4 * (r - 24)) + cl;
  const unsigned cb = CV_BASE + cl * CV_CH;
  constexpr int NCHK = G1R ? 2 : 4;
  unsigned dv;
  { const rsrc_t rsD = mkrs(p.Dv, D * 2); asm volatile("buffer_load_ushort %0, %1, %2, 0 offen" : "=v"(dv) : "v"(ch * 2), "s"(rsD)); }
  const rsrc_t rsHh = mkrs(p.h, D * S * 2), rsU = mkrs(p.ws + WS_KVT, D * S * 2), rsY = mkrs(p.ws + WS_YT, D * S * 2);
  const rsrc_t rsW2 = mkrs(p.ws + WS_W2T, 8 * 48 * 5 * 1024), rsH = mkrs(p.ws + WS_HF, 128 * 48 * 1024);
#define MAP42 1
  const int rb = MAP42 ? 8 * (x >> 1) + (r & 7) : 4 * x + (r & 3), nt = MAP42 ? 4 * (x & 1) + (r >> 3) : r >> 2;
  const unsigned vl = lane * 16;
  const unsigned sW = (nt * 48 + 12 * wave) * 5 * 1024;
  const unsigned foW = C_FW + lane * FS, foKV = C_FKV + lane * FS;
  v4u W[12][5];
  auto ldW = [&](auto I) { constexpr int i = I.value; if constexpr (i >= 0 && i < 60) ldAo<(i & 3) * 1024>(W[i / 5][i % 5], vl, rsW2, sW + (i & ~3) * 1024); };
  v4u hc[NCHK], uc[NCHK]; v2u hp0;
  auto ld_h = [&]() {
    ldV2<AUXP>(hp0, half > 0 ? (unsigned)(ch * 4096 + half * NCHK * 1024 - 8) : 0x80000000u, rsHh, 0);
#pragma unroll
    for (int i = 0; i < NCHK; ++i) ldV<AUXP>(hc[i], ch * 4096 + ((half * NCHK + i) * 64 + lane) * 16, rsHh, 0);
  };
  auto ld_u = [&]() {
#pragma unroll
    for (int i = 0; i < NCHK; ++i) ldV<SC1>(uc[i], ch * 4096 + ((half * NCHK + i) * 64 + lane) * 16, rsU, 0);
  };
  auto fill_h = [&]() {
    asm volatile("" : "+v"(hp0));
#pragma unroll
    for (int i = 0; i < NCHK; ++i) asm volatile("" : "+v"(hc[i]));
    unsigned e0 = hp0.x, e1 = hp0.y;
#pragma unroll
    for (int i = 0; i < NCHK; ++i) {
      const int n = (half * NCHK + i) * 64 + lane;
      const v4u cur = hc[i];
      const v2u prv = v2u{(unsigned)__builtin_amdgcn_update_dpp((int)e0, (int)cur.z, 0x138, 0xf, 0xf, false), (unsigned)__builtin_amdgcn_update_dpp((int)e1, (int)cur.w, 0x138, 0xf, 0xf, false)};
      e0 = __builtin_amdgcn_readlane(cur.z, 63); e1 = __builtin_amdgcn_readlane(cur.w, 63);
      const unsigned om2 = __builtin_amdgcn_alignbit(prv.y, prv.x, 16), om1 = __builtin_amdgcn_alignbit(cur.x, prv.y, 16);
      const unsigned o0 = __builtin_amdgcn_alignbit(cur.y, cur.x, 16), o1 = __builtin_amdgcn_alignbit(cur.z, cur.y, 16), o2 = __builtin_amdgcn_alignbit(cur.w, cur.z, 16);
      LAS char* dst = lds + cb + (n + 2) * 16;
      *(LAS v4u*)(dst) = cur;
      *(LAS v4u*)(dst + CV_HC) = v4u{om1, o0, o1, o2};
      *(LAS v4u*)(dst + 2 * CV_HC) = v4u{prv.y, cur.x, cur.y, cur.z};
      *(LAS v4u*)(dst + 3 * CV_HC) = v4u{om2, om1, o0, o1};
    }
    if (half == 0) {
      if (lane < 8) *(LAS v4u*)(lds + cb + (lane >> 1) * CV_HC + (lane & 1) * 16) = v4u{0, 0, 0, 0};
      if (lane >= 32 && lane < 62) *(LAS v4u*)(lds + cb + CV_U + (lane - 32) * 16) = v4u{0, 0, 0, 0};
    }
  };
  auto fill_u = [&]() {
#pragma unroll
    for (int i = 0; i < NCHK; ++i) {
      asm volatile("" : "+v"(uc[i]));
      *(LAS v4u*)(lds + cb + CV_U + 480 + ((half * NCHK + i) * 64 + lane) * 16) = uc[i];
    }
  };
  unsigned fw;

  if constexpr (G1R) {
    const int sb = 4 * (x >> 1) + (r & 3), fs = 6 * (x & 1) + (r >> 2);
    f32x16 acc1[2][2];
    const rsrc_t rsX = mkrs(p.q, S * D * 2), rsW = mkrs(p.w1, D * F * 2);
    unsigned vX[4], vW[4];
#pragma unroll
    for (int ii = 0; ii < 4; ++ii) {
      vX[ii] = (32 * wave + 8 * ii + (lane >> 3)) * 1280 + (((lane & 7) ^ (4 * (ii & 1) + g)) << 4) - 1024 * ii;
      vW[ii] = (16 * wave + 4 * ii + g) * 3072 + (((((c16 >> 1) ^ (g << 1)) << 1) | (c16 & 1)) << 4) - 1024 * ii;
    }
    const unsigned sX0 = sb * 128 * 1280, sW0 = fs * 256;
    const unsigned mX = wave * 4096, mW = 16384 + wave * 4096;
    const int rot = ROT ? (2 * (r >> 2) + 3 * (r & 3)) % 10 : 0;
    auto kslice = [&](int t) { const int k = t + rot; return k >= 10 ? k - 10 : k; };
    unsigned sq;
    auto grp_begin = [&](auto Q) { constexpr int q = Q.value, t = q >> 1;
      if constexpr (q < 20) { sq = (q & 1) ? sW0 + kslice(t) * (64 * 3072) : sX0 + kslice(t) * 128; setm0_add<(t % 5) * 32768>((q & 1) ? mW : mX); } };
    auto grp_dma = [&](auto Q, auto I) { constexpr int q = Q.value, i = I.value;
      if constexpr (q < 20) { if constexpr (q & 1) dma16<1024 * i, NT_W1>(vW[i], rsW, sq); else dma16<1024 * i, NT_X>(vX[i], rsX, sq); } };
    FT(10);
    static_for<0, Q0>([&](auto Q) { grp_begin(Q); asm volatile("s_nop 0"); static_for<0, 4>([&](auto I) { grp_dma(Q, I); }); });
    FT(11);
    const int ws_ = wave & 1, wf = wave >> 1, h = lane >> 5, r32 = lane & 31;
    unsigned aX[3][4], aW[3][2];
#pragma unroll
    for (int w = 0; w < 3; ++w) {
#pragma unroll
      for (int ks = 0; ks < 4; ++ks) aX[w][ks] = w * 65536 + (64 * ws_ + r32) * 128 + (((2 * ks + h) ^ ((r32 >> 1) & 7)) << 4);
#pragma unroll
      for (int ft = 0; ft < 2; ++ft) aW[w][ft] = w * 65536 + 16384 + (8 * h + q4) * 256 + (((2 * (2 * wf + ft) + (g & 1)) ^ (q4 << 1)) << 5) + 8 * p4;
    }
    v4u A[2][2][2], B[2][2][2];
    v2u At;
    auto rd = [&](auto T, auto HF, auto SET, auto N) {
      constexpr int t = T.value, hf = HF.value, set = SET.value, n = N.value;
      if constexpr (t < 10) {
        constexpr int b = t % 5, w = b >> 1, bo = (b & 1) * 32768;
        constexpr int kk = n / 6, rr = n % 6, ks = 2 * hf + kk;
        if constexpr (rr == 0) B[set][kk][0] = lds128(lds + aX[w][ks] + bo);
        else if constexpr (rr == 3) B[set][kk][1] = lds128(lds + aX[w][ks] + bo + 32 * 128);
        else { constexpr int ft = rr / 3, jj = (rr % 3) - 1;
          v2u xx = ldstr(lds + aW[w][ft] + bo + ks * 4096 + jj * 1024);
          if constexpr (jj == 0) At = xx; else A[set][kk][ft] = cat(At, xx); }
      }
    };

    unsigned fk;
    waitv<4 * (Q0 - 2)>(); sbar();
    STAMP(1);
    static_for<0, 12>([&](auto N) { rd(IC(0), IC(0), IC(0), N); });
    SCHED();
    static_for<0, 10>([&](auto T) {
      constexpr int t = T.value;
      static_for<0, 2>([&](auto HF) {
        constexpr int hf = HF.value, hi = 2 * t + hf, q = Q0 + hi;
        static_for<0, 8>([&](auto M) {
          constexpr int m = M.value, kk = m >> 2, ft = (m >> 1) & 1, st = m & 1;
          if constexpr (t == 0 && hf == 0 && kk == 0) mfma32_z(acc1[ft][st], A[hf][kk][ft], B[hf][kk][st]);
          else mfma32_ip(acc1[ft][st], A[hf][kk][ft], B[hf][kk][st]);
          if constexpr (m < 6) {
            if constexpr (hf == 0) { rd(T, IC(1), IC(1), IC(2 * m)); rd(T, IC(1), IC(1), IC(2 * m + 1)); }
            else { rd(IC(t + 1), IC(0), IC(0), IC(2 * m)); rd(IC(t + 1), IC(0), IC(0), IC(2 * m + 1)); }
          }
          if constexpr (m == 0) grp_begin(IC(q));
          if constexpr (m & 1) grp_dma(IC(q), IC(m >> 1));
          SCHED();
        });
        if constexpr (hf == 0 && t < 9) {
          constexpr int lastq = (Q0 + 2 * t < 19) ? Q0 + 2 * t : 19;
          constexpr int younger = 4 * (lastq - (2 * t + 3) > 0 ? lastq - (2 * t + 3) : 0);
          waitv<younger>(); sbar(); SCHED();
        }
      });
      FT(t); SCHED();
    });
    asm volatile("s_nop 15" : "+v"(acc1[0][0]), "+v"(acc1[0][1]), "+v"(acc1[1][0]), "+v"(acc1[1][1]));
    sbar();
    STAMP(2);
    tag = mktag(epoch);
    ld_h();
    const unsigned vo = (g & 1) * (48 * 1024) + (c16 + 16 * h) * 16;
    static_for<0, 4>([&](auto Q) {
      constexpr int qd = Q.value, st = qd >> 1, ft = qd & 1;
      const f32x16 a = acc1[ft][st];
      unsigned o[8];
      static_for<0, 2>([&](auto G) {
        constexpr int gq = G.value;
        static_for<0, 4>([&](auto E) {
          constexpr int e = 8 * gq + 2 * E.value;
          const float r0 = a[e] * __builtin_amdgcn_rcpf(1.f + __builtin_amdgcn_exp2f(a[e] * -1.44269504088896f));
          const float r1 = a[e + 1] * __builtin_amdgcn_rcpf(1.f + __builtin_amdgcn_exp2f(a[e + 1] * -1.44269504088896f));
          o[e >> 1] = pk2(r0, r1);
        });
        SCHED();
      });
      const unsigned so = ((8 * sb + 4 * ws_ + 2 * st) * 48 + 4 * fs + 2 * wf + ft) * 1024;
      __builtin_amdgcn_raw_buffer_store_b128(v4u{o[0], o[1], o[4], o[5]}, rsH, vo, so, SC1);
      __builtin_amdgcn_raw_buffer_store_b128(v4u{o[2], o[3], o[6], o[7]}, rsH, vo + 512, so, SC1);
      SCHED();
      FT(32 + qd); SCHED();
    });
    ld32_sc1(fk, foKV, rsC); ld32_sc1(fw, foW, rsC);
    waitv<10>();
    fill_h();
    FT(54);
    waitv<0>();
    if (lane == 0) st32_sc1(tag, C_FH + ((sb * 2 + ws_) * 24 + fs * 2 + wf) * FS, rsC);
    FT(55);
    poll_finish(fk, foKV, rsC, tag);
    ld_u();
    FT(36);
    poll_finish(fw, foW, rsC, tag);
    FT(37);
    static_for<0, WPRE>([&](auto I) { ldW(I); });
    STAMP(3);
    waitv<WPRE>();
    fill_u();
    waitl<0>(); sbar();
    STAMP(4);
  } else {
    const int j = (r - 24) * 8 + x, rbk = j >> 1, cg0 = 5 * (j & 1);
    const rsrc_t rsK = mkrs(p.k, S * D * 2), rsV = mkrs(p.v, S * D * 2), rsS = mkrs(p.sk, D * 8), rsO = mkrs(p.ws + WS_KVT, D * S * 2);
    const unsigned vr = (lane >> 3) * 1280 + ((lane & 7) << 4), vr4 = vr ^ 64;
    auto dmaK = [&](int cgl, int G) {
      const int row = 64 * rbk - 8 + 8 * G;
      dma16s(((G >> 1) & 1) ? vr4 : vr, rsK, row >= 0 ? (unsigned)(row * 1280 + (cg0 + cgl) * 128) : 0x80000000u, KV_BASE + cgl * KV_CG + G * KV_GS);
    };
    auto dmaV = [&](int cgl, int G) {
      dma16s(((G >> 1) & 1) ? vr4 : vr, rsV, (64 * rbk + 8 * G) * 1280 + (cg0 + cgl) * 128, KV_BASE + cgl * KV_CG + KV_V + G * KV_GS);
    };
#pragma unroll
    for (int G = 0; G < 9; ++G) dmaK(wave, G);
#pragma unroll
    for (int G = 0; G < 8; ++G) dmaV(wave, G);
    const unsigned pw = PW_BASE + wave * PW_SZ;
    dma16s(lane < 32 ? (unsigned)((cg0 + wave) * 512 + lane * 16) : (lane < 40 ? (unsigned)((64 * (cg0 + 4) + 16 * wave) * 8 + (lane - 32) * 16) : 0x80000000u), rsS, 0, pw);
    dmaK(4, wave); dmaK(4, wave + 4); dmaV(4, wave); dmaV(4, wave + 4);
    if (wave == 0) dmaK(4, 8);
    const int cc = lane >> 3, rg = lane & 7;
    FT(10);
    waitv<4>();
    STAMP(1);
    {
      const unsigned kb = KV_BASE + wave * KV_CG;
      const unsigned a0 = kb + rg * KV_GS + ((cc ^ (4 * ((rg >> 1) & 1))) << 4), a1 = kb + (rg + 1) * KV_GS + ((cc ^ (4 * (((rg + 1) >> 1) & 1))) << 4);
      v4u kk[11], vv[8], ww[4];
#pragma unroll
      for (int i = 0; i < 4; ++i) ww[i] = lds128(lds + pw + cc * 64 + i * 16);
#pragma unroll
      for (int i = 0; i < 11; ++i) kk[i] = i < 3 ? lds128(lds + a0 + (5 + i) * 128) : lds128(lds + a1 + (i - 3) * 128);
#pragma unroll
      for (int i = 0; i < 8; ++i) vv[i] = lds128(lds + a0 + KV_V + i * 128);
      const unsigned ob = (64 * (cg0 + wave) + 8 * cc) * 4096 + (64 * rbk + 8 * rg) * 2;
      kv_math<8>(kk, vv, ww, [&](int cp, const f32x2 (&rr)[8]) {
#pragma unroll
        for (int c = 0; c < 2; ++c) {
          v4u o{pk2(rr[0][c], rr[1][c]), pk2(rr[2][c], rr[3][c]), pk2(rr[4][c], rr[5][c]), pk2(rr[6][c], rr[7][c])};
          __builtin_amdgcn_raw_buffer_store_b128(o, rsO, ob + (2 * cp + c) * 4096, 0, SC1);
        }
      });
    }
    const int id = 4 * j + wave, pk = id / 5, blk = id % 5;
    const rsrc_t rsWs = mkrs(p.w2, F * D * 2);
    const unsigned ldsb = wave * 8192;
#pragma unroll
    for (int i = 0; i < 8; ++i) {
      const int row = 4 * i + g;
      dma16s((32 * pk + row) * 1280 + blk * 256 + (((((c16 >> 1) ^ (row & 7)) << 1) | (c16 & 1)) << 4), rsWs, id < 240 ? 0u : 0x80000000u, ldsb + i * 1024);
    }
    waitv<16>(); sbar();
    STAMP(2);
    {
      const int cc4 = 2 * wave + (lane >> 5), rp = lane & 31;
      const unsigned kb = KV_BASE + 4 * KV_CG;
      v4u kk[5], vv[2], w4[4];
#pragma unroll
      for (int i = 0; i < 4; ++i) w4[i] = lds128(lds + pw + 512 + (lane >> 5) * 64 + i * 16);
#pragma unroll
      for (int i = 0; i < 5; ++i) { const int row = 2 * rp + 5 + i; kk[i] = lds128(lds + kb + (row >> 3) * KV_GS + (row & 7) * 128 + ((cc4 ^ (4 * ((row >> 4) & 1))) << 4)); }
#pragma unroll
      for (int i = 0; i < 2; ++i) { const int row = 2 * rp + i; vv[i] = lds128(lds + kb + KV_V + (row >> 3) * KV_GS + (row & 7) * 128 + ((cc4 ^ (4 * ((row >> 4) & 1))) << 4)); }
      const unsigned ob = (64 * (cg0 + 4) + 8 * cc4) * 4096 + (64 * rbk + 2 * rp) * 2;
      kv_math<2>(kk, vv, w4, [&](int cp, const f32x2 (&rr)[2]) {
#pragma unroll
        for (int c = 0; c < 2; ++c) __builtin_amdgcn_raw_buffer_store_b32(pk2(rr[0][c], rr[1][c]), rsO, ob + (2 * cp + c) * 4096, 0, SC1);
      });
    }
    FT(11);
    waitv<8>();
    {
      const rsrc_t rsWo = mkrs(p.ws + WS_W2T, 8 * 48 * 5 * 1024);
      const int r0 = 4 * g + q4;
#pragma unroll
      for (int i = 0; i < 8; ++i) {
        const unsigned a = ldsb + r0 * 256 + ((i ^ (r0 & 7)) << 5) + 8 * p4;
        const v4u fr = cat(ldstr(lds + a), ldstr(lds + a + 4096));
        const int ctg = 8 * blk + i, ntt = ctg / 5, ct = ctg % 5;
        __builtin_amdgcn_raw_buffer_store_b128(fr, rsWo, vl, id < 240 ? (unsigned)(((ntt * 48 + pk) * 5 + ct) * 1024) : 0x80000000u, SC1);
      }
    }
    FT(0);
    waitv<8>();
    waitl<0>(); sbar();
    tag = mktag(epoch);
    if (threadIdx.x == 0) st32_sc1(tag, C_FKV + j * FS, rsC);
    STAMP(3);
    ld_h();
    waitv<5>(); sbar();
    if (threadIdx.x == 0) st32_sc1(tag, C_FW + j * FS, rsC);
    unsigned fk;
    ld32_sc1(fk, foKV, rsC);
    FT(3);
    waitv<1>();
    waitv<0>();
    fill_h();
    FT(2);
    poll_finish(fk, foKV, rsC, tag);
    FT(4);
    ld_u();
    const rsrc_t rsX = mkrs(p.q, S * D * 2), rsG = mkrs(p.gw, D * 2), rsGo = mkrs(p.ws + WS_G, S * 4);
    const int grow = 256 * x + 32 * (r - 24) + 8 * wave + (lane >> 3), part = lane & 7;
    v4u xx[10];
    dma16s(lane * 16, rsG, 0, pw + 1024); dma16s(1024 + lane * 16, rsG, 0, pw + 2048);
#pragma unroll
    for (int i = 0; i < 10; ++i) ldV<0>(xx[i], grow * 1280 + i * 128 + part * 16, rsX, 0);
    waitv<12>();
    fill_u();
    ld32_sc1(fw, foW, rsC);
    waitv<1>();
    {
      float a0 = 0.f, a1 = 0.f;
#pragma unroll
      for (int i = 0; i < 10; ++i) {
        asm volatile("" : "+v"(xx[i]));
        const v4u gg = lds128(lds + pw + 1024 + (i * 8 + part) * 16);
        a0 = dot2(xx[i].x, gg.x, a0); a1 = dot2(xx[i].y, gg.y, a1); a0 = dot2(xx[i].z, gg.z, a0); a1 = dot2(xx[i].w, gg.w, a1);
      }
      float a = a0 + a1;
      a += __shfl_xor(a, 1); a += __shfl_xor(a, 2); a += __shfl_xor(a, 4);
      const float gt = __builtin_amdgcn_rcpf(1.f + __builtin_amdgcn_exp2f(a * -1.44269504088896f));
      if (part == 0) __builtin_amdgcn_raw_buffer_store_b32(__builtin_bit_cast(unsigned, gt), rsGo, grow * 4, 0, SC1);
    }
    FT(5);
    waitl<0>();
    STAMP(4);
  }
  STAMP(5);
  constexpr int NZ = G1R ? 4 : 8;
  constexpr int W0 = G1R ? WPRE : 0, WG0 = G1R ? 0 : 14;
  auto wcount = [](int gap) constexpr { return gap < WG0 ? 0 : (G1R ? ((gap - WG0) * (60 - WPRE)) / 140 + 1 : (gap - WG0) / 4 + 1); };
  const unsigned foH = C_FH + (((rb >> 1) * 2 + (rb & 1)) * 24 + (lane < 24 ? lane : 0)) * FS;
  unsigned fh;
  constexpr int GPOLL = NZ * 36 - 12;
  f32x4 acc[8];
  {
    const unsigned aA = cb + q4 * CV_HC + 56 - 16 * g + 8 * p4 + 256 * half;
    const unsigned aB = cb + CV_U + 448 + 32 * c16 + 16 * g - 256 * half - 64 * (NZ - 1);
    v4u A[8], B[8];
#pragma unroll
    for (int i = 0; i < 8; ++i) {
      B[7 - i] = lds128(lds + aB + 64 * (NZ - 1) + 512 * (7 - i));
      A[i] = cat(ldstr(lds + aA + 512 * i + 8), ldstr(lds + aA + 512 * i));
    }
    SCHED();
    static_for<0, NZ>([&](auto Z) {
      constexpr int z = Z.value;
      static_for<0, 8>([&](auto I) {
        constexpr int i = I.value;
        static_for<0, 8 - i>([&](auto DD) {
          constexpr int d = 7 - i - DD.value;
          constexpr int gap = z * 36 + (i * (17 - i)) / 2 + DD.value;
          if constexpr (z == 0 && i == 0) mfma_z(acc[i + d], A[i], B[d]); else mfma_ip(acc[i + d], A[i], B[d]);
          if constexpr (d == 7 - i && z + 1 < NZ) B[7 - i] = lds128(lds + aB + 64 * (NZ - 2 - z) + 512 * (7 - i));
          if constexpr (!G1R && gap == WG0 - 2) { waitv<0>(); poll_finish(fw, foW, rsC, tag); }
          if constexpr (wcount(gap) > wcount(gap - 1)) ldW(IC(W0 + wcount(gap) - 1));
          if constexpr (gap == GPOLL) ld32_sc1(fh, foH, rsC);
          SCHED();
        });
        if constexpr (i == 7) { FT(40 + z); SCHED(); }
        if constexpr (z + 1 < NZ) { A[i] = cat(ldstr(lds + aA + 64 * (z + 1) + 512 * i + 8), ldstr(lds + aA + 64 * (z + 1) + 512 * i)); SCHED(); }
      });
    });
  }
  static_assert(W0 + wcount(NZ * 36 - 1) >= 60, "all W2 fragments issued");
  STAMP(6);
  constexpr int WAFT = 60 - (W0 + wcount(GPOLL));
  waitv<(WAFT > 0 ? WAFT : 0)>();
  FT(48);
  poll_finish(fh, foH, rsC, tag);
  FT(49);
  v4u Hf[HWM];
  const unsigned sH = (4 * rb * 48 + 12 * wave) * 1024;
  auto ldH = [&](auto N) { constexpr int n = N.value; if constexpr (n < 48) ldVo<(n & 3) * 1024, SC1>(Hf[n % HWM], vl, rsH, sH + ((n / 12) * 48 + ((n % 12) & ~3)) * 1024); };
  static_for<0, HW0>([&](auto I) { ldH(I); });
  FT(14);
  asm volatile("s_nop 7" : "+v"(acc[0]), "+v"(acc[1]), "+v"(acc[2]), "+v"(acc[3]), "+v"(acc[4]), "+v"(acc[5]), "+v"(acc[6]), "+v"(acc[7]));
  SCHED();
  int ctb = 0;
  if constexpr (G1R) {
    LAS char* ex = lds + CV_EX + cl * 8192;
    f32x4 keep[4];
#pragma unroll
    for (int i = 0; i < 4; ++i) {
      f32x4 lo = acc[i], hi = acc[4 + i];
      asm volatile("" : "+v"(lo), "+v"(hi));
      *(LAS f32x4*)(ex + half * 4096 + i * 1024 + lane * 16) = half == 0 ? hi : lo;
      keep[i] = half == 0 ? lo : hi;
    }
    waitl<0>(); sbar();
#pragma unroll
    for (int i = 0; i < 4; ++i) acc[i] = keep[i] + *(LAS f32x4*)(ex + (1 - half) * 4096 + i * 1024 + lane * 16);
    ctb = 4 * half;
  }
  FT(51);
  constexpr int NT = G1R ? 4 : 8, NYS = NT / 2;
  {
    asm volatile("" : "+v"(dv));
    const float Dc = blo(dv);
#pragma unroll
    for (int i = 0; i < NT; i += 2) {
      unsigned w0[2][2];
#pragma unroll
      for (int e = 0; e < 2; ++e) {
        const int ct = ctb + i + e;
        const f32x4 a = acc[i + e];
        const v2u uu = lds64(lds + cb + CV_U + 480 + 512 * ct + 32 * c16 + 8 * g);
        w0[e][0] = pk2(fmaf(blo(uu.x), Dc, a[0]), fmaf(bhi(uu.x), Dc, a[1]));
        w0[e][1] = pk2(fmaf(blo(uu.y), Dc, a[2]), fmaf(bhi(uu.y), Dc, a[3]));
      }
      auto s0 = __builtin_amdgcn_permlane16_swap(w0[0][0], w0[1][0], false, false);
      auto s1 = __builtin_amdgcn_permlane16_swap(w0[0][1], w0[1][1], false, false);
      const int ct = ctb + i + (g & 1);
      __builtin_amdgcn_raw_buffer_store_b128(v4u{s0[0], s1[0], s0[1], s1[1]}, rsY, ch * 4096 + 512 * ct + 32 * c16 + 8 * (g & 2), 0, SC1);
    }
  }
  FT(52);
  waitl<0>(); sbar();
  STAMP(7);
  f32x4 ac2[2][5];
  const unsigned foY0 = C_FY + (nt * 128 + lane) * FS, foY1 = foY0 + 64 * FS;
  const unsigned foG = C_FY + ((rb >> 2) * 128 + 96 + (lane & 31)) * FS;
  unsigned fy0, fy1, fg, gtu;
  v4u yv[3]; v2u qq[5];
  const int s = 64 * rb + 16 * wave + c16;
#define SF_ 5
  constexpr int SF = SF_;
  constexpr int SP = 25;
  constexpr int SY = 40;
  static_assert(g2_issued(SP) == 48 && SY >= SP + 8, "");
  auto extras_at = [](int st) constexpr { return (st == SF ? 1 : 0) + ((st >= SP && st < SP + 8) ? 1 : 0) + (st == SY ? 4 : 0); };
  static_for<0, 48>([&](auto I) {
    constexpr int i = I.value, rt = i / 12, k = i % 12, set = rt & 1;
    constexpr int younger = [&]() constexpr {
      int y = g2_issued(i) - (i + 1);
      if (i < HW0) y += NYS;
      int si = -1; while (i >= HW0 && g2_issued(si + 1) <= i) ++si;
      for (int st = (si < 0 ? 0 : si); st < i; ++st) y += extras_at(st);
      return y; }();
    waitv<(younger < 63 ? younger : 63)>();
    static_for<0, 5>([&](auto C) { constexpr int ct = C.value;
      if constexpr (k == 0) mfma_zA(ac2[set][ct], W[k][ct], Hf[i % HWM]); else mfma_ipA(ac2[set][ct], W[k][ct], Hf[i % HWM]);
      if constexpr (rt > 0 && k >= 1 && k <= 5 && ct == 3) *(LAS f32x4*)(lds + G2_PART + (((rt - 1) * 4 + wave) * 5 + (k - 1)) * 1024 + lane * 16) = ac2[set ^ 1][k - 1];
      if constexpr (ct == 1) { static_for<g2_issued(i), g2_issued(i + 1)>([&](auto N) { ldH(N); }); }
    });
    if constexpr (i == SF) {
      waitv<g2_issued(SF + 1) - HW0>();
      if (lane == 0) st32_sc1(tag, C_FY + ((x * 32 + r) * 4 + wave) * FS, rsC);
    }
    if constexpr (i == SP) ld32_sc1(fy0, foY0, rsC);
    if constexpr (i == SP + 1) ld32_sc1(fy1, foY1, rsC);
    if constexpr (i == SP + 2) ld32_sc1(fg, foG, rsC);
    if constexpr (i >= SP + 3 && i < SP + 8) { const rsrc_t rsQ = mkrs(p.q, S * D * 2); ldV2<AUXP>(qq[i - SP - 3], (s * D + 80 * nt + 16 * (i - SP - 3) + 4 * g) * 2, rsQ, 0); }
    if constexpr (i == SY) {
      waitv<5>();
      poll_finish2(fy0, foY0, fy1, foY1, rsC, tag);
      poll_finish(fg, foG, rsC, tag);
      { const rsrc_t rsG = mkrs(p.ws + WS_G, S * 4); ld32_sc1(gtu, s * 4, rsG); }
#pragma unroll
      for (int u = 0; u < 3; ++u) {
        const int idx = u * 256 + wave * 64 + lane, d = idx >> 3, cch = idx & 7;
        ldV<SC1>(yv[u], idx < 640 ? (unsigned)((80 * nt + d) * 4096 + rb * 128 + cch * 16) : 0x80000000u, rsY, 0);
      }
    }
    if constexpr (i % 4 == 3) { FT(16 + i / 4); }
    SCHED();
  });
  STAMP(9);
  asm volatile("s_nop 7" : "+v"(ac2[1][0]), "+v"(ac2[1][1]), "+v"(ac2[1][2]), "+v"(ac2[1][3]), "+v"(ac2[1][4]));
#pragma unroll
  for (int ct = 0; ct < 5; ++ct) *(LAS f32x4*)(lds + G2_PART + ((3 * 4 + wave) * 5 + ct) * 1024 + lane * 16) = ac2[1][ct];
  unsigned fe[16];
  const bool bump = blockIdx.x == 0 && wave == 0;
  if (bump) {
#pragma unroll
    for (int i = 0; i < 16; ++i) ld32_sc1(fe[i], C_FY + (i * 64 + lane) * FS, rsC);
  }
  waitv<0>();
#pragma unroll
  for (int u = 0; u < 3; ++u) {
    asm volatile("" : "+v"(yv[u]));
    const int idx = u * 256 + wave * 64 + lane, d = idx >> 3, cch = idx & 7;
    if (idx < 640) { *(LAS v2u*)(lds + G2_Y + d * 136 + cch * 16) = v2u{yv[u].x, yv[u].y}; *(LAS v2u*)(lds + G2_Y + d * 136 + cch * 16 + 8) = v2u{yv[u].z, yv[u].w}; }
  }
  asm volatile("" : "+v"(gtu));
#pragma unroll
  for (int ct = 0; ct < 5; ++ct) asm volatile("" : "+v"(qq[ct]));
  waitl<0>(); sbar();
  STAMP(10);
  const float gt = __builtin_bit_cast(float, gtu);
#pragma unroll
  for (int ct = 0; ct < 5; ++ct) {
    f32x4 b = *(LAS f32x4*)(lds + G2_PART + ((wave * 4 + 0) * 5 + ct) * 1024 + lane * 16);
#pragma unroll
    for (int w = 1; w < 4; ++w) b += *(LAS f32x4*)(lds + G2_PART + ((wave * 4 + w) * 5 + ct) * 1024 + lane * 16);
    const v2u yy = ldstr(lds + G2_Y + (16 * ct + 4 * g + q4) * 136 + (16 * wave + 4 * p4) * 2);
    const float o0 = gt * (blo(qq[ct].x) * blo(yy.x)) + (1.f - gt) * b[0];
    const float o1 = gt * (bhi(qq[ct].x) * bhi(yy.x)) + (1.f - gt) * b[1];
    const float o2 = gt * (blo(qq[ct].y) * blo(yy.y)) + (1.f - gt) * b[2];
    const float o3 = gt * (bhi(qq[ct].y) * bhi(yy.y)) + (1.f - gt) * b[3];
    *(GAS v2u*)(p.out + s * D + 80 * nt + 16 * ct + 4 * g) = v2u{pk2(o0, o1), pk2(o2, o3)};
  }
  if (bump) {
    unsigned bad = 0;
#pragma unroll
    for (int e = 0; e < 16; ++e) { asm volatile("" : "+v"(fe[e])); bad |= fe[e] ^ tag; }
    if (__builtin_amdgcn_ballot_w64(bad != 0) != 0) {
#pragma unroll 1
      for (int e = 0; e < 16; ++e) { const unsigned fo = C_FY + (e * 64 + lane) * FS; unsigned f; ld32_sc1(f, fo, rsC); waitv<0>(); poll_finish(f, fo, rsC, tag); }
    }
    if (lane == 0) st32_sc1(tag, C_EPOCH, rsC);
  }
  STAMP(11);
  STAMP_FLUSH(); FT_FLUSH();
}

__global__ __launch_bounds__(256, 1) void fused(Params p_unused) {
  unsigned long long e_r0, e_r1;
  asm volatile("s_memrealtime %0\n\ts_waitcnt lgkmcnt(0)" : "=s"(e_r0));
  typedef __attribute__((address_space(4))) const Params* kargp;
  kargp kp = (kargp)__builtin_amdgcn_kernarg_segment_ptr();
  asm volatile("" : "+s"(kp) : "s"(e_r0));
  Params p;
  p.q = kp->q; p.k = kp->k; p.v = kp->v; p.sk = kp->sk; p.h = kp->h; p.Dv = kp->Dv; p.w1 = kp->w1; p.w2 = kp->w2; p.gw = kp->gw; p.out = kp->out; p.ws = kp->ws;
  { const char* w_ = p.ws; const u16* q_ = p.q; asm volatile("s_waitcnt lgkmcnt(0)\n\ts_memrealtime %0\n\ts_waitcnt lgkmcnt(0)" : "=s"(e_r1) : "s"(w_), "s"(q_)); }
  extern __shared__ __attribute__((aligned(16))) char smem[];
  LAS char* lds = (LAS char*)smem;
  const int lane = threadIdx.x & 63, wave = __builtin_amdgcn_readfirstlane(threadIdx.x >> 6);
  const int x = blockIdx.x & 7, rp_ = blockIdx.x >> 3, r = PREP_FIRST ? (rp_ < 8 ? rp_ + 24 : rp_ - 8) : rp_;
  unsigned pf = 0;
  {
    unsigned long long pc; unsigned len;
    asm volatile("s_getpc_b64 %0\nLpf_here:\n\ts_mov_b32 %1, Lpf_end-Lpf_here" : "=s"(pc), "=s"(len));
    const unsigned off = (rp_ * 4 + wave) * 1024 + lane * 16;
    const rsrc_t rs = mkrs((const void*)pc, len);
    v4u t;
    asm volatile("buffer_load_dwordx4 %0, %1, %2, 0 offen" : "=v"(t) : "v"(off), "s"(rs));
    if (r < 24) { body<true>(p, lds, x, r, wave, lane); }
    else { body<false>(p, lds, x, r, wave, lane); }
    asm volatile("s_waitcnt vmcnt(0)\nLpf_end:" : "+v"(t));
    pf = t.x;
  }
  asm volatile("" ::"v"(pf));
  if (blockIdx.x == 5) { const unsigned long long d_ = (e_r1 - e_r0) * 0 + 300, e0 = __builtin_amdgcn_s_memrealtime(); while (__builtin_amdgcn_s_memrealtime() - e0 < d_) __builtin_amdgcn_s_sleep(1); }
}

extern "C" void kernel_launch(void* const* d_in, const int* in_sizes, int n_in, void* d_out, int out_size, void* d_ws, size_t ws_size, hipStream_t stream) {
  Params p;
  p.q = (const u16*)d_in[0]; p.k = (const u16*)d_in[1]; p.v = (const u16*)d_in[2]; p.sk = (const u16*)d_in[3];
  p.h = (const u16*)d_in[4]; p.Dv = (const u16*)d_in[5]; p.w1 = (const u16*)d_in[7]; p.w2 = (const u16*)d_in[8]; p.gw = (const u16*)d_in[9];
  p.out = (u16*)d_out; p.ws = (char*)d_ws;
  (void)hipFuncSetAttribute((const void*)fused, hipFuncAttributeMaxDynamicSharedMemorySize, LDS_BYTES);
  fused<<<256, 256, LDS_BYTES, stream>>>(p);
}
```

```cpp
#include <hip/hip_runtime.h>
#include <type_traits>
#define SPIN_SLEEP 4
#define FSTR 64
#include <stdint.h>

typedef unsigned short u16;
typedef unsigned v4u __attribute__((ext_vector_type(4)));
typedef unsigned v2u __attribute__((ext_vector_type(2)));
typedef float f32x4 __attribute__((ext_vector_type(4)));
typedef float f32x2 __attribute__((ext_vector_type(2)));
typedef __bf16 bf16x8 __attribute__((ext_vector_type(8)));
typedef __bf16 bf16x2 __attribute__((ext_vector_type(2)));
typedef short v4s __attribute__((ext_vector_type(4)));
typedef short v8s __attribute__((ext_vector_type(8)));
#define GAS __attribute__((address_space(1)))
#define LAS __attribute__((address_space(3)))
#define DEV __device__ __forceinline__
typedef __amdgpu_buffer_rsrc_t rsrc_t;

constexpr int S = 2048, D = 640, F = 1536;

template <int N> DEV void waitv() { asm volatile("s_waitcnt vmcnt(%0)" ::"i"(N) : "memory"); }
template <int N> DEV void waitl() { asm volatile("s_waitcnt lgkmcnt(%0)" ::"i"(N) : "memory"); }
DEV void sbar() { asm volatile("s_barrier" ::: "memory"); }
#define SCHED() __builtin_amdgcn_sched_barrier(0)
DEV rsrc_t mkrs(const void* p, unsigned bytes) { return __builtin_amdgcn_make_buffer_rsrc((void*)p, 0, bytes, 0x00020000); }
DEV f32x4 mfma(v4u a, v4u b, f32x4 c) {
  return __builtin_amdgcn_mfma_f32_16x16x32_bf16(__builtin_bit_cast(bf16x8, a), __builtin_bit_cast(bf16x8, b), c, 0, 0, 0);
}
DEV v4u lds128(LAS const char* p) { return *(LAS const v4u*)p; }
DEV v2u lds64(LAS const char* p) { return *(LAS const v2u*)p; }
DEV v2u ldstr(LAS const char* p) { return __builtin_bit_cast(v2u, __builtin_amdgcn_ds_read_tr16_b64_v4i16((LAS v4s*)p)); }
DEV v4u cat(v2u a, v2u b) { return v4u{a.x, a.y, b.x, b.y}; }
DEV unsigned pk2(float a, float b) { bf16x2 r = __builtin_convertvector(f32x2{a, b}, bf16x2); return __builtin_bit_cast(unsigned, r); }
DEV float blo(unsigned d) { return __builtin_bit_cast(float, d << 16); }
DEV float bhi(unsigned d) { return __builtin_bit_cast(float, d & 0xffff0000u); }
DEV void setm0(unsigned v) { asm volatile("s_mov_b32 m0, %0\n\ts_nop 0" ::"s"(v)); }
template <int OFF, int NT = 0> DEV void dma16(unsigned voff, rsrc_t rs, unsigned soff) {
  if constexpr (NT) asm volatile("buffer_load_dwordx4 %0, %1, %2 offen offset:%3 nt lds" ::"v"(voff), "s"(rs), "s"(soff), "i"(OFF));
  else asm volatile("buffer_load_dwordx4 %0, %1, %2 offen offset:%3 lds" ::"v"(voff), "s"(rs), "s"(soff), "i"(OFF));
}
typedef float f32x16 __attribute__((ext_vector_type(16)));
DEV f32x16 mfma32(v4u a, v4u b, f32x16 c) {
  return __builtin_amdgcn_mfma_f32_32x32x16_bf16(__builtin_bit_cast(bf16x8, a), __builtin_bit_cast(bf16x8, b), c, 0, 0, 0);
}
template <int IMM> DEV void setm0_add(unsigned base) { asm volatile("s_add_u32 m0, %0, %1" ::"s"(base), "i"(IMM) : "scc"); }
template <int I, int N, class Fn> DEV void static_for(Fn&& f) { if constexpr (I < N) { f(std::integral_constant<int, I>{}); static_for<I + 1, N>(f); } }
DEV void mfma_ip(f32x4& c, const v4u& a, const v4u& b) { asm volatile("v_mfma_f32_16x16x32_bf16 %0, %1, %2, %0" : "+v"(c) : "v"(a), "v"(b)); }
DEV void mfma_ipA(f32x4& c, const v4u& a, const v4u& b) { asm volatile("v_mfma_f32_16x16x32_bf16 %0, %1, %2, %0" : "+v"(c) : "a"(a), "v"(b)); }
DEV void mfma_z(f32x4& c, const v4u& a, const v4u& b) { asm volatile("v_mfma_f32_16x16x32_bf16 %0, %1, %2, 0" : "=&v"(c) : "v"(a), "v"(b)); }
DEV void mfma_zA(f32x4& c, const v4u& a, const v4u& b) { asm volatile("v_mfma_f32_16x16x32_bf16 %0, %1, %2, 0" : "=&v"(c) : "a"(a), "v"(b)); }
template <int AUX> DEV void ldA(v4u& d, unsigned voff, rsrc_t rs, unsigned soff) {
  if constexpr (AUX) asm volatile("buffer_load_dwordx4 %0, %1, %2, %3 offen sc1" : "=a"(d) : "v"(voff), "s"(rs), "s"(soff));
  else asm volatile("buffer_load_dwordx4 %0, %1, %2, %3 offen" : "=a"(d) : "v"(voff), "s"(rs), "s"(soff));
}
template <int AUX> DEV void ldV(v4u& d, unsigned voff, rsrc_t rs, unsigned soff) {
  if constexpr (AUX == 2) asm volatile("buffer_load_dwordx4 %0, %1, %2, %3 offen nt" : "=v"(d) : "v"(voff), "s"(rs), "s"(soff));
  else if constexpr (AUX == 16) asm volatile("buffer_load_dwordx4 %0, %1, %2, %3 offen sc1" : "=v"(d) : "v"(voff), "s"(rs), "s"(soff));
  else asm volatile("buffer_load_dwordx4 %0, %1, %2, %3 offen" : "=v"(d) : "v"(voff), "s"(rs), "s"(soff));
}
DEV void nop8() { asm volatile("s_nop 7"); }

constexpr size_t WS_CTRL = 0;
constexpr size_t WS_KVT = 262144;
constexpr size_t WS_YT = WS_KVT + (size_t)D * S * 2;
constexpr size_t WS_W2T = WS_YT + (size_t)D * S * 2;
constexpr size_t WS_HF = WS_W2T + (size_t)8 * 48 * 5 * 1024;
constexpr size_t WS_G = WS_HF + (size_t)128 * 48 * 1024;
constexpr size_t WS_END = WS_G + S * 4;
constexpr unsigned C_EPOCH = 0, C_TMO = 256;
constexpr unsigned FS = FSTR;
constexpr unsigned C_FW = 4096;
constexpr unsigned C_FKV = C_FW + 64 * FS;
constexpr unsigned C_FH = C_FKV + 64 * FS;
constexpr unsigned C_FY = C_FH + 768 * FS;
constexpr unsigned C_BYTES = 262144;
static_assert(C_FY + 1024 * FS <= C_BYTES, "");

#define NSTAMP 16
#define STAMP_OFF (size_t)(32u << 20)
#define STAMP_DECL
#define STAMP(i)
#define STAMP_FLUSH()
#define FT_DECL
#define FT(i)
#define FT_FLUSH()

struct Params {
  const u16 *q, *k, *v, *sk, *h, *Dv, *w1, *w2, *gw;
  u16* out;
  char* ws;
};

constexpr int LDS_BYTES = 160 * 1024;
constexpr int SC1 = 16;
#define NT_X 0
#define NT_W1 0
#define NT_P 0
constexpr int AUXP = NT_P ? 2 : 0;
#define IC(n) std::integral_constant<int, (n)>{}

DEV void ld32_sc1(unsigned& v, unsigned voff, rsrc_t rs) { asm volatile("buffer_load_dword %0, %1, %2, 0 offen sc1" : "=v"(v) : "v"(voff), "s"(rs)); }
DEV void st32_sc1(unsigned v, unsigned voff, rsrc_t rs) { asm volatile("buffer_store_dword %0, %1, %2, 0 offen sc1" ::"v"(v), "v"(voff), "s"(rs)); }
DEV void poll_finish(unsigned v, unsigned voff, rsrc_t rsC, unsigned tag) {
  asm volatile("" : "+v"(v));
  if (__builtin_amdgcn_ballot_w64(v != tag) == 0) return;
#pragma unroll 1
  for (int spins = 0; spins < (1 << 15); ++spins) {
    __builtin_amdgcn_s_sleep(SPIN_SLEEP);
    ld32_sc1(v, voff, rsC);
    asm volatile("s_waitcnt vmcnt(0)" : "+v"(v)::"memory");
    if (__builtin_amdgcn_ballot_w64(v != tag) == 0) return;
  }
  st32_sc1(1u, C_TMO, rsC);
}

DEV void poll_finish2(unsigned a, unsigned oa, unsigned b, unsigned ob, rsrc_t rsC, unsigned tag) {
  asm volatile("" : "+v"(a), "+v"(b));
  if (__builtin_amdgcn_ballot_w64(a != tag || b != tag) == 0) return;
#pragma unroll 1
  for (int spins = 0; spins < (1 << 15); ++spins) {
    __builtin_amdgcn_s_sleep(SPIN_SLEEP);
    ld32_sc1(a, oa, rsC); ld32_sc1(b, ob, rsC);
    asm volatile("s_waitcnt vmcnt(0)" : "+v"(a), "+v"(b)::"memory");
    if (__builtin_amdgcn_ballot_w64(a != tag || b != tag) == 0) return;
  }
  st32_sc1(1u, C_TMO, rsC);
}

DEV void mfma32_ip(f32x16& c, const v4u& a, const v4u& b) { asm volatile("v_mfma_f32_32x32x16_bf16 %0, %1, %2, %0" : "+v"(c) : "v"(a), "v"(b)); }
DEV void mfma32_z(f32x16& c, const v4u& a, const v4u& b) { asm volatile("v_mfma_f32_32x32x16_bf16 %0, %1, %2, 0" : "=&v"(c) : "v"(a), "v"(b)); }

constexpr int KV_BASE = 32768, KV_GS = 1152, KV_V = 9 * KV_GS, KV_CG = 17 * KV_GS;
constexpr int CV_BASE = 32768;
constexpr int CV_HC = 4160;
constexpr int CV_U = 4 * CV_HC;
constexpr int CV_CH = CV_U + 4608;
constexpr int CV_EX = CV_BASE + 4 * CV_CH;
constexpr int G2_PART = 0;
constexpr int G2_Y = 81920;
constexpr int HW = 24;

template <int AUX> DEV void ldV2(v2u& d, unsigned voff, rsrc_t rs, unsigned soff) {
  if constexpr (AUX == 2) asm volatile("buffer_load_dwordx2 %0, %1, %2, %3 offen nt" : "=v"(d) : "v"(voff), "s"(rs), "s"(soff));
  else if constexpr (AUX == 16) asm volatile("buffer_load_dwordx2 %0, %1, %2, %3 offen sc1" : "=v"(d) : "v"(voff), "s"(rs), "s"(soff));
  else asm volatile("buffer_load_dwordx2 %0, %1, %2, %3 offen" : "=v"(d) : "v"(voff), "s"(rs), "s"(soff));
}
DEV void dma16s(unsigned voff, rsrc_t rs, unsigned soff, unsigned m0v) {
  if constexpr (NT_P) asm volatile("s_mov_b32 m0, %3\n\ts_nop 0\n\tbuffer_load_dwordx4 %0, %1, %2 offen nt lds" ::"v"(voff), "s"(rs), "s"(soff), "s"(m0v));
  else asm volatile("s_mov_b32 m0, %3\n\ts_nop 0\n\tbuffer_load_dwordx4 %0, %1, %2 offen lds" ::"v"(voff), "s"(rs), "s"(soff), "s"(m0v));
}
DEV float dot2(unsigned a, unsigned b, float c) { return __builtin_amdgcn_fdot2_f32_bf16(__builtin_bit_cast(bf16x2, a), __builtin_bit_cast(bf16x2, b), c, false); }
DEV unsigned mktag(unsigned& epoch) {
  asm volatile("" : "+v"(epoch));
  return __builtin_amdgcn_readfirstlane(epoch * 0x9E3779B1u + 0x7F4A7C15u);
}

template <int NR, class ST> DEV void kv_math(const v4u (&kk)[NR + 3], const v4u (&vv)[NR], const v4u (&ww)[4], ST&& st) {
#pragma unroll
  for (int cp = 0; cp < 4; ++cp) {
    f32x2 w[4];
    w[0] = f32x2{blo(ww[cp].x), blo(ww[cp].z)}; w[1] = f32x2{bhi(ww[cp].x), bhi(ww[cp].z)};
    w[2] = f32x2{blo(ww[cp].y), blo(ww[cp].w)}; w[3] = f32x2{bhi(ww[cp].y), bhi(ww[cp].w)};
    f32x2 kf[NR + 3];
#pragma unroll
    for (int i = 0; i < NR + 3; ++i) kf[i] = f32x2{blo(kk[i][cp]), bhi(kk[i][cp])};
    f32x2 r[NR];
#pragma unroll
    for (int i = 0; i < NR; ++i) {
      f32x2 a = kf[i + 3] * w[0];
      a = __builtin_elementwise_fma(kf[i + 2], w[1], a);
      a = __builtin_elementwise_fma(kf[i + 1], w[2], a);
      a = __builtin_elementwise_fma(kf[i], w[3], a);
      r[i] = a * f32x2{blo(vv[i][cp]), bhi(vv[i][cp])};
    }
    st(cp, r);
  }
}

#define Q0 7
#define ROT 0
#define PREP_FIRST 0
#define HPUB 90
#define WPRE_ 16
constexpr int WPRE = WPRE_;
constexpr int HW0 = 16, HWM = 24;
constexpr int PW_BASE = 131072, PW_SZ = 3072;
constexpr int g2_issued(int i) { int r = HW0; for (int s = 0; s < i; ++s) r += (r - s < HWM) ? ((r + 1 - s < HWM) ? 2 : 1) : 0; return r < 48 ? r : 48; }

template <int OFF, int AUX> DEV void ldVo(v4u& d, unsigned voff, rsrc_t rs, unsigned soff) {
  static_assert(AUX == 16, "");
  asm volatile("buffer_load_dwordx4 %0, %1, %2, %3 offen offset:%4 sc1" : "=v"(d) : "v"(voff), "s"(rs), "s"(soff), "i"(OFF));
}
template <int OFF> DEV void ldAo(v4u& d, unsigned voff, rsrc_t rs, unsigned soff) {
  asm volatile("buffer_load_dwordx4 %0, %1, %2, %3 offen offset:%4 sc1" : "=a"(d) : "v"(voff), "s"(rs), "s"(soff), "i"(OFF));
}

template <bool G1R>
DEV void body(const Params& p, LAS char* lds, const int x, const int r, const int wave, const int lane) {
  const int g = lane >> 4, q4 = (lane >> 2) & 3, p4 = lane & 3, c16 = lane & 15;
  STAMP_DECL; FT_DECL;
  STAMP(0); FT(13);
  const rsrc_t rsC = mkrs(p.ws + WS_CTRL, C_BYTES);
  unsigned epoch, tag;
  ld32_sc1(epoch, C_EPOCH, rsC);
  const int cl = G1R ? (wave >> 1) : wave, half = G1R ? (wave & 1) : 0;
  const int ch = (G1R ? 80 * x + 2 * r : 80 * x + 48 + 4 * (r - 24)) + cl;
  const unsigned cb = CV_BASE + cl * CV_CH;
  constexpr int NCHK = G1R ? 2 : 4;
  unsigned dv;
  { const rsrc_t rsD = mkrs(p.Dv, D * 2); asm volatile("buffer_load_ushort %0, %1, %2, 0 offen" : "=v"(dv) : "v"(ch * 2), "s"(rsD)); }
  const rsrc_t rsHh = mkrs(p.h, D * S * 2), rsU = mkrs(p.ws + WS_KVT, D * S * 2), rsY = mkrs(p.ws + WS_YT, D * S * 2);
  const rsrc_t rsW2 = mkrs(p.ws + WS_W2T, 8 * 48 * 5 * 1024), rsH = mkrs(p.ws + WS_HF, 128 * 48 * 1024);
#define MAP42 1
  const int rb = MAP42 ? 8 * (x >> 1) + (r & 7) : 4 * x + (r & 3), nt = MAP42 ? 4 * (x & 1) + (r >> 3) : r >> 2;
  const unsigned vl = lane * 16;
  const unsigned sW = (nt * 48 + 12 * wave) * 5 * 1024;
  const unsigned foW = C_FW + lane * FS, foKV = C_FKV + lane * FS;
  v4u W[12][5];
  auto ldW = [&](auto I) { constexpr int i = I.value; if constexpr (i >= 0 && i < 60) ldAo<(i & 3) * 1024>(W[i / 5][i % 5], vl, rsW2, sW + (i & ~3) * 1024); };
  v4u hc[NCHK], uc[NCHK]; v2u hp0;
  auto ld_h = [&]() {
    ldV2<AUXP>(hp0, half > 0 ? (unsigned)(ch * 4096 + half * NCHK * 1024 - 8) : 0x80000000u, rsHh, 0);
#pragma unroll
    for (int i = 0; i < NCHK; ++i) ldV<AUXP>(hc[i], ch * 4096 + ((half * NCHK + i) * 64 + lane) * 16, rsHh, 0);
  };
  auto ld_u = [&]() {
#pragma unroll
    for (int i = 0; i < NCHK; ++i) ldV<SC1>(uc[i], ch * 4096 + ((half * NCHK + i) * 64 + lane) * 16, rsU, 0);
  };
  auto fill_h = [&]() {
    asm volatile("" : "+v"(hp0));
#pragma unroll
    for (int i = 0; i < NCHK; ++i) asm volatile("" : "+v"(hc[i]));
    unsigned e0 = hp0.x, e1 = hp0.y;
#pragma unroll
    for (int i = 0; i < NCHK; ++i) {
      const int n = (half * NCHK + i) * 64 + lane;
      const v4u cur = hc[i];
      const v2u prv = v2u{(unsigned)__builtin_amdgcn_update_dpp((int)e0, (int)cur.z, 0x138, 0xf, 0xf, false), (unsigned)__builtin_amdgcn_update_dpp((int)e1, (int)cur.w, 0x138, 0xf, 0xf, false)};
      e0 = __builtin_amdgcn_readlane(cur.z, 63); e1 = __builtin_amdgcn_readlane(cur.w, 63);
      const unsigned om2 = __builtin_amdgcn_alignbit(prv.y, prv.x, 16), om1 = __builtin_amdgcn_alignbit(cur.x, prv.y, 16);
      const unsigned o0 = __builtin_amdgcn_alignbit(cur.y, cur.x, 16), o1 = __builtin_amdgcn_alignbit(cur.z, cur.y, 16), o2 = __builtin_amdgcn_alignbit(cur.w, cur.z, 16);
      LAS char* dst = lds + cb + (n + 2) * 16;
      *(LAS v4u*)(dst) = cur;
      *(LAS v4u*)(dst + CV_HC) = v4u{om1, o0, o1, o2};
      *(LAS v4u*)(dst + 2 * CV_HC) = v4u{prv.y, cur.x, cur.y, cur.z};
      *(LAS v4u*)(dst + 3 * CV_HC) = v4u{om2, om1, o0, o1};
    }
    if (half == 0) {
      if (lane < 8) *(LAS v4u*)(lds + cb + (lane >> 1) * CV_HC + (lane & 1) * 16) = v4u{0, 0, 0, 0};
      if (lane >= 32 && lane < 62) *(LAS v4u*)(lds + cb + CV_U + (lane - 32) * 16) = v4u{0, 0, 0, 0};
    }
  };
  auto fill_u = [&]() {
#pragma unroll
    for (int i = 0; i < NCHK; ++i) {
      asm volatile("" : "+v"(uc[i]));
      *(LAS v4u*)(lds + cb + CV_U + 480 + ((half * NCHK + i) * 64 + lane) * 16) = uc[i];
    }
  };
  unsigned fw;

  if constexpr (G1R) {
    const int sb = 4 * (x >> 1) + (r & 3), fs = 6 * (x & 1) + (r >> 2);
    f32x16 acc1[2][2];
    const rsrc_t rsX = mkrs(p.q, S * D * 2), rsW = mkrs(p.w1, D * F * 2);
    unsigned vX[4], vW[4];
#pragma unroll
    for (int ii = 0; ii < 4; ++ii) {
      vX[ii] = (32 * wave + 8 * ii + (lane >> 3)) * 1280 + (((lane & 7) ^ (4 * (ii & 1) + g)) << 4) - 1024 * ii;
      vW[ii] = (16 * wave + 4 * ii + g) * 3072 + (((((c16 >> 1) ^ (g << 1)) << 1) | (c16 & 1)) << 4) - 1024 * ii;
    }
    const unsigned sX0 = sb * 128 * 1280, sW0 = fs * 256;
    const unsigned mX = wave * 4096, mW = 16384 + wave * 4096;
    const int rot = ROT ? (2 * (r >> 2) + 3 * (r & 3)) % 10 : 0;
    auto kslice = [&](int t) { const int k = t + rot; return k >= 10 ? k - 10 : k; };
    unsigned sq;
    auto grp_begin = [&](auto Q) { constexpr int q = Q.value, t = q >> 1;
      if constexpr (q < 20) { sq = (q & 1) ? sW0 + kslice(t) * (64 * 3072) : sX0 + kslice(t) * 128; setm0_add<(t % 5) * 32768>((q & 1) ? mW : mX); } };
    auto grp_dma = [&](auto Q, auto I) { constexpr int q = Q.value, i = I.value;
      if constexpr (q < 20) { if constexpr (q & 1) dma16<1024 * i, NT_W1>(vW[i], rsW, sq); else dma16<1024 * i, NT_X>(vX[i], rsX, sq); } };
    FT(10);
    static_for<0, Q0>([&](auto Q) { grp_begin(Q); asm volatile("s_nop 0"); static_for<0, 4>([&](auto I) { grp_dma(Q, I); }); });
    FT(11);
    const int ws_ = wave & 1, wf = wave >> 1, h = lane >> 5, r32 = lane & 31;
    unsigned aX[3][4], aW[3][2];
#pragma unroll
    for (int w = 0; w < 3; ++w) {
#pragma unroll
      for (int ks = 0; ks < 4; ++ks) aX[w][ks] = w * 65536 + (64 * ws_ + r32) * 128 + (((2 * ks + h) ^ ((r32 >> 1) & 7)) << 4);
#pragma unroll
      for (int ft = 0; ft < 2; ++ft) aW[w][ft] = w * 65536 + 16384 + (8 * h + q4) * 256 + (((2 * (2 * wf + ft) + (g & 1)) ^ (q4 << 1)) << 5) + 8 * p4;
    }
    v4u A[2][2][2], B[2][2][2];
    v2u At;
    auto rd = [&](auto T, auto HF, auto SET, auto N) {
      constexpr int t = T.value, hf = HF.value, set = SET.value, n = N.value;
      if constexpr (t < 10) {
        constexpr int b = t % 5, w = b >> 1, bo = (b & 1) * 32768;
        constexpr int kk = n / 6, rr = n % 6, ks = 2 * hf + kk;
        if constexpr (rr == 0) B[set][kk][0] = lds128(lds + aX[w][ks] + bo);
        else if constexpr (rr == 3) B[set][kk][1] = lds128(lds + aX[w][ks] + bo + 32 * 128);
        else { constexpr int ft = rr / 3, jj = (rr % 3) - 1;
          v2u xx = ldstr(lds + aW[w][ft] + bo + ks * 4096 + jj * 1024);
          if constexpr (jj == 0) At = xx; else A[set][kk][ft] = cat(At, xx); }
      }
    };

    unsigned fk;
    waitv<4 * (Q0 - 2)>(); sbar();
    STAMP(1);
    static_for<0, 12>([&](auto N) { rd(IC(0), IC(0), IC(0), N); });
    SCHED();
    static_for<0, 10>([&](auto T) {
      constexpr int t = T.value;
      static_for<0, 2>([&](auto HF) {
        constexpr int hf = HF.value, hi = 2 * t + hf, q = Q0 + hi;
        static_for<0, 8>([&](auto M) {
          constexpr int m = M.value, kk = m >> 2, ft = (m >> 1) & 1, st = m & 1;
          if constexpr (t == 0 && hf == 0 && kk == 0) mfma32_z(acc1[ft][st], A[hf][kk][ft], B[hf][kk][st]);
          else mfma32_ip(acc1[ft][st], A[hf][kk][ft], B[hf][kk][st]);
          if constexpr (m < 6) {
            if constexpr (hf == 0) { rd(T, IC(1), IC(1), IC(2 * m)); rd(T, IC(1), IC(1), IC(2 * m + 1)); }
            else { rd(IC(t + 1), IC(0), IC(0), IC(2 * m)); rd(IC(t + 1), IC(0), IC(0), IC(2 * m + 1)); }
          }
          if constexpr (m == 0) grp_begin(IC(q));
          if constexpr (m & 1) grp_dma(IC(q), IC(m >> 1));
          SCHED();
        });
        if constexpr (hf == 0 && t < 9) {
          constexpr int lastq = (Q0 + 2 * t < 19) ? Q0 + 2 * t : 19;
          constexpr int younger = 4 * (lastq - (2 * t + 3) > 0 ? lastq - (2 * t + 3) : 0);
          waitv<younger>(); sbar(); SCHED();
        }
      });
      FT(t); SCHED();
    });
    asm volatile("s_nop 15" : "+v"(acc1[0][0]), "+v"(acc1[0][1]), "+v"(acc1[1][0]), "+v"(acc1[1][1]));
    sbar();
    STAMP(2);
    tag = mktag(epoch);
    ld_h();
    const unsigned vo = (g & 1) * (48 * 1024) + (c16 + 16 * h) * 16;
    static_for<0, 4>([&](auto Q) {
      constexpr int qd = Q.value, st = qd >> 1, ft = qd & 1;
      const f32x16 a = acc1[ft][st];
      unsigned o[8];
      static_for<0, 2>([&](auto G) {
        constexpr int gq = G.value;
        static_for<0, 4>([&](auto E) {
          constexpr int e = 8 * gq + 2 * E.value;
          const float r0 = a[e] * __builtin_amdgcn_rcpf(1.f + __builtin_amdgcn_exp2f(a[e] * -1.44269504088896f));
          const float r1 = a[e + 1] * __builtin_amdgcn_rcpf(1.f + __builtin_amdgcn_exp2f(a[e + 1] * -1.44269504088896f));
          o[e >> 1] = pk2(r0, r1);
        });
        SCHED();
      });
      const unsigned so = ((8 * sb + 4 * ws_ + 2 * st) * 48 + 4 * fs + 2 * wf + ft) * 1024;
      __builtin_amdgcn_raw_buffer_store_b128(v4u{o[0], o[1], o[4], o[5]}, rsH, vo, so, SC1);
      __builtin_amdgcn_raw_buffer_store_b128(v4u{o[2], o[3], o[6], o[7]}, rsH, vo + 512, so, SC1);
      SCHED();
      FT(32 + qd); SCHED();
    });
    ld32_sc1(fk, foKV, rsC); ld32_sc1(fw, foW, rsC);
    waitv<10>();
    fill_h();
    FT(54);
    waitv<0>();
    if (lane == 0) st32_sc1(tag, C_FH + ((sb * 2 + ws_) * 24 + fs * 2 + wf) * FS, rsC);
    FT(55);
    poll_finish(fk, foKV, rsC, tag);
    ld_u();
    FT(36);
    poll_finish(fw, foW, rsC, tag);
    FT(37);
    static_for<0, WPRE>([&](auto I) { ldW(I); });
    STAMP(3);
    waitv<WPRE>();
    fill_u();
    waitl<0>(); sbar();
    STAMP(4);
  } else {
    const int j = (r - 24) * 8 + x, rbk = j >> 1, cg0 = 5 * (j & 1);
    const rsrc_t rsK = mkrs(p.k, S * D * 2), rsV = mkrs(p.v, S * D * 2), rsS = mkrs(p.sk, D * 8), rsO = mkrs(p.ws + WS_KVT, D * S * 2);
    const unsigned vr = (lane >> 3) * 1280 + ((lane & 7) << 4), vr4 = vr ^ 64;
    auto dmaK = [&](int cgl, int G) {
      const int row = 64 * rbk - 8 + 8 * G;
      dma16s(((G >> 1) & 1) ? vr4 : vr, rsK, row >= 0 ? (unsigned)(row * 1280 + (cg0 + cgl) * 128) : 0x80000000u, KV_BASE + cgl * KV_CG + G * KV_GS);
    };
    auto dmaV = [&](int cgl, int G) {
      dma16s(((G >> 1) & 1) ? vr4 : vr, rsV, (64 * rbk + 8 * G) * 1280 + (cg0 + cgl) * 128, KV_BASE + cgl * KV_CG + KV_V + G * KV_GS);
    };
#pragma unroll
    for (int G = 0; G < 9; ++G) dmaK(wave, G);
#pragma unroll
    for (int G = 0; G < 8; ++G) dmaV(wave, G);
    const unsigned pw = PW_BASE + wave * PW_SZ;
    dma16s(lane < 32 ? (unsigned)((cg0 + wave) * 512 + lane * 16) : (lane < 40 ? (unsigned)((64 * (cg0 + 4) + 16 * wave) * 8 + (lane - 32) * 16) : 0x80000000u), rsS, 0, pw);
    dmaK(4, wave); dmaK(4, wave + 4); dmaV(4, wave); dmaV(4, wave + 4);
    if (wave == 0) dmaK(4, 8);
    const int cc = lane >> 3, rg = lane & 7;
    FT(10);
    waitv<4>();
    STAMP(1);
    {
      const unsigned kb = KV_BASE + wave * KV_CG;
      const unsigned a0 = kb + rg * KV_GS + ((cc ^ (4 * ((rg >> 1) & 1))) << 4), a1 = kb + (rg + 1) * KV_GS + ((cc ^ (4 * (((rg + 1) >> 1) & 1))) << 4);
      v4u kk[11], vv[8], ww[4];
#pragma unroll
      for (int i = 0; i < 4; ++i) ww[i] = lds128(lds + pw + cc * 64 + i * 16);
#pragma unroll
      for (int i = 0; i < 11; ++i) kk[i] = i < 3 ? lds128(lds + a0 + (5 + i) * 128) : lds128(lds + a1 + (i - 3) * 128);
#pragma unroll
      for (int i = 0; i < 8; ++i) vv[i] = lds128(lds + a0 + KV_V + i * 128);
      const unsigned ob = (64 * (cg0 + wave) + 8 * cc) * 4096 + (64 * rbk + 8 * rg) * 2;
      kv_math<8>(kk, vv, ww, [&](int cp, const f32x2 (&rr)[8]) {
#pragma unroll
        for (int c = 0; c < 2; ++c) {
          v4u o{pk2(rr[0][c], rr[1][c]), pk2(rr[2][c], rr[3][c]), pk2(rr[4][c], rr[5][c]), pk2(rr[6][c], rr[7][c])};
          __builtin_amdgcn_raw_buffer_store_b128(o, rsO, ob + (2 * cp + c) * 4096, 0, SC1);
        }
      });
    }
    const int id = 4 * j + wave, pk = id / 5, blk = id % 5;
    const rsrc_t rsWs = mkrs(p.w2, F * D * 2);
    const unsigned ldsb = wave * 8192;
#pragma unroll
    for (int i = 0; i < 8; ++i) {
      const int row = 4 * i + g;
      dma16s((32 * pk + row) * 1280 + blk * 256 + (((((c16 >> 1) ^ (row & 7)) << 1) | (c16 & 1)) << 4), rsWs, id < 240 ? 0u : 0x80000000u, ldsb + i * 1024);
    }
    waitv<16>(); sbar();
    STAMP(2);
    {
      const int cc4 = 2 * wave + (lane >> 5), rp = lane & 31;
      const unsigned kb = KV_BASE + 4 * KV_CG;
      v4u kk[5], vv[2], w4[4];
#pragma unroll
      for (int i = 0; i < 4; ++i) w4[i] = lds128(lds + pw + 512 + (lane >> 5) * 64 + i * 16);
#pragma unroll
      for (int i = 0; i < 5; ++i) { const int row = 2 * rp + 5 + i; kk[i] = lds128(lds + kb + (row >> 3) * KV_GS + (row & 7) * 128 + ((cc4 ^ (4 * ((row >> 4) & 1))) << 4)); }
#pragma unroll
      for (int i = 0; i < 2; ++i) { const int row = 2 * rp + i; vv[i] = lds128(lds + kb + KV_V + (row >> 3) * KV_GS + (row & 7) * 128 + ((cc4 ^ (4 * ((row >> 4) & 1))) << 4)); }
      const unsigned ob = (64 * (cg0 + 4) + 8 * cc4) * 4096 + (64 * rbk + 2 * rp) * 2;
      kv_math<2>(kk, vv, w4, [&](int cp, const f32x2 (&rr)[2]) {
#pragma unroll
        for (int c = 0; c < 2; ++c) __builtin_amdgcn_raw_buffer_store_b32(pk2(rr[0][c], rr[1][c]), rsO, ob + (2 * cp + c) * 4096, 0, SC1);
      });
    }
    FT(11);
    waitv<8>();
    {
      const rsrc_t rsWo = mkrs(p.ws + WS_W2T, 8 * 48 * 5 * 1024);
      const int r0 = 4 * g + q4;
#pragma unroll
      for (int i = 0; i < 8; ++i) {
        const unsigned a = ldsb + r0 * 256 + ((i ^ (r0 & 7)) << 5) + 8 * p4;
        const v4u fr = cat(ldstr(lds + a), ldstr(lds + a + 4096));
        const int ctg = 8 * blk + i, ntt = ctg / 5, ct = ctg % 5;
        __builtin_amdgcn_raw_buffer_store_b128(fr, rsWo, vl, id < 240 ? (unsigned)(((ntt * 48 + pk) * 5 + ct) * 1024) : 0x80000000u, SC1);
      }
    }
    FT(0);
    waitv<8>();
    waitl<0>(); sbar();
    tag = mktag(epoch);
    if (threadIdx.x == 0) st32_sc1(tag, C_FKV + j * FS, rsC);
    STAMP(3);
    ld_h();
    waitv<5>(); sbar();
    if (threadIdx.x == 0) st32_sc1(tag, C_FW + j * FS, rsC);
    unsigned fk;
    ld32_sc1(fk, foKV, rsC);
    FT(3);
    waitv<1>();
    waitv<0>();
    fill_h();
    FT(2);
    poll_finish(fk, foKV, rsC, tag);
    FT(4);
    ld_u();
    const rsrc_t rsX = mkrs(p.q, S * D * 2), rsG = mkrs(p.gw, D * 2), rsGo = mkrs(p.ws + WS_G, S * 4);
    const int grow = 256 * x + 32 * (r - 24) + 8 * wave + (lane >> 3), part = lane & 7;
    v4u xx[10];
    dma16s(lane * 16, rsG, 0, pw + 1024); dma16s(1024 + lane * 16, rsG, 0, pw + 2048);
#pragma unroll
    for (int i = 0; i < 10; ++i) ldV<0>(xx[i], grow * 1280 + i * 128 + part * 16, rsX, 0);
    waitv<12>();
    fill_u();
    ld32_sc1(fw, foW, rsC);
    waitv<1>();
    {
      float a0 = 0.f, a1 = 0.f;
#pragma unroll
      for (int i = 0; i < 10; ++i) {
        asm volatile("" : "+v"(xx[i]));
        const v4u gg = lds128(lds + pw + 1024 + (i * 8 + part) * 16);
        a0 = dot2(xx[i].x, gg.x, a0); a1 = dot2(xx[i].y, gg.y, a1); a0 = dot2(xx[i].z, gg.z, a0); a1 = dot2(xx[i].w, gg.w, a1);
      }
      float a = a0 + a1;
      a += __shfl_xor(a, 1); a += __shfl_xor(a, 2); a += __shfl_xor(a, 4);
      const float gt = __builtin_amdgcn_rcpf(1.f + __builtin_amdgcn_exp2f(a * -1.44269504088896f));
      if (part == 0) __builtin_amdgcn_raw_buffer_store_b32(__builtin_bit_cast(unsigned, gt), rsGo, grow * 4, 0, SC1);
    }
    FT(5);
    waitl<0>();
    STAMP(4);
  }
  STAMP(5);
  constexpr int NZ = G1R ? 4 : 8;
  constexpr int W0 = G1R ? WPRE : 0, WG0 = G1R ? 0 : 14;
  auto wcount = [](int gap) constexpr { return gap < WG0 ? 0 : (G1R ? ((gap - WG0) * (60 - WPRE)) / 140 + 1 : (gap - WG0) / 4 + 1); };
  const unsigned foH = C_FH + (((rb >> 1) * 2 + (rb & 1)) * 24 + (lane < 24 ? lane : 0)) * FS;
  unsigned fh;
  constexpr int GPOLL = NZ * 36 - 12;
  f32x4 acc[8];
  {
    const unsigned aA = cb + q4 * CV_HC + 56 - 16 * g + 8 * p4 + 256 * half;
    const unsigned aB = cb + CV_U + 448 + 32 * c16 + 16 * g - 256 * half - 64 * (NZ - 1);
    v4u A[8], B[8];
#pragma unroll
    for (int i = 0; i < 8; ++i) {
      B[7 - i] = lds128(lds + aB + 64 * (NZ - 1) + 512 * (7 - i));
      A[i] = cat(ldstr(lds + aA + 512 * i + 8), ldstr(lds + aA + 512 * i));
    }
    SCHED();
    static_for<0, NZ>([&](auto Z) {
      constexpr int z = Z.value;
      static_for<0, 8>([&](auto I) {
        constexpr int i = I.value;
        static_for<0, 8 - i>([&](auto DD) {
          constexpr int d = 7 - i - DD.value;
          constexpr int gap = z * 36 + (i * (17 - i)) / 2 + DD.value;
          if constexpr (z == 0 && i == 0) mfma_z(acc[i + d], A[i], B[d]); else mfma_ip(acc[i + d], A[i], B[d]);
          if constexpr (d == 7 - i && z + 1 < NZ) B[7 - i] = lds128(lds + aB + 64 * (NZ - 2 - z) + 512 * (7 - i));
          if constexpr (!G1R && gap == WG0 - 2) { waitv<0>(); poll_finish(fw, foW, rsC, tag); }
          if constexpr (wcount(gap) > wcount(gap - 1)) ldW(IC(W0 + wcount(gap) - 1));
          if constexpr (gap == GPOLL) ld32_sc1(fh, foH, rsC);
          SCHED();
        });
        if constexpr (i == 7) { FT(40 + z); SCHED(); }
        if constexpr (z + 1 < NZ) { A[i] = cat(ldstr(lds + aA + 64 * (z + 1) + 512 * i + 8), ldstr(lds + aA + 64 * (z + 1) + 512 * i)); SCHED(); }
      });
    });
  }
  static_assert(W0 + wcount(NZ * 36 - 1) >= 60, "all W2 fragments issued");
  STAMP(6);
  constexpr int WAFT = 60 - (W0 + wcount(GPOLL));
  waitv<(WAFT > 0 ? WAFT : 0)>();
  FT(48);
  poll_finish(fh, foH, rsC, tag);
  FT(49);
  v4u Hf[HWM];
  const unsigned sH = (4 * rb * 48 + 12 * wave) * 1024;
  auto ldH = [&](auto N) { constexpr int n = N.value; if constexpr (n < 48) ldVo<(n & 3) * 1024, SC1>(Hf[n % HWM], vl, rsH, sH + ((n / 12) * 48 + ((n % 12) & ~3)) * 1024); };
  static_for<0, HW0>([&](auto I) { ldH(I); });
  FT(14);
  asm volatile("s_nop 7" : "+v"(acc[0]), "+v"(acc[1]), "+v"(acc[2]), "+v"(acc[3]), "+v"(acc[4]), "+v"(acc[5]), "+v"(acc[6]), "+v"(acc[7]));
  SCHED();
  int ctb = 0;
  if constexpr (G1R) {
    LAS char* ex = lds + CV_EX + cl * 8192;
    f32x4 keep[4];
#pragma unroll
    for (int i = 0; i < 4; ++i) {
      f32x4 lo = acc[i], hi = acc[4 + i];
      asm volatile("" : "+v"(lo), "+v"(hi));
      *(LAS f32x4*)(ex + half * 4096 + i * 1024 + lane * 16) = half == 0 ? hi : lo;
      keep[i] = half == 0 ? lo : hi;
    }
    waitl<0>(); sbar();
#pragma unroll
    for (int i = 0; i < 4; ++i) acc[i] = keep[i] + *(LAS f32x4*)(ex + (1 - half) * 4096 + i * 1024 + lane * 16);
    ctb = 4 * half;
  }
  FT(51);
  constexpr int NT = G1R ? 4 : 8, NYS = NT / 2;
  {
    asm volatile("" : "+v"(dv));
    const float Dc = blo(dv);
#pragma unroll
    for (int i = 0; i < NT; i += 2) {
      unsigned w0[2][2];
#pragma unroll
      for (int e = 0; e < 2; ++e) {
        const int ct = ctb + i + e;
        const f32x4 a = acc[i + e];
        const v2u uu = lds64(lds + cb + CV_U + 480 + 512 * ct + 32 * c16 + 8 * g);
        w0[e][0] = pk2(fmaf(blo(uu.x), Dc, a[0]), fmaf(bhi(uu.x), Dc, a[1]));
        w0[e][1] = pk2(fmaf(blo(uu.y), Dc, a[2]), fmaf(bhi(uu.y), Dc, a[3]));
      }
      auto s0 = __builtin_amdgcn_permlane16_swap(w0[0][0], w0[1][0], false, false);
      auto s1 = __builtin_amdgcn_permlane16_swap(w0[0][1], w0[1][1], false, false);
      const int ct = ctb + i + (g & 1);
      __builtin_amdgcn_raw_buffer_store_b128(v4u{s0[0], s1[0], s0[1], s1[1]}, rsY, ch * 4096 + 512 * ct + 32 * c16 + 8 * (g & 2), 0, SC1);
    }
  }
  FT(52);
  waitl<0>(); sbar();
  STAMP(7);
  unsigned fe[13];
  const bool bump = blockIdx.x == 0 && wave == 0;
  f32x4 ac2[2][5];
  const unsigned foY0 = C_FY + (nt * 128 + lane) * FS, foY1 = foY0 + 64 * FS;
  const unsigned foG = C_FY + ((rb >> 2) * 128 + 96 + (lane & 31)) * FS;
  unsigned fy0, fy1, fg, gtu;
  v4u yv[3]; v2u qq[5];
  const int s = 64 * rb + 16 * wave + c16;
#define SF_ 5
  constexpr int SF = SF_;
  constexpr int SP = 25;
  constexpr int SY = 40;
  static_assert(g2_issued(SP) == 48 && SY >= SP + 8, "");
  auto extras_at = [](int st) constexpr { return (st == SF ? 1 : 0) + ((st >= SP && st < SP + 8) ? 1 : 0) + (st == SY ? 4 : 0); };
  static_for<0, 48>([&](auto I) {
    constexpr int i = I.value, rt = i / 12, k = i % 12, set = rt & 1;
    constexpr int younger = [&]() constexpr {
      int y = g2_issued(i) - (i + 1);
      if (i < HW0) y += NYS;
      int si = -1; while (i >= HW0 && g2_issued(si + 1) <= i) ++si;
      for (int st = (si < 0 ? 0 : si); st < i; ++st) y += extras_at(st);
      return y; }();
    waitv<(younger < 63 ? younger : 63)>();
    static_for<0, 5>([&](auto C) { constexpr int ct = C.value;
      if constexpr (k == 0) mfma_zA(ac2[set][ct], W[k][ct], Hf[i % HWM]); else mfma_ipA(ac2[set][ct], W[k][ct], Hf[i % HWM]);
      if constexpr (rt > 0 && k >= 1 && k <= 5 && ct == 3) *(LAS f32x4*)(lds + G2_PART + (((rt - 1) * 4 + wave) * 5 + (k - 1)) * 1024 + lane * 16) = ac2[set ^ 1][k - 1];
      if constexpr (ct == 1) { static_for<g2_issued(i), g2_issued(i + 1)>([&](auto N) { ldH(N); }); }
    });
    if constexpr (i == SF) {
      waitv<g2_issued(SF + 1) - HW0>();
      if (lane == 0) st32_sc1(tag, C_FY + ((x * 32 + r) * 4 + wave) * FS, rsC);
    }
    if constexpr (i == SP) ld32_sc1(fy0, foY0, rsC);
    if constexpr (i == SP + 1) ld32_sc1(fy1, foY1, rsC);
    if constexpr (i == SP + 2) ld32_sc1(fg, foG, rsC);
    if constexpr (i >= SP + 3 && i < SP + 8) { const rsrc_t rsQ = mkrs(p.q, S * D * 2); ldV2<AUXP>(qq[i - SP - 3], (s * D + 80 * nt + 16 * (i - SP - 3) + 4 * g) * 2, rsQ, 0); }
    if constexpr (i == 34) {
      if (bump) {
#pragma unroll
        for (int e = 0; e < 13; ++e) ld32_sc1(fe[e], e < 12 ? C_FH + (e * 64 + lane) * FS : C_FKV + lane * FS, rsC);
      }
    }
    if constexpr (i == SY) {
      waitv<5>();
      poll_finish2(fy0, foY0, fy1, foY1, rsC, tag);
      poll_finish(fg, foG, rsC, tag);
      { const rsrc_t rsG = mkrs(p.ws + WS_G, S * 4); ld32_sc1(gtu, s * 4, rsG); }
#pragma unroll
      for (int u = 0; u < 3; ++u) {
        const int idx = u * 256 + wave * 64 + lane, d = idx >> 3, cch = idx & 7;
        ldV<SC1>(yv[u], idx < 640 ? (unsigned)((80 * nt + d) * 4096 + rb * 128 + cch * 16) : 0x80000000u, rsY, 0);
      }
    }
    if constexpr (i % 4 == 3) { FT(16 + i / 4); }
    SCHED();
  });
  STAMP(9);
  asm volatile("s_nop 7" : "+v"(ac2[1][0]), "+v"(ac2[1][1]), "+v"(ac2[1][2]), "+v"(ac2[1][3]), "+v"(ac2[1][4]));
#pragma unroll
  for (int ct = 0; ct < 5; ++ct) *(LAS f32x4*)(lds + G2_PART + ((3 * 4 + wave) * 5 + ct) * 1024 + lane * 16) = ac2[1][ct];
  waitv<0>();
#pragma unroll
  for (int u = 0; u < 3; ++u) {
    asm volatile("" : "+v"(yv[u]));
    const int idx = u * 256 + wave * 64 + lane, d = idx >> 3, cch = idx & 7;
    if (idx < 640) { *(LAS v2u*)(lds + G2_Y + d * 136 + cch * 16) = v2u{yv[u].x, yv[u].y}; *(LAS v2u*)(lds + G2_Y + d * 136 + cch * 16 + 8) = v2u{yv[u].z, yv[u].w}; }
  }
  asm volatile("" : "+v"(gtu));
#pragma unroll
  for (int ct = 0; ct < 5; ++ct) asm volatile("" : "+v"(qq[ct]));
  waitl<0>(); sbar();
  STAMP(10);
  const float gt = __builtin_bit_cast(float, gtu);
#pragma unroll
  for (int ct = 0; ct < 5; ++ct) {
    f32x4 b = *(LAS f32x4*)(lds + G2_PART + ((wave * 4 + 0) * 5 + ct) * 1024 + lane * 16);
#pragma unroll
    for (int w = 1; w < 4; ++w) b += *(LAS f32x4*)(lds + G2_PART + ((wave * 4 + w) * 5 + ct) * 1024 + lane * 16);
    const v2u yy = ldstr(lds + G2_Y + (16 * ct + 4 * g + q4) * 136 + (16 * wave + 4 * p4) * 2);
    const float o0 = gt * (blo(qq[ct].x) * blo(yy.x)) + (1.f - gt) * b[0];
    const float o1 = gt * (bhi(qq[ct].x) * bhi(yy.x)) + (1.f - gt) * b[1];
    const float o2 = gt * (blo(qq[ct].y) * blo(yy.y)) + (1.f - gt) * b[2];
    const float o3 = gt * (bhi(qq[ct].y) * bhi(yy.y)) + (1.f - gt) * b[3];
    *(GAS v2u*)(p.out + s * D + 80 * nt + 16 * ct + 4 * g) = v2u{pk2(o0, o1), pk2(o2, o3)};
  }
  if (bump) {
    unsigned bad = 0;
#pragma unroll
    for (int e = 0; e < 13; ++e) { asm volatile("" : "+v"(fe[e])); bad |= fe[e] ^ tag; }
    if (__builtin_amdgcn_ballot_w64(bad != 0) != 0) {
#pragma unroll 1
      for (int e = 0; e < 13; ++e) { const unsigned fo = (e < 12 ? C_FH + (e * 64 + lane) * FS : C_FKV + lane * FS); unsigned f; ld32_sc1(f, fo, rsC); waitv<0>(); poll_finish(f, fo, rsC, tag); }
    }
    if (lane == 0) st32_sc1(tag, C_EPOCH, rsC);
  }
  STAMP(11);
  STAMP_FLUSH(); FT_FLUSH();
}

__global__ __launch_bounds__(256, 1) void fused(Params p) {
  extern __shared__ __attribute__((aligned(16))) char smem[];
  LAS char* lds = (LAS char*)smem;
  const int lane = threadIdx.x & 63, wave = __builtin_amdgcn_readfirstlane(threadIdx.x >> 6);
  const int x = blockIdx.x & 7, rp_ = blockIdx.x >> 3, r = PREP_FIRST ? (rp_ < 8 ? rp_ + 24 : rp_ - 8) : rp_;
  unsigned pf = 0;
  {
    unsigned long long pc; unsigned len;
    asm volatile("s_getpc_b64 %0\nLpf_here:\n\ts_mov_b32 %1, Lpf_end-Lpf_here" : "=s"(pc), "=s"(len));
    const unsigned off = (rp_ * 4 + wave) * 1024 + lane * 16;
    const rsrc_t rs = mkrs((const void*)pc, len);
    v4u t;
    asm volatile("buffer_load_dwordx4 %0, %1, %2, 0 offen" : "=v"(t) : "v"(off), "s"(rs));
    if (r < 24) { body<true>(p, lds, x, r, wave, lane); }
    else { body<false>(p, lds, x, r, wave, lane); }
    asm volatile("s_waitcnt vmcnt(0)\nLpf_end:" : "+v"(t));
    pf = t.x;
  }
  asm volatile("" ::"v"(pf));
}

extern "C" void kernel_launch(void* const* d_in, const int* in_sizes, int n_in, void* d_out, int out_size, void* d_ws, size_t ws_size, hipStream_t stream) {
  Params p;
  p.q = (const u16*)d_in[0]; p.k = (const u16*)d_in[1]; p.v = (const u16*)d_in[2]; p.sk = (const u16*)d_in[3];
  p.h = (const u16*)d_in[4]; p.Dv = (const u16*)d_in[5]; p.w1 = (const u16*)d_in[7]; p.w2 = (const u16*)d_in[8]; p.gw = (const u16*)d_in[9];
  p.out = (u16*)d_out; p.ws = (char*)d_ws;
  (void)hipFuncSetAttribute((const void*)fused, hipFuncAttributeMaxDynamicSharedMemorySize, LDS_BYTES);
  fused<<<256, 256, LDS_BYTES, stream>>>(p);
}
```

```cpp
#include <hip/hip_runtime.h>
#include <type_traits>
#define SPIN_SLEEP 4
#define FSTR 64
#include <stdint.h>

typedef unsigned short u16;
typedef unsigned v4u __attribute__((ext_vector_type(4)));
typedef unsigned v2u __attribute__((ext_vector_type(2)));
typedef float f32x4 __attribute__((ext_vector_type(4)));
typedef float f32x2 __attribute__((ext_vector_type(2)));
typedef __bf16 bf16x8 __attribute__((ext_vector_type(8)));
typedef __bf16 bf16x2 __attribute__((ext_vector_type(2)));
typedef short v4s __attribute__((ext_vector_type(4)));
typedef short v8s __attribute__((ext_vector_type(8)));
#define GAS __attribute__((address_space(1)))
#define LAS __attribute__((address_space(3)))
#define DEV __device__ __forceinline__
typedef __amdgpu_buffer_rsrc_t rsrc_t;

constexpr int S = 2048, D = 640, F = 1536;

template <int N> DEV void waitv() { asm volatile("s_waitcnt vmcnt(%0)" ::"i"(N) : "memory"); }
template <int N> DEV void waitl() { asm volatile("s_waitcnt lgkmcnt(%0)" ::"i"(N) : "memory"); }
DEV void sbar() { asm volatile("s_barrier" ::: "memory"); }
#define SCHED() __builtin_amdgcn_sched_barrier(0)
DEV rsrc_t mkrs(const void* p, unsigned bytes) { return __builtin_amdgcn_make_buffer_rsrc((void*)p, 0, bytes, 0x00020000); }
DEV f32x4 mfma(v4u a, v4u b, f32x4 c) {
  return __builtin_amdgcn_mfma_f32_16x16x32_bf16(__builtin_bit_cast(bf16x8, a), __builtin_bit_cast(bf16x8, b), c, 0, 0, 0);
}
DEV v4u lds128(LAS const char* p) { return *(LAS const v4u*)p; }
DEV v2u lds64(LAS const char* p) { return *(LAS const v2u*)p; }
DEV v2u ldstr(LAS const char* p) { return __builtin_bit_cast(v2u, __builtin_amdgcn_ds_read_tr16_b64_v4i16((LAS v4s*)p)); }
DEV v4u cat(v2u a, v2u b) { return v4u{a.x, a.y, b.x, b.y}; }
DEV unsigned pk2(float a, float b) { bf16x2 r = __builtin_convertvector(f32x2{a, b}, bf16x2); return __builtin_bit_cast(unsigned, r); }
DEV float blo(unsigned d) { return __builtin_bit_cast(float, d << 16); }
DEV float bhi(unsigned d) { return __builtin_bit_cast(float, d & 0xffff0000u); }
DEV void setm0(unsigned v) { asm volatile("s_mov_b32 m0, %0\n\ts_nop 0" ::"s"(v)); }
template <int OFF, int NT = 0> DEV void dma16(unsigned voff, rsrc_t rs, unsigned soff) {
  if constexpr (NT) asm volatile("buffer_load_dwordx4 %0, %1, %2 offen offset:%3 nt lds" ::"v"(voff), "s"(rs), "s"(soff), "i"(OFF));
  else asm volatile("buffer_load_dwordx4 %0, %1, %2 offen offset:%3 lds" ::"v"(voff), "s"(rs), "s"(soff), "i"(OFF));
}
typedef float f32x16 __attribute__((ext_vector_type(16)));
DEV f32x16 mfma32(v4u a, v4u b, f32x16 c) {
  return __builtin_amdgcn_mfma_f32_32x32x16_bf16(__builtin_bit_cast(bf16x8, a), __builtin_bit_cast(bf16x8, b), c, 0, 0, 0);
}
template <int IMM> DEV void setm0_add(unsigned base) { asm volatile("s_add_u32 m0, %0, %1" ::"s"(base), "i"(IMM) : "scc"); }
template <int I, int N, class Fn> DEV void static_for(Fn&& f) { if constexpr (I < N) { f(std::integral_constant<int, I>{}); static_for<I + 1, N>(f); } }
DEV void mfma_ip(f32x4& c, const v4u& a, const v4u& b) { asm volatile("v_mfma_f32_16x16x32_bf16 %0, %1, %2, %0" : "+v"(c) : "v"(a), "v"(b)); }
DEV void mfma_ipA(f32x4& c, const v4u& a, const v4u& b) { asm volatile("v_mfma_f32_16x16x32_bf16 %0, %1, %2, %0" : "+v"(c) : "a"(a), "v"(b)); }
DEV void mfma_z(f32x4& c, const v4u& a, const v4u& b) { asm volatile("v_mfma_f32_16x16x32_bf16 %0, %1, %2, 0" : "=&v"(c) : "v"(a), "v"(b)); }
DEV void mfma_zA(f32x4& c, const v4u& a, const v4u& b) { asm volatile("v_mfma_f32_16x16x32_bf16 %0, %1, %2, 0" : "=&v"(c) : "a"(a), "v"(b)); }
template <int AUX> DEV void ldA(v4u& d, unsigned voff, rsrc_t rs, unsigned soff) {
  if constexpr (AUX) asm volatile("buffer_load_dwordx4 %0, %1, %2, %3 offen sc1" : "=a"(d) : "v"(voff), "s"(rs), "s"(soff));
  else asm volatile("buffer_load_dwordx4 %0, %1, %2, %3 offen" : "=a"(d) : "v"(voff), "s"(rs), "s"(soff));
}
template <int AUX> DEV void ldV(v4u& d, unsigned voff, rsrc_t rs, unsigned soff) {
  if constexpr (AUX == 2) asm volatile("buffer_load_dwordx4 %0, %1, %2, %3 offen nt" : "=v"(d) : "v"(voff), "s"(rs), "s"(soff));
  else if constexpr (AUX == 16) asm volatile("buffer_load_dwordx4 %0, %1, %2, %3 offen sc1" : "=v"(d) : "v"(voff), "s"(rs), "s"(soff));
  else asm volatile("buffer_load_dwordx4 %0, %1, %2, %3 offen" : "=v"(d) : "v"(voff), "s"(rs), "s"(soff));
}
DEV void nop8() { asm volatile("s_nop 7"); }

constexpr size_t WS_CTRL = 0;
constexpr size_t WS_KVT = 262144;
constexpr size_t WS_YT = WS_KVT + (size_t)D * S * 2;
constexpr size_t WS_W2T = WS_YT + (size_t)D * S * 2;
constexpr size_t WS_HF = WS_W2T + (size_t)8 * 48 * 5 * 1024;
constexpr size_t WS_G = WS_HF + (size_t)128 * 48 * 1024;
constexpr size_t WS_END = WS_G + S * 4;
constexpr unsigned C_EPOCH = 0, C_TMO = 256;
constexpr unsigned FS = FSTR;
constexpr unsigned C_FW = 4096;
constexpr unsigned C_FKV = C_FW + 64 * FS;
constexpr unsigned C_FH = C_FKV + 64 * FS;
constexpr unsigned C_FY = C_FH + 768 * FS;
constexpr unsigned C_BYTES = 262144;
static_assert(C_FY + 1024 * FS <= C_BYTES, "");

#define NSTAMP 16
#define STAMP_OFF (size_t)(32u << 20)
#define STAMP_DECL
#define STAMP(i)
#define STAMP_FLUSH()
#define FT_DECL
#define FT(i)
#define FT_FLUSH()

struct Params {
  const u16 *q, *k, *v, *sk, *h, *Dv, *w1, *w2, *gw;
  u16* out;
  char* ws;
  const u16* bx;
};

constexpr int LDS_BYTES = 160 * 1024;
constexpr int SC1 = 16;
#define NT_X 0
#define NT_W1 0
#define NT_P 0
constexpr int AUXP = NT_P ? 2 : 0;
#define IC(n) std::integral_constant<int, (n)>{}

DEV void ld32_sc1(unsigned& v, unsigned voff, rsrc_t rs) { asm volatile("buffer_load_dword %0, %1, %2, 0 offen sc1" : "=v"(v) : "v"(voff), "s"(rs)); }
DEV void st32_sc1(unsigned v, unsigned voff, rsrc_t rs) { asm volatile("buffer_store_dword %0, %1, %2, 0 offen sc1" ::"v"(v), "v"(voff), "s"(rs)); }
DEV void poll_finish(unsigned v, unsigned voff, rsrc_t rsC, unsigned tag) {
  asm volatile("" : "+v"(v));
  if (__builtin_amdgcn_ballot_w64(v != tag) == 0) return;
#pragma unroll 1
  for (int spins = 0; spins < (1 << 15); ++spins) {
    __builtin_amdgcn_s_sleep(SPIN_SLEEP);
    ld32_sc1(v, voff, rsC);
    asm volatile("s_waitcnt vmcnt(0)" : "+v"(v)::"memory");
    if (__builtin_amdgcn_ballot_w64(v != tag) == 0) return;
  }
  st32_sc1(1u, C_TMO, rsC);
}

DEV void poll_finish2(unsigned a, unsigned oa, unsigned b, unsigned ob, rsrc_t rsC, unsigned tag) {
  asm volatile("" : "+v"(a), "+v"(b));
  if (__builtin_amdgcn_ballot_w64(a != tag || b != tag) == 0) return;
#pragma unroll 1
  for (int spins = 0; spins < (1 << 15); ++spins) {
    __builtin_amdgcn_s_sleep(SPIN_SLEEP);
    ld32_sc1(a, oa, rsC); ld32_sc1(b, ob, rsC);
    asm volatile("s_waitcnt vmcnt(0)" : "+v"(a), "+v"(b)::"memory");
    if (__builtin_amdgcn_ballot_w64(a != tag || b != tag) == 0) return;
  }
  st32_sc1(1u, C_TMO, rsC);
}

DEV void mfma32_ip(f32x16& c, const v4u& a, const v4u& b) { asm volatile("v_mfma_f32_32x32x16_bf16 %0, %1, %2, %0" : "+v"(c) : "v"(a), "v"(b)); }
DEV void mfma32_z(f32x16& c, const v4u& a, const v4u& b) { asm volatile("v_mfma_f32_32x32x16_bf16 %0, %1, %2, 0" : "=&v"(c) : "v"(a), "v"(b)); }

constexpr int KV_BASE = 32768, KV_GS = 1152, KV_V = 9 * KV_GS, KV_CG = 17 * KV_GS;
constexpr int CV_BASE = 32768;
constexpr int CV_HC = 4160;
constexpr int CV_U = 4 * CV_HC;
constexpr int CV_CH = CV_U + 4608;
constexpr int CV_EX = CV_BASE + 4 * CV_CH;
constexpr int G2_PART = 0;
constexpr int G2_Y = 81920;
constexpr int HW = 24;

template <int AUX> DEV void ldV2(v2u& d, unsigned voff, rsrc_t rs, unsigned soff) {
  if constexpr (AUX == 2) asm volatile("buffer_load_dwordx2 %0, %1, %2, %3 offen nt" : "=v"(d) : "v"(voff), "s"(rs), "s"(soff));
  else if constexpr (AUX == 16) asm volatile("buffer_load_dwordx2 %0, %1, %2, %3 offen sc1" : "=v"(d) : "v"(voff), "s"(rs), "s"(soff));
  else asm volatile("buffer_load_dwordx2 %0, %1, %2, %3 offen" : "=v"(d) : "v"(voff), "s"(rs), "s"(soff));
}
DEV void dma16s(unsigned voff, rsrc_t rs, unsigned soff, unsigned m0v) {
  if constexpr (NT_P) asm volatile("s_mov_b32 m0, %3\n\ts_nop 0\n\tbuffer_load_dwordx4 %0, %1, %2 offen nt lds" ::"v"(voff), "s"(rs), "s"(soff), "s"(m0v));
  else asm volatile("s_mov_b32 m0, %3\n\ts_nop 0\n\tbuffer_load_dwordx4 %0, %1, %2 offen lds" ::"v"(voff), "s"(rs), "s"(soff), "s"(m0v));
}
DEV float dot2(unsigned a, unsigned b, float c) { return __builtin_amdgcn_fdot2_f32_bf16(__builtin_bit_cast(bf16x2, a), __builtin_bit_cast(bf16x2, b), c, false); }
DEV unsigned mktag(unsigned& epoch) {
  asm volatile("" : "+v"(epoch));
  return __builtin_amdgcn_readfirstlane(epoch * 0x9E3779B1u + 0x7F4A7C15u);
}

template <int NR, class ST> DEV void kv_math(const v4u (&kk)[NR + 3], const v4u (&vv)[NR], const v4u (&ww)[4], ST&& st) {
#pragma unroll
  for (int cp = 0; cp < 4; ++cp) {
    f32x2 w[4];
    w[0] = f32x2{blo(ww[cp].x), blo(ww[cp].z)}; w[1] = f32x2{bhi(ww[cp].x), bhi(ww[cp].z)};
    w[2] = f32x2{blo(ww[cp].y), blo(ww[cp].w)}; w[3] = f32x2{bhi(ww[cp].y), bhi(ww[cp].w)};
    f32x2 kf[NR + 3];
#pragma unroll
    for (int i = 0; i < NR + 3; ++i) kf[i] = f32x2{blo(kk[i][cp]), bhi(kk[i][cp])};
    f32x2 r[NR];
#pragma unroll
    for (int i = 0; i < NR; ++i) {
      f32x2 a = kf[i + 3] * w[0];
      a = __builtin_elementwise_fma(kf[i + 2], w[1], a);
      a = __builtin_elementwise_fma(kf[i + 1], w[2], a);
      a = __builtin_elementwise_fma(kf[i], w[3], a);
      r[i] = a * f32x2{blo(vv[i][cp]), bhi(vv[i][cp])};
    }
    st(cp, r);
  }
}

#define Q0 7
#define ROT 0
#define PREP_FIRST 0
#define HPUB 90
#define WPRE_ 16
constexpr int WPRE = WPRE_;
constexpr int HW0 = 16, HWM = 24;
constexpr int PW_BASE = 131072, PW_SZ = 3072;
constexpr int g2_issued(int i) { int r = HW0; for (int s = 0; s < i; ++s) r += (r - s < HWM) ? ((r + 1 - s < HWM) ? 2 : 1) : 0; return r < 48 ? r : 48; }

template <int OFF, int AUX> DEV void ldVo(v4u& d, unsigned voff, rsrc_t rs, unsigned soff) {
  static_assert(AUX == 16, "");
  asm volatile("buffer_load_dwordx4 %0, %1, %2, %3 offen offset:%4 sc1" : "=v"(d) : "v"(voff), "s"(rs), "s"(soff), "i"(OFF));
}
template <int OFF> DEV void ldAo(v4u& d, unsigned voff, rsrc_t rs, unsigned soff) {
  asm volatile("buffer_load_dwordx4 %0, %1, %2, %3 offen offset:%4 sc1" : "=a"(d) : "v"(voff), "s"(rs), "s"(soff), "i"(OFF));
}

template <bool G1R>
DEV void body(const Params& p, LAS char* lds, const int x, const int r, const int wave, const int lane) {
  const int g = lane >> 4, q4 = (lane >> 2) & 3, p4 = lane & 3, c16 = lane & 15;
  STAMP_DECL; FT_DECL;
  STAMP(0); FT(13);
  const rsrc_t rsC = mkrs(p.ws + WS_CTRL, C_BYTES);
  unsigned epoch, tag;
  ld32_sc1(epoch, C_EPOCH, rsC);
  const int cl = G1R ? (wave >> 1) : wave, half = G1R ? (wave & 1) : 0;
  const int ch = (G1R ? 80 * x + 2 * r : 80 * x + 48 + 4 * (r - 24)) + cl;
  const unsigned cb = CV_BASE + cl * CV_CH;
  constexpr int NCHK = G1R ? 2 : 4;
  unsigned dv;
  { const rsrc_t rsD = mkrs(p.Dv, D * 2); asm volatile("buffer_load_ushort %0, %1, %2, 0 offen" : "=v"(dv) : "v"(ch * 2), "s"(rsD)); }
  const rsrc_t rsHh = mkrs(p.h, D * S * 2), rsU = mkrs(p.ws + WS_KVT, D * S * 2), rsY = mkrs(p.ws + WS_YT, D * S * 2);
  const rsrc_t rsW2 = mkrs(p.ws + WS_W2T, 8 * 48 * 5 * 1024), rsH = mkrs(p.ws + WS_HF, 128 * 48 * 1024);
#define MAP42 1
  const int rb = MAP42 ? 8 * (x >> 1) + (r & 7) : 4 * x + (r & 3), nt = MAP42 ? 4 * (x & 1) + (r >> 3) : r >> 2;
  const unsigned vl = lane * 16;
  const unsigned sW = (nt * 48 + 12 * wave) * 5 * 1024;
  const unsigned foW = C_FW + lane * FS, foKV = C_FKV + lane * FS;
  v4u W[12][5];
  auto ldW = [&](auto I) { constexpr int i = I.value; if constexpr (i >= 0 && i < 60) ldAo<(i & 3) * 1024>(W[i / 5][i % 5], vl, rsW2, sW + (i & ~3) * 1024); };
  v4u hc[NCHK], uc[NCHK]; v2u hp0;
  auto ld_h = [&]() {
    ldV2<AUXP>(hp0, half > 0 ? (unsigned)(ch * 4096 + half * NCHK * 1024 - 8) : 0x80000000u, rsHh, 0);
#pragma unroll
    for (int i = 0; i < NCHK; ++i) ldV<AUXP>(hc[i], ch * 4096 + ((half * NCHK + i) * 64 + lane) * 16, rsHh, 0);
  };
  auto ld_u = [&]() {
#pragma unroll
    for (int i = 0; i < NCHK; ++i) ldV<SC1>(uc[i], ch * 4096 + ((half * NCHK + i) * 64 + lane) * 16, rsU, 0);
  };
  auto fill_h = [&]() {
    asm volatile("" : "+v"(hp0));
#pragma unroll
    for (int i = 0; i < NCHK; ++i) asm volatile("" : "+v"(hc[i]));
    unsigned e0 = hp0.x, e1 = hp0.y;
#pragma unroll
    for (int i = 0; i < NCHK; ++i) {
      const int n = (half * NCHK + i) * 64 + lane;
      const v4u cur = hc[i];
      const v2u prv = v2u{(unsigned)__builtin_amdgcn_update_dpp((int)e0, (int)cur.z, 0x138, 0xf, 0xf, false), (unsigned)__builtin_amdgcn_update_dpp((int)e1, (int)cur.w, 0x138, 0xf, 0xf, false)};
      e0 = __builtin_amdgcn_readlane(cur.z, 63); e1 = __builtin_amdgcn_readlane(cur.w, 63);
      const unsigned om2 = __builtin_amdgcn_alignbit(prv.y, prv.x, 16), om1 = __builtin_amdgcn_alignbit(cur.x, prv.y, 16);
      const unsigned o0 = __builtin_amdgcn_alignbit(cur.y, cur.x, 16), o1 = __builtin_amdgcn_alignbit(cur.z, cur.y, 16), o2 = __builtin_amdgcn_alignbit(cur.w, cur.z, 16);
      LAS char* dst = lds + cb + (n + 2) * 16;
      *(LAS v4u*)(dst) = cur;
      *(LAS v4u*)(dst + CV_HC) = v4u{om1, o0, o1, o2};
      *(LAS v4u*)(dst + 2 * CV_HC) = v4u{prv.y, cur.x, cur.y, cur.z};
      *(LAS v4u*)(dst + 3 * CV_HC) = v4u{om2, om1, o0, o1};
    }
    if (half == 0) {
      if (lane < 8) *(LAS v4u*)(lds + cb + (lane >> 1) * CV_HC + (lane & 1) * 16) = v4u{0, 0, 0, 0};
      if (lane >= 32 && lane < 62) *(LAS v4u*)(lds + cb + CV_U + (lane - 32) * 16) = v4u{0, 0, 0, 0};
    }
  };
  auto fill_u = [&]() {
#pragma unroll
    for (int i = 0; i < NCHK; ++i) {
      asm volatile("" : "+v"(uc[i]));
      *(LAS v4u*)(lds + cb + CV_U + 480 + ((half * NCHK + i) * 64 + lane) * 16) = uc[i];
    }
  };
  unsigned fw;

  if constexpr (G1R) {
    const int sb = 4 * (x >> 1) + (r & 3), fs = 6 * (x & 1) + (r >> 2);
    f32x16 acc1[2][2];
    const rsrc_t rsX = mkrs(p.q, S * D * 2), rsW = mkrs(p.w1, D * F * 2);
    unsigned vX[4], vW[4];
#pragma unroll
    for (int ii = 0; ii < 4; ++ii) {
      vX[ii] = (32 * wave + 8 * ii + (lane >> 3)) * 1280 + (((lane & 7) ^ (4 * (ii & 1) + g)) << 4) - 1024 * ii;
      vW[ii] = (16 * wave + 4 * ii + g) * 3072 + (((((c16 >> 1) ^ (g << 1)) << 1) | (c16 & 1)) << 4) - 1024 * ii;
    }
    const unsigned sX0 = sb * 128 * 1280, sW0 = fs * 256;
    const unsigned mX = wave * 4096, mW = 16384 + wave * 4096;
    static_for<0, 7>([&](auto Q) { constexpr int q = Q.value, t = q >> 1;
      setm0_add<t * 32768>((q & 1) ? mW : mX); asm volatile("s_nop 0");
      static_for<0, 4>([&](auto I) { constexpr int i = I.value;
        if constexpr (q & 1) dma16<1024 * i, NT_W1>(vW[i], rsW, sW0 + t * (64 * 3072)); else dma16<1024 * i, NT_X>(vX[i], rsX, sX0 + t * 128); });
    });
    FT(11);
    const int ws_ = wave & 1, wf = wave >> 1, h = lane >> 5, r32 = lane & 31;
    unsigned aX[4], aW[2];
#pragma unroll
    for (int ks = 0; ks < 4; ++ks) aX[ks] = (64 * ws_ + r32) * 128 + (((2 * ks + h) ^ ((r32 >> 1) & 7)) << 4);
#pragma unroll
    for (int ft = 0; ft < 2; ++ft) aW[ft] = 16384 + (8 * h + q4) * 256 + (((2 * (2 * wf + ft) + (g & 1)) ^ (q4 << 1)) << 5) + 8 * p4;
#pragma unroll
    for (int a = 0; a < 2; ++a)
#pragma unroll
      for (int b = 0; b < 2; ++b)
#pragma unroll
        for (int e = 0; e < 16; ++e) acc1[a][b][e] = 0.f;
    asm volatile("s_nop 1" : "+v"(acc1[0][0]), "+v"(acc1[0][1]), "+v"(acc1[1][0]), "+v"(acc1[1][1]));
    v4u A[2][2][2], B[2][2][2];
    v2u At;
    unsigned cX[2], cW[2];
    auto rd = [&](auto HF, auto SET, auto N) {
      constexpr int hf = HF.value, set = SET.value, n = N.value;
      constexpr int kk = n / 6, rr = n % 6, ks = 2 * hf + kk;
      if constexpr (rr == 0) B[set][kk][0] = lds128(lds + cX[kk]);
      else if constexpr (rr == 3) B[set][kk][1] = lds128(lds + cX[kk] + 32 * 128);
      else { constexpr int ft = rr / 3, jj = (rr % 3) - 1;
        v2u xx = ldstr(lds + cW[ft] + ks * 4096 + jj * 1024);
        if constexpr (jj == 0) At = xx; else A[set][kk][ft] = cat(At, xx); }
    };
    auto set_addr = [&](int hf, unsigned bo) { cX[0] = aX[2 * hf] + bo; cX[1] = aX[2 * hf + 1] + bo; cW[0] = aW[0] + bo; cW[1] = aW[1] + bo; };
    unsigned fk;
    waitv<20>(); sbar();
    STAMP(1);
    set_addr(0, 0);
    static_for<0, 12>([&](auto N) { rd(IC(0), IC(0), N); });
    SCHED();
    unsigned bo = 0;
    unsigned bW = 3 * 32768, bX = 4 * 32768;
    unsigned sWn = sW0 + 3 * (64 * 3072), sXn = sX0 + 4 * 128;
    auto stage = [&](auto DMA, int t) {
      constexpr bool dma = DMA.value;
      const unsigned nbo = bo == 4 * 32768 ? 0 : bo + 32768;
      set_addr(1, bo);
      static_for<0, 8>([&](auto M) {
        constexpr int m = M.value, kk = m >> 2, ft = (m >> 1) & 1, st = m & 1;
        mfma32_ip(acc1[ft][st], A[0][kk][ft], B[0][kk][st]);
        if constexpr (m < 6) { rd(IC(1), IC(1), IC(2 * m)); rd(IC(1), IC(1), IC(2 * m + 1)); }
        if constexpr (dma && m == 0) asm volatile("s_add_u32 m0, %0, %1" ::"s"(mW), "s"(bW) : "scc");
        if constexpr (dma && (m & 1)) dma16<1024 * (m >> 1), NT_W1>(vW[m >> 1], rsW, sWn);
        SCHED();
      });
      if constexpr (dma) { waitv<16>(); sbar(); }
      else { if (t == 7) waitv<8>(); else waitv<0>(); sbar(); }
      SCHED();
      set_addr(0, nbo);
      const unsigned sx = (dma && t < 6) ? sXn : 0x80000000u;
      static_for<0, 8>([&](auto M) {
        constexpr int m = M.value, kk = m >> 2, ft = (m >> 1) & 1, st = m & 1;
        mfma32_ip(acc1[ft][st], A[1][kk][ft], B[1][kk][st]);
        if constexpr (m < 6) { rd(IC(0), IC(0), IC(2 * m)); rd(IC(0), IC(0), IC(2 * m + 1)); }
        if constexpr (dma && m == 0) asm volatile("s_add_u32 m0, %0, %1" ::"s"(mX), "s"(bX) : "scc");
        if constexpr (dma && (m & 1)) dma16<1024 * (m >> 1), NT_X>(vX[m >> 1], rsX, sx);
        SCHED();
      });
      bo = nbo;
      bW = bW == 4 * 32768 ? 0 : bW + 32768; bX = bX == 4 * 32768 ? 0 : bX + 32768;
      sWn += 64 * 3072; sXn += 128;
    };
#pragma unroll 1
    for (int t = 0; t < 7; ++t) stage(std::true_type{}, t);
#pragma unroll 1
    for (int t = 7; t < 10; ++t) stage(std::false_type{}, t);
    asm volatile("s_nop 15" : "+v"(acc1[0][0]), "+v"(acc1[0][1]), "+v"(acc1[1][0]), "+v"(acc1[1][1]));
    sbar();
    STAMP(2);
    tag = mktag(epoch);
    ld_h();
    const unsigned vo = (g & 1) * (48 * 1024) + (c16 + 16 * h) * 16;
    static_for<0, 4>([&](auto Q) {
      constexpr int qd = Q.value, st = qd >> 1, ft = qd & 1;
      const f32x16 a = acc1[ft][st];
      unsigned o[8];
      static_for<0, 2>([&](auto G) {
        constexpr int gq = G.value;
        static_for<0, 4>([&](auto E) {
          constexpr int e = 8 * gq + 2 * E.value;
          const float r0 = a[e] * __builtin_amdgcn_rcpf(1.f + __builtin_amdgcn_exp2f(a[e] * -1.44269504088896f));
          const float r1 = a[e + 1] * __builtin_amdgcn_rcpf(1.f + __builtin_amdgcn_exp2f(a[e + 1] * -1.44269504088896f));
          o[e >> 1] = pk2(r0, r1);
        });
        SCHED();
      });
      const unsigned so = ((8 * sb + 4 * ws_ + 2 * st) * 48 + 4 * fs + 2 * wf + ft) * 1024;
      __builtin_amdgcn_raw_buffer_store_b128(v4u{o[0], o[1], o[4], o[5]}, rsH, vo, so, SC1);
      __builtin_amdgcn_raw_buffer_store_b128(v4u{o[2], o[3], o[6], o[7]}, rsH, vo + 512, so, SC1);
      SCHED();
      FT(32 + qd); SCHED();
    });
    ld32_sc1(fk, foKV, rsC); ld32_sc1(fw, foW, rsC);
    waitv<10>();
    fill_h();
    FT(54);
    waitv<0>();
    if (lane == 0) st32_sc1(tag, C_FH + ((sb * 2 + ws_) * 24 + fs * 2 + wf) * FS, rsC);
    FT(55);
    poll_finish(fk, foKV, rsC, tag);
    ld_u();
    FT(36);
    poll_finish(fw, foW, rsC, tag);
    FT(37);
    static_for<0, WPRE>([&](auto I) { ldW(I); });
    STAMP(3);
    waitv<WPRE>();
    fill_u();
    waitl<0>(); sbar();
    STAMP(4);
  } else {
    const int j = (r - 24) * 8 + x, rbk = j >> 1, cg0 = 5 * (j & 1);
    const rsrc_t rsK = mkrs(p.k, S * D * 2), rsV = mkrs(p.v, S * D * 2), rsS = mkrs(p.sk, D * 8), rsO = mkrs(p.ws + WS_KVT, D * S * 2);
    const unsigned vr = (lane >> 3) * 1280 + ((lane & 7) << 4), vr4 = vr ^ 64;
    auto dmaK = [&](int cgl, int G) {
      const int row = 64 * rbk - 8 + 8 * G;
      dma16s(((G >> 1) & 1) ? vr4 : vr, rsK, row >= 0 ? (unsigned)(row * 1280 + (cg0 + cgl) * 128) : 0x80000000u, KV_BASE + cgl * KV_CG + G * KV_GS);
    };
    auto dmaV = [&](int cgl, int G) {
      dma16s(((G >> 1) & 1) ? vr4 : vr, rsV, (64 * rbk + 8 * G) * 1280 + (cg0 + cgl) * 128, KV_BASE + cgl * KV_CG + KV_V + G * KV_GS);
    };
#pragma unroll
    for (int G = 0; G < 9; ++G) dmaK(wave, G);
#pragma unroll
    for (int G = 0; G < 8; ++G) dmaV(wave, G);
    const unsigned pw = PW_BASE + wave * PW_SZ;
    dma16s(lane < 32 ? (unsigned)((cg0 + wave) * 512 + lane * 16) : (lane < 40 ? (unsigned)((64 * (cg0 + 4) + 16 * wave) * 8 + (lane - 32) * 16) : 0x80000000u), rsS, 0, pw);
    dmaK(4, wave); dmaK(4, wave + 4); dmaV(4, wave); dmaV(4, wave + 4);
    if (wave == 0) dmaK(4, 8);
    const int cc = lane >> 3, rg = lane & 7;
    FT(10);
    waitv<4>();
    STAMP(1);
    {
      const unsigned kb = KV_BASE + wave * KV_CG;
      const unsigned a0 = kb + rg * KV_GS + ((cc ^ (4 * ((rg >> 1) & 1))) << 4), a1 = kb + (rg + 1) * KV_GS + ((cc ^ (4 * (((rg + 1) >> 1) & 1))) << 4);
      v4u kk[11], vv[8], ww[4];
#pragma unroll
      for (int i = 0; i < 4; ++i) ww[i] = lds128(lds + pw + cc * 64 + i * 16);
#pragma unroll
      for (int i = 0; i < 11; ++i) kk[i] = i < 3 ? lds128(lds + a0 + (5 + i) * 128) : lds128(lds + a1 + (i - 3) * 128);
#pragma unroll
      for (int i = 0; i < 8; ++i) vv[i] = lds128(lds + a0 + KV_V + i * 128);
      const unsigned ob = (64 * (cg0 + wave) + 8 * cc) * 4096 + (64 * rbk + 8 * rg) * 2;
      kv_math<8>(kk, vv, ww, [&](int cp, const f32x2 (&rr)[8]) {
#pragma unroll
        for (int c = 0; c < 2; ++c) {
          v4u o{pk2(rr[0][c], rr[1][c]), pk2(rr[2][c], rr[3][c]), pk2(rr[4][c], rr[5][c]), pk2(rr[6][c], rr[7][c])};
          __builtin_amdgcn_raw_buffer_store_b128(o, rsO, ob + (2 * cp + c) * 4096, 0, SC1);
        }
      });
    }
    const int id = 4 * j + wave, pk = id / 5, blk = id % 5;
    const rsrc_t rsWs = mkrs(p.w2, F * D * 2);
    const unsigned ldsb = wave * 8192;
#pragma unroll
    for (int i = 0; i < 8; ++i) {
      const int row = 4 * i + g;
      dma16s((32 * pk + row) * 1280 + blk * 256 + (((((c16 >> 1) ^ (row & 7)) << 1) | (c16 & 1)) << 4), rsWs, id < 240 ? 0u : 0x80000000u, ldsb + i * 1024);
    }
    waitv<16>(); sbar();
    STAMP(2);
    {
      const int cc4 = 2 * wave + (lane >> 5), rp = lane & 31;
      const unsigned kb = KV_BASE + 4 * KV_CG;
      v4u kk[5], vv[2], w4[4];
#pragma unroll
      for (int i = 0; i < 4; ++i) w4[i] = lds128(lds + pw + 512 + (lane >> 5) * 64 + i * 16);
#pragma unroll
      for (int i = 0; i < 5; ++i) { const int row = 2 * rp + 5 + i; kk[i] = lds128(lds + kb + (row >> 3) * KV_GS + (row & 7) * 128 + ((cc4 ^ (4 * ((row >> 4) & 1))) << 4)); }
#pragma unroll
      for (int i = 0; i < 2; ++i) { const int row = 2 * rp + i; vv[i] = lds128(lds + kb + KV_V + (row >> 3) * KV_GS + (row & 7) * 128 + ((cc4 ^ (4 * ((row >> 4) & 1))) << 4)); }
      const unsigned ob = (64 * (cg0 + 4) + 8 * cc4) * 4096 + (64 * rbk + 2 * rp) * 2;
      kv_math<2>(kk, vv, w4, [&](int cp, const f32x2 (&rr)[2]) {
#pragma unroll
        for (int c = 0; c < 2; ++c) __builtin_amdgcn_raw_buffer_store_b32(pk2(rr[0][c], rr[1][c]), rsO, ob + (2 * cp + c) * 4096, 0, SC1);
      });
    }
    FT(11);
    waitv<8>();
    {
      const rsrc_t rsWo = mkrs(p.ws + WS_W2T, 8 * 48 * 5 * 1024);
      const int r0 = 4 * g + q4;
#pragma unroll
      for (int i = 0; i < 8; ++i) {
        const unsigned a = ldsb + r0 * 256 + ((i ^ (r0 & 7)) << 5) + 8 * p4;
        const v4u fr = cat(ldstr(lds + a), ldstr(lds + a + 4096));
        const int ctg = 8 * blk + i, ntt = ctg / 5, ct = ctg % 5;
        __builtin_amdgcn_raw_buffer_store_b128(fr, rsWo, vl, id < 240 ? (unsigned)(((ntt * 48 + pk) * 5 + ct) * 1024) : 0x80000000u, SC1);
      }
    }
    FT(0);
    waitv<8>();
    waitl<0>(); sbar();
    tag = mktag(epoch);
    if (threadIdx.x == 0) st32_sc1(tag, C_FKV + j * FS, rsC);
    STAMP(3);
    ld_h();
    waitv<5>(); sbar();
    if (threadIdx.x == 0) st32_sc1(tag, C_FW + j * FS, rsC);
    unsigned fk;
    ld32_sc1(fk, foKV, rsC);
    FT(3);
    waitv<1>();
    waitv<0>();
    fill_h();
    FT(2);
    poll_finish(fk, foKV, rsC, tag);
    FT(4);
    ld_u();
    const rsrc_t rsX = mkrs(p.q, S * D * 2), rsG = mkrs(p.gw, D * 2), rsGo = mkrs(p.ws + WS_G, S * 4);
    const int grow = 256 * x + 32 * (r - 24) + 8 * wave + (lane >> 3), part = lane & 7;
    v4u xx[10];
    dma16s(lane * 16, rsG, 0, pw + 1024); dma16s(1024 + lane * 16, rsG, 0, pw + 2048);
#pragma unroll
    for (int i = 0; i < 10; ++i) ldV<0>(xx[i], grow * 1280 + i * 128 + part * 16, rsX, 0);
    waitv<12>();
    fill_u();
    ld32_sc1(fw, foW, rsC);
    waitv<1>();
    {
      float a0 = 0.f, a1 = 0.f;
#pragma unroll
      for (int i = 0; i < 10; ++i) {
        asm volatile("" : "+v"(xx[i]));
        const v4u gg = lds128(lds + pw + 1024 + (i * 8 + part) * 16);
        a0 = dot2(xx[i].x, gg.x, a0); a1 = dot2(xx[i].y, gg.y, a1); a0 = dot2(xx[i].z, gg.z, a0); a1 = dot2(xx[i].w, gg.w, a1);
      }
      float a = a0 + a1;
      a += __shfl_xor(a, 1); a += __shfl_xor(a, 2); a += __shfl_xor(a, 4);
      const float gt = __builtin_amdgcn_rcpf(1.f + __builtin_amdgcn_exp2f(a * -1.44269504088896f));
      if (part == 0) __builtin_amdgcn_raw_buffer_store_b32(__builtin_bit_cast(unsigned, gt), rsGo, grow * 4, 0, SC1);
    }
    FT(5);
    waitl<0>();
    STAMP(4);
  }
  STAMP(5);
  constexpr int NZ = G1R ? 4 : 8;
  constexpr int W0 = G1R ? WPRE : 0, WG0 = G1R ? 0 : 14;
  auto wcount = [](int gap) constexpr { return gap < WG0 ? 0 : (G1R ? ((gap - WG0) * (60 - WPRE)) / 140 + 1 : (gap - WG0) / 4 + 1); };
  const unsigned foH = C_FH + (((rb >> 1) * 2 + (rb & 1)) * 24 + (lane < 24 ? lane : 0)) * FS;
  unsigned fh;
  constexpr int GPOLL = NZ * 36 - 12;
  f32x4 acc[8];
  {
    const unsigned aA = cb + q4 * CV_HC + 56 - 16 * g + 8 * p4 + 256 * half;
    const unsigned aB = cb + CV_U + 448 + 32 * c16 + 16 * g - 256 * half - 64 * (NZ - 1);
    v4u A[8], B[8];
#pragma unroll
    for (int i = 0; i < 8; ++i) {
      B[7 - i] = lds128(lds + aB + 64 * (NZ - 1) + 512 * (7 - i));
      A[i] = cat(ldstr(lds + aA + 512 * i + 8), ldstr(lds + aA + 512 * i));
    }
    SCHED();
    static_for<0, NZ>([&](auto Z) {
      constexpr int z = Z.value;
      static_for<0, 8>([&](auto I) {
        constexpr int i = I.value;
        static_for<0, 8 - i>([&](auto DD) {
          constexpr int d = 7 - i - DD.value;
          constexpr int gap = z * 36 + (i * (17 - i)) / 2 + DD.value;
          if constexpr (z == 0 && i == 0) mfma_z(acc[i + d], A[i], B[d]); else mfma_ip(acc[i + d], A[i], B[d]);
          if constexpr (d == 7 - i && z + 1 < NZ) B[7 - i] = lds128(lds + aB + 64 * (NZ - 2 - z) + 512 * (7 - i));
          if constexpr (!G1R && gap == WG0 - 2) { waitv<0>(); poll_finish(fw, foW, rsC, tag); }
          if constexpr (wcount(gap) > wcount(gap - 1)) ldW(IC(W0 + wcount(gap) - 1));
          if constexpr (gap == GPOLL) ld32_sc1(fh, foH, rsC);
          SCHED();
        });
        if constexpr (i == 7) { FT(40 + z); SCHED(); }
        if constexpr (z + 1 < NZ) { A[i] = cat(ldstr(lds + aA + 64 * (z + 1) + 512 * i + 8), ldstr(lds + aA + 64 * (z + 1) + 512 * i)); SCHED(); }
      });
    });
  }
  static_assert(W0 + wcount(NZ * 36 - 1) >= 60, "all W2 fragments issued");
  STAMP(6);
  constexpr int WAFT = 60 - (W0 + wcount(GPOLL));
  waitv<(WAFT > 0 ? WAFT : 0)>();
  FT(48);
  poll_finish(fh, foH, rsC, tag);
  FT(49);
  v4u Hf[HWM];
  const unsigned sH = (4 * rb * 48 + 12 * wave) * 1024;
  auto ldH = [&](auto N) { constexpr int n = N.value; if constexpr (n < 48) ldVo<(n & 3) * 1024, SC1>(Hf[n % HWM], vl, rsH, sH + ((n / 12) * 48 + ((n % 12) & ~3)) * 1024); };
  static_for<0, HW0>([&](auto I) { ldH(I); });
  FT(14);
  asm volatile("s_nop 7" : "+v"(acc[0]), "+v"(acc[1]), "+v"(acc[2]), "+v"(acc[3]), "+v"(acc[4]), "+v"(acc[5]), "+v"(acc[6]), "+v"(acc[7]));
  SCHED();
  int ctb = 0;
  if constexpr (G1R) {
    LAS char* ex = lds + CV_EX + cl * 8192;
    f32x4 keep[4];
#pragma unroll
    for (int i = 0; i < 4; ++i) {
      f32x4 lo = acc[i], hi = acc[4 + i];
      asm volatile("" : "+v"(lo), "+v"(hi));
      *(LAS f32x4*)(ex + half * 4096 + i * 1024 + lane * 16) = half == 0 ? hi : lo;
      keep[i] = half == 0 ? lo : hi;
    }
    waitl<0>(); sbar();
#pragma unroll
    for (int i = 0; i < 4; ++i) acc[i] = keep[i] + *(LAS f32x4*)(ex + (1 - half) * 4096 + i * 1024 + lane * 16);
    ctb = 4 * half;
  }
  FT(51);
  constexpr int NT = G1R ? 4 : 8, NYS = NT / 2;
  {
    asm volatile("" : "+v"(dv));
    const float Dc = blo(dv);
#pragma unroll
    for (int i = 0; i < NT; i += 2) {
      unsigned w0[2][2];
#pragma unroll
      for (int e = 0; e < 2; ++e) {
        const int ct = ctb + i + e;
        const f32x4 a = acc[i + e];
        const v2u uu = lds64(lds + cb + CV_U + 480 + 512 * ct + 32 * c16 + 8 * g);
        w0[e][0] = pk2(fmaf(blo(uu.x), Dc, a[0]), fmaf(bhi(uu.x), Dc, a[1]));
        w0[e][1] = pk2(fmaf(blo(uu.y), Dc, a[2]), fmaf(bhi(uu.y), Dc, a[3]));
      }
      auto s0 = __builtin_amdgcn_permlane16_swap(w0[0][0], w0[1][0], false, false);
      auto s1 = __builtin_amdgcn_permlane16_swap(w0[0][1], w0[1][1], false, false);
      const int ct = ctb + i + (g & 1);
      __builtin_amdgcn_raw_buffer_store_b128(v4u{s0[0], s1[0], s0[1], s1[1]}, rsY, ch * 4096 + 512 * ct + 32 * c16 + 8 * (g & 2), 0, SC1);
    }
  }
  FT(52);
  waitl<0>(); sbar();
  STAMP(7);
  unsigned fe[13];
  const bool bump = blockIdx.x == 0 && wave == 0;
  f32x4 ac2[2][5];
  const unsigned foY0 = C_FY + (nt * 128 + lane) * FS, foY1 = foY0 + 64 * FS;
  const unsigned foG = C_FY + ((rb >> 2) * 128 + 96 + (lane & 31)) * FS;
  unsigned fy0, fy1, fg, gtu;
  v4u yv[3]; v2u qq[5];
  const int s = 64 * rb + 16 * wave + c16;
#define SF_ 5
  constexpr int SF = SF_;
  constexpr int SP = 25;
  constexpr int SY = 40;
  static_assert(g2_issued(SP) == 48 && SY >= SP + 8, "");
  auto extras_at = [](int st) constexpr { return (st == SF ? 1 : 0) + ((st >= SP && st < SP + 8) ? 1 : 0) + (st == SY ? 4 : 0); };
  static_for<0, 48>([&](auto I) {
    constexpr int i = I.value, rt = i / 12, k = i % 12, set = rt & 1;
    constexpr int younger = [&]() constexpr {
      int y = g2_issued(i) - (i + 1);
      if (i < HW0) y += NYS;
      int si = -1; while (i >= HW0 && g2_issued(si + 1) <= i) ++si;
      for (int st = (si < 0 ? 0 : si); st < i; ++st) y += extras_at(st);
      return y; }();
    waitv<(younger < 63 ? younger : 63)>();
    static_for<0, 5>([&](auto C) { constexpr int ct = C.value;
      if constexpr (k == 0) mfma_zA(ac2[set][ct], W[k][ct], Hf[i % HWM]); else mfma_ipA(ac2[set][ct], W[k][ct], Hf[i % HWM]);
      if constexpr (rt > 0 && k >= 1 && k <= 5 && ct == 3) *(LAS f32x4*)(lds + G2_PART + (((rt - 1) * 4 + wave) * 5 + (k - 1)) * 1024 + lane * 16) = ac2[set ^ 1][k - 1];
      if constexpr (ct == 1) { static_for<g2_issued(i), g2_issued(i + 1)>([&](auto N) { ldH(N); }); }
    });
    if constexpr (i == SF) {
      waitv<g2_issued(SF + 1) - HW0>();
      if (lane == 0) st32_sc1(tag, C_FY + ((x * 32 + r) * 4 + wave) * FS, rsC);
    }
    if constexpr (i == SP) ld32_sc1(fy0, foY0, rsC);
    if constexpr (i == SP + 1) ld32_sc1(fy1, foY1, rsC);
    if constexpr (i == SP + 2) ld32_sc1(fg, foG, rsC);
    if constexpr (i >= SP + 3 && i < SP + 8) { const rsrc_t rsQ = mkrs(p.q, S * D * 2); ldV2<AUXP>(qq[i - SP - 3], (s * D + 80 * nt + 16 * (i - SP - 3) + 4 * g) * 2, rsQ, 0); }
    if constexpr (i == 34) {
      if (bump) {
#pragma unroll
        for (int e = 0; e < 13; ++e) ld32_sc1(fe[e], e < 12 ? C_FH + (e * 64 + lane) * FS : C_FKV + lane * FS, rsC);
      }
    }
    if constexpr (i == SY) {
      waitv<5>();
      poll_finish2(fy0, foY0, fy1, foY1, rsC, tag);
      poll_finish(fg, foG, rsC, tag);
      { const rsrc_t rsG = mkrs(p.ws + WS_G, S * 4); ld32_sc1(gtu, s * 4, rsG); }
#pragma unroll
      for (int u = 0; u < 3; ++u) {
        const int idx = u * 256 + wave * 64 + lane, d = idx >> 3, cch = idx & 7;
        ldV<SC1>(yv[u], idx < 640 ? (unsigned)((80 * nt + d) * 4096 + rb * 128 + cch * 16) : 0x80000000u, rsY, 0);
      }
    }
    if constexpr (i % 4 == 3) { FT(16 + i / 4); }
    SCHED();
  });
  STAMP(9);
  asm volatile("s_nop 7" : "+v"(ac2[1][0]), "+v"(ac2[1][1]), "+v"(ac2[1][2]), "+v"(ac2[1][3]), "+v"(ac2[1][4]));
#pragma unroll
  for (int ct = 0; ct < 5; ++ct) *(LAS f32x4*)(lds + G2_PART + ((3 * 4 + wave) * 5 + ct) * 1024 + lane * 16) = ac2[1][ct];
  waitv<0>();
#pragma unroll
  for (int u = 0; u < 3; ++u) {
    asm volatile("" : "+v"(yv[u]));
    const int idx = u * 256 + wave * 64 + lane, d = idx >> 3, cch = idx & 7;
    if (idx < 640) { *(LAS v2u*)(lds + G2_Y + d * 136 + cch * 16) = v2u{yv[u].x, yv[u].y}; *(LAS v2u*)(lds + G2_Y + d * 136 + cch * 16 + 8) = v2u{yv[u].z, yv[u].w}; }
  }
  asm volatile("" : "+v"(gtu));
#pragma unroll
  for (int ct = 0; ct < 5; ++ct) asm volatile("" : "+v"(qq[ct]));
  waitl<0>(); sbar();
  STAMP(10);
  const float gt = __builtin_bit_cast(float, gtu);
#pragma unroll
  for (int ct = 0; ct < 5; ++ct) {
    f32x4 b = *(LAS f32x4*)(lds + G2_PART + ((wave * 4 + 0) * 5 + ct) * 1024 + lane * 16);
#pragma unroll
    for (int w = 1; w < 4; ++w) b += *(LAS f32x4*)(lds + G2_PART + ((wave * 4 + w) * 5 + ct) * 1024 + lane * 16);
    const v2u yy = ldstr(lds + G2_Y + (16 * ct + 4 * g + q4) * 136 + (16 * wave + 4 * p4) * 2);
    const float o0 = gt * (blo(qq[ct].x) * blo(yy.x)) + (1.f - gt) * b[0];
    const float o1 = gt * (bhi(qq[ct].x) * bhi(yy.x)) + (1.f - gt) * b[1];
    const float o2 = gt * (blo(qq[ct].y) * blo(yy.y)) + (1.f - gt) * b[2];
    const float o3 = gt * (bhi(qq[ct].y) * bhi(yy.y)) + (1.f - gt) * b[3];
    *(GAS v2u*)(p.out + s * D + 80 * nt + 16 * ct + 4 * g) = v2u{pk2(o0, o1), pk2(o2, o3)};
  }
  if (bump) {
    unsigned bad = 0;
#pragma unroll
    for (int e = 0; e < 13; ++e) { asm volatile("" : "+v"(fe[e])); bad |= fe[e] ^ tag; }
    if (__builtin_amdgcn_ballot_w64(bad != 0) != 0) {
#pragma unroll 1
      for (int e = 0; e < 13; ++e) { const unsigned fo = (e < 12 ? C_FH + (e * 64 + lane) * FS : C_FKV + lane * FS); unsigned f; ld32_sc1(f, fo, rsC); waitv<0>(); poll_finish(f, fo, rsC, tag); }
    }
    if (lane == 0) st32_sc1(tag, C_EPOCH, rsC);
  }
  STAMP(11);
  STAMP_FLUSH(); FT_FLUSH();
}

__global__ __launch_bounds__(256, 1) void fused(Params p) {
  v4u pf;
  {
    unsigned long long pc; unsigned len;
    asm volatile("s_getpc_b64 %0\nLpf_here:\n\ts_mov_b32 %1, Lpf_end-Lpf_here" : "=s"(pc), "=s"(len));
    const rsrc_t rs = mkrs((const void*)pc, len);
    asm volatile("buffer_load_dwordx4 %0, %1, %2, 0 offen" : "=v"(pf) : "v"(((blockIdx.x >> 3) * 256 + threadIdx.x) * 16), "s"(rs));
    SCHED();
  }
  extern __shared__ __attribute__((aligned(16))) char smem[];
  LAS char* lds = (LAS char*)smem;
  const int lane = threadIdx.x & 63, wave = __builtin_amdgcn_readfirstlane(threadIdx.x >> 6);
  const int x = blockIdx.x & 7, r = blockIdx.x >> 3;
  if (__builtin_expect(r < 24, 1)) body<true>(p, lds, x, r, wave, lane);
  else body<false>(p, lds, x, r, wave, lane);
  asm volatile("s_waitcnt vmcnt(0)\nLpf_end:" : "+v"(pf));
}

extern "C" void kernel_launch(void* const* d_in, const int* in_sizes, int n_in, void* d_out, int out_size, void* d_ws, size_t ws_size, hipStream_t stream) {
  Params p;
  p.q = (const u16*)d_in[0]; p.k = (const u16*)d_in[1]; p.v = (const u16*)d_in[2]; p.sk = (const u16*)d_in[3];
  p.h = (const u16*)d_in[4]; p.Dv = (const u16*)d_in[5]; p.w1 = (const u16*)d_in[7]; p.w2 = (const u16*)d_in[8]; p.gw = (const u16*)d_in[9];
  p.out = (u16*)d_out; p.ws = (char*)d_ws; p.bx = (const u16*)d_in[6];
  (void)hipFuncSetAttribute((const void*)fused, hipFuncAttributeMaxDynamicSharedMemorySize, LDS_BYTES);
  fused<<<256, 256, LDS_BYTES, stream>>>(p);
}
```

```cpp
#include <hip/hip_runtime.h>
#include <type_traits>
#define SPIN_SLEEP 4
#define FSTR 64
#include <stdint.h>

typedef unsigned short u16;
typedef unsigned v4u __attribute__((ext_vector_type(4)));
typedef unsigned v2u __attribute__((ext_vector_type(2)));
typedef float f32x4 __attribute__((ext_vector_type(4)));
typedef float f32x2 __attribute__((ext_vector_type(2)));
typedef __bf16 bf16x8 __attribute__((ext_vector_type(8)));
typedef __bf16 bf16x2 __attribute__((ext_vector_type(2)));
typedef short v4s __attribute__((ext_vector_type(4)));
typedef short v8s __attribute__((ext_vector_type(8)));
#define GAS __attribute__((address_space(1)))
#define LAS __attribute__((address_space(3)))
#define DEV __device__ __forceinline__
typedef __amdgpu_buffer_rsrc_t rsrc_t;

constexpr int S = 2048, D = 640, F = 1536;

template <int N> DEV void waitv() { asm volatile("s_waitcnt vmcnt(%0)" ::"i"(N) : "memory"); }
template <int N> DEV void waitl() { asm volatile("s_waitcnt lgkmcnt(%0)" ::"i"(N) : "memory"); }
DEV void sbar() { asm volatile("s_barrier" ::: "memory"); }
#define SCHED() __builtin_amdgcn_sched_barrier(0)
DEV rsrc_t mkrs(const void* p, unsigned bytes) { return __builtin_amdgcn_make_buffer_rsrc((void*)p, 0, bytes, 0x00020000); }
DEV f32x4 mfma(v4u a, v4u b, f32x4 c) {
  return __builtin_amdgcn_mfma_f32_16x16x32_bf16(__builtin_bit_cast(bf16x8, a), __builtin_bit_cast(bf16x8, b), c, 0, 0, 0);
}
DEV v4u lds128(LAS const char* p) { return *(LAS const v4u*)p; }
DEV v2u lds64(LAS const char* p) { return *(LAS const v2u*)p; }
DEV v2u ldstr(LAS const char* p) { return __builtin_bit_cast(v2u, __builtin_amdgcn_ds_read_tr16_b64_v4i16((LAS v4s*)p)); }
DEV v4u cat(v2u a, v2u b) { return v4u{a.x, a.y, b.x, b.y}; }
DEV unsigned pk2(float a, float b) { bf16x2 r = __builtin_convertvector(f32x2{a, b}, bf16x2); return __builtin_bit_cast(unsigned, r); }
DEV float blo(unsigned d) { return __builtin_bit_cast(float, d << 16); }
DEV float bhi(unsigned d) { return __builtin_bit_cast(float, d & 0xffff0000u); }
DEV void setm0(unsigned v) { asm volatile("s_mov_b32 m0, %0\n\ts_nop 0" ::"s"(v)); }
template <int OFF, int NT = 0> DEV void dma16(unsigned voff, rsrc_t rs, unsigned soff) {
  if constexpr (NT) asm volatile("buffer_load_dwordx4 %0, %1, %2 offen offset:%3 nt lds" ::"v"(voff), "s"(rs), "s"(soff), "i"(OFF));
  else asm volatile("buffer_load_dwordx4 %0, %1, %2 offen offset:%3 lds" ::"v"(voff), "s"(rs), "s"(soff), "i"(OFF));
}
typedef float f32x16 __attribute__((ext_vector_type(16)));
DEV f32x16 mfma32(v4u a, v4u b, f32x16 c) {
  return __builtin_amdgcn_mfma_f32_32x32x16_bf16(__builtin_bit_cast(bf16x8, a), __builtin_bit_cast(bf16x8, b), c, 0, 0, 0);
}
template <int IMM> DEV void setm0_add(unsigned base) { asm volatile("s_add_u32 m0, %0, %1" ::"s"(base), "i"(IMM) : "scc"); }
template <int I, int N, class Fn> DEV void static_for(Fn&& f) { if constexpr (I < N) { f(std::integral_constant<int, I>{}); static_for<I + 1, N>(f); } }
DEV void mfma_ip(f32x4& c, const v4u& a, const v4u& b) { asm volatile("v_mfma_f32_16x16x32_bf16 %0, %1, %2, %0" : "+v"(c) : "v"(a), "v"(b)); }
DEV void mfma_ipA(f32x4& c, const v4u& a, const v4u& b) { asm volatile("v_mfma_f32_16x16x32_bf16 %0, %1, %2, %0" : "+v"(c) : "a"(a), "v"(b)); }
DEV void mfma_z(f32x4& c, const v4u& a, const v4u& b) { asm volatile("v_mfma_f32_16x16x32_bf16 %0, %1, %2, 0" : "=&v"(c) : "v"(a), "v"(b)); }
DEV void mfma_zA(f32x4& c, const v4u& a, const v4u& b) { asm volatile("v_mfma_f32_16x16x32_bf16 %0, %1, %2, 0" : "=&v"(c) : "a"(a), "v"(b)); }
template <int AUX> DEV void ldA(v4u& d, unsigned voff, rsrc_t rs, unsigned soff) {
  if constexpr (AUX) asm volatile("buffer_load_dwordx4 %0, %1, %2, %3 offen sc1" : "=a"(d) : "v"(voff), "s"(rs), "s"(soff));
  else asm volatile("buffer_load_dwordx4 %0, %1, %2, %3 offen" : "=a"(d) : "v"(voff), "s"(rs), "s"(soff));
}
template <int AUX> DEV void ldV(v4u& d, unsigned voff, rsrc_t rs, unsigned soff) {
  if constexpr (AUX == 2) asm volatile("buffer_load_dwordx4 %0, %1, %2, %3 offen nt" : "=v"(d) : "v"(voff), "s"(rs), "s"(soff));
  else if constexpr (AUX == 16) asm volatile("buffer_load_dwordx4 %0, %1, %2, %3 offen sc1" : "=v"(d) : "v"(voff), "s"(rs), "s"(soff));
  else asm volatile("buffer_load_dwordx4 %0, %1, %2, %3 offen" : "=v"(d) : "v"(voff), "s"(rs), "s"(soff));
}
DEV void nop8() { asm volatile("s_nop 7"); }

constexpr size_t WS_CTRL = 0;
constexpr size_t WS_KVT = 262144;
constexpr size_t WS_YT = WS_KVT + (size_t)D * S * 2;
constexpr size_t WS_W2T = WS_YT + (size_t)D * S * 2;
constexpr size_t WS_HF = WS_W2T + (size_t)8 * 48 * 5 * 1024;
constexpr size_t WS_G = WS_HF + (size_t)128 * 48 * 1024;
constexpr size_t WS_END = WS_G + S * 4;
constexpr unsigned C_EPOCH = 0, C_TMO = 256;
constexpr unsigned FS = FSTR;
constexpr unsigned C_FW = 4096;
constexpr unsigned C_FKV = C_FW + 64 * FS;
constexpr unsigned C_FH = C_FKV + 64 * FS;
constexpr unsigned C_FY = C_FH + 768 * FS;
constexpr unsigned C_BYTES = 262144;
static_assert(C_FY + 1024 * FS <= C_BYTES, "");

#define NSTAMP 16
#define STAMP_OFF (size_t)(32u << 20)
#define STAMP_DECL
#define STAMP(i)
#define STAMP_FLUSH()
#define FT_DECL
#define FT(i)
#define FT_FLUSH()

struct Params {
  const u16 *q, *k, *v, *sk, *h, *Dv, *w1, *w2, *gw;
  u16* out;
  char* ws;
};

constexpr int LDS_BYTES = 160 * 1024;
constexpr int SC1 = 16;
#define NT_X 0
#define NT_W1 0
#define NT_P 0
constexpr int AUXP = NT_P ? 2 : 0;
#define IC(n) std::integral_constant<int, (n)>{}

DEV void ld32_sc1(unsigned& v, unsigned voff, rsrc_t rs) { asm volatile("buffer_load_dword %0, %1, %2, 0 offen sc1" : "=v"(v) : "v"(voff), "s"(rs)); }
DEV void st32_sc1(unsigned v, unsigned voff, rsrc_t rs) { asm volatile("buffer_store_dword %0, %1, %2, 0 offen sc1" ::"v"(v), "v"(voff), "s"(rs)); }
DEV void poll_finish(unsigned v, unsigned voff, rsrc_t rsC, unsigned tag) {
  asm volatile("" : "+v"(v));
  if (__builtin_amdgcn_ballot_w64(v != tag) == 0) return;
#pragma unroll 1
  for (int spins = 0; spins < (1 << 15); ++spins) {
    __builtin_amdgcn_s_sleep(SPIN_SLEEP);
    ld32_sc1(v, voff, rsC);
    asm volatile("s_waitcnt vmcnt(0)" : "+v"(v)::"memory");
    if (__builtin_amdgcn_ballot_w64(v != tag) == 0) return;
  }
  st32_sc1(1u, C_TMO, rsC);
}

DEV void poll_finish2(unsigned a, unsigned oa, unsigned b, unsigned ob, rsrc_t rsC, unsigned tag) {
  asm volatile("" : "+v"(a), "+v"(b));
  if (__builtin_amdgcn_ballot_w64(a != tag || b != tag) == 0) return;
#pragma unroll 1
  for (int spins = 0; spins < (1 << 15); ++spins) {
    __builtin_amdgcn_s_sleep(SPIN_SLEEP);
    ld32_sc1(a, oa, rsC); ld32_sc1(b, ob, rsC);
    asm volatile("s_waitcnt vmcnt(0)" : "+v"(a), "+v"(b)::"memory");
    if (__builtin_amdgcn_ballot_w64(a != tag || b != tag) == 0) return;
  }
  st32_sc1(1u, C_TMO, rsC);
}

DEV void mfma32_ip(f32x16& c, const v4u& a, const v4u& b) { asm volatile("v_mfma_f32_32x32x16_bf16 %0, %1, %2, %0" : "+v"(c) : "v"(a), "v"(b)); }
DEV void mfma32_z(f32x16& c, const v4u& a, const v4u& b) { asm volatile("v_mfma_f32_32x32x16_bf16 %0, %1, %2, 0" : "=&v"(c) : "v"(a), "v"(b)); }

constexpr int KV_BASE = 32768, KV_GS = 1152, KV_V = 9 * KV_GS, KV_CG = 17 * KV_GS;
constexpr int CV_BASE = 32768;
constexpr int CV_HC = 4160;
constexpr int CV_U = 4 * CV_HC;
constexpr int CV_CH = CV_U + 4608;
constexpr int CV_EX = CV_BASE + 4 * CV_CH;
constexpr int G2_PART = 0;
constexpr int G2_Y = 81920;
constexpr int HW = 24;

template <int AUX> DEV void ldV2(v2u& d, unsigned voff, rsrc_t rs, unsigned soff) {
  if constexpr (AUX == 2) asm volatile("buffer_load_dwordx2 %0, %1, %2, %3 offen nt" : "=v"(d) : "v"(voff), "s"(rs), "s"(soff));
  else if constexpr (AUX == 16) asm volatile("buffer_load_dwordx2 %0, %1, %2, %3 offen sc1" : "=v"(d) : "v"(voff), "s"(rs), "s"(soff));
  else asm volatile("buffer_load_dwordx2 %0, %1, %2, %3 offen" : "=v"(d) : "v"(voff), "s"(rs), "s"(soff));
}
DEV void dma16s(unsigned voff, rsrc_t rs, unsigned soff, unsigned m0v) {
  if constexpr (NT_P) asm volatile("s_mov_b32 m0, %3\n\ts_nop 0\n\tbuffer_load_dwordx4 %0, %1, %2 offen nt lds" ::"v"(voff), "s"(rs), "s"(soff), "s"(m0v));
  else asm volatile("s_mov_b32 m0, %3\n\ts_nop 0\n\tbuffer_load_dwordx4 %0, %1, %2 offen lds" ::"v"(voff), "s"(rs), "s"(soff), "s"(m0v));
}
DEV float dot2(unsigned a, unsigned b, float c) { return __builtin_amdgcn_fdot2_f32_bf16(__builtin_bit_cast(bf16x2, a), __builtin_bit_cast(bf16x2, b), c, false); }
DEV unsigned mktag(unsigned& epoch) {
  asm volatile("" : "+v"(epoch));
  return __builtin_amdgcn_readfirstlane(epoch * 0x9E3779B1u + 0x7F4A7C15u);
}

template <int NR, class ST> DEV void kv_math(const v4u (&kk)[NR + 3], const v4u (&vv)[NR], const v4u (&ww)[4], ST&& st) {
#pragma unroll
  for (int cp = 0; cp < 4; ++cp) {
    f32x2 w[4];
    w[0] = f32x2{blo(ww[cp].x), blo(ww[cp].z)}; w[1] = f32x2{bhi(ww[cp].x), bhi(ww[cp].z)};
    w[2] = f32x2{blo(ww[cp].y), blo(ww[cp].w)}; w[3] = f32x2{bhi(ww[cp].y), bhi(ww[cp].w)};
    f32x2 kf[NR + 3];
#pragma unroll
    for (int i = 0; i < NR + 3; ++i) kf[i] = f32x2{blo(kk[i][cp]), bhi(kk[i][cp])};
    f32x2 r[NR];
#pragma unroll
    for (int i = 0; i < NR; ++i) {
      f32x2 a = kf[i + 3] * w[0];
      a = __builtin_elementwise_fma(kf[i + 2], w[1], a);
      a = __builtin_elementwise_fma(kf[i + 1], w[2], a);
      a = __builtin_elementwise_fma(kf[i], w[3], a);
      r[i] = a * f32x2{blo(vv[i][cp]), bhi(vv[i][cp])};
    }
    st(cp, r);
  }
}

#define Q0 7
#define ROT 0
#define PREP_FIRST 0
#define HPUB 90
#define WPRE_ 16
constexpr int WPRE = WPRE_;
constexpr int HW0 = 16, HWM = 24;
constexpr int PW_BASE = 131072, PW_SZ = 3072;
constexpr int g2_issued(int i) { int r = HW0; for (int s = 0; s < i; ++s) r += (r - s < HWM) ? ((r + 1 - s < HWM) ? 2 : 1) : 0; return r < 48 ? r : 48; }

template <int OFF, int AUX> DEV void ldVo(v4u& d, unsigned voff, rsrc_t rs, unsigned soff) {
  static_assert(AUX == 16, "");
  asm volatile("buffer_load_dwordx4 %0, %1, %2, %3 offen offset:%4 sc1" : "=v"(d) : "v"(voff), "s"(rs), "s"(soff), "i"(OFF));
}
template <int OFF> DEV void ldAo(v4u& d, unsigned voff, rsrc_t rs, unsigned soff) {
  asm volatile("buffer_load_dwordx4 %0, %1, %2, %3 offen offset:%4 sc1" : "=a"(d) : "v"(voff), "s"(rs), "s"(soff), "i"(OFF));
}

template <bool G1R>
DEV void body(const Params& p, LAS char* lds, const int x, const int r, const int wave, const int lane) {
  const int g = lane >> 4, q4 = (lane >> 2) & 3, p4 = lane & 3, c16 = lane & 15;
  STAMP_DECL; FT_DECL;
  STAMP(0); FT(13);
  const rsrc_t rsC = mkrs(p.ws + WS_CTRL, C_BYTES);
  unsigned epoch, tag;
  ld32_sc1(epoch, C_EPOCH, rsC);
  const int cl = G1R ? (wave >> 1) : wave, half = G1R ? (wave & 1) : 0;
  const int ch = (G1R ? 80 * x + 2 * r : 80 * x + 48 + 4 * (r - 24)) + cl;
  const unsigned cb = CV_BASE + cl * CV_CH;
  constexpr int NCHK = G1R ? 2 : 4;
  unsigned dv;
  { const rsrc_t rsD = mkrs(p.Dv, D * 2); asm volatile("buffer_load_ushort %0, %1, %2, 0 offen" : "=v"(dv) : "v"(ch * 2), "s"(rsD)); }
  const rsrc_t rsHh = mkrs(p.h, D * S * 2), rsU = mkrs(p.ws + WS_KVT, D * S * 2), rsY = mkrs(p.ws + WS_YT, D * S * 2);
  const rsrc_t rsW2 = mkrs(p.ws + WS_W2T, 8 * 48 * 5 * 1024), rsH = mkrs(p.ws + WS_HF, 128 * 48 * 1024);
#define MAP42 1
  const int rb = MAP42 ? 8 * (x >> 1) + (r & 7) : 4 * x + (r & 3), nt = MAP42 ? 4 * (x & 1) + (r >> 3) : r >> 2;
  const unsigned vl = lane * 16;
  const unsigned sW = (nt * 48 + 12 * wave) * 5 * 1024;
  const unsigned foW = C_FW + lane * FS, foKV = C_FKV + lane * FS;
  v4u W[12][5];
  auto ldW = [&](auto I) { constexpr int i = I.value; if constexpr (i >= 0 && i < 60) ldAo<(i & 3) * 1024>(W[i / 5][i % 5], vl, rsW2, sW + (i & ~3) * 1024); };
  v4u hc[NCHK], uc[NCHK]; v2u hp0;
  auto ld_h = [&]() {
    ldV2<AUXP>(hp0, half > 0 ? (unsigned)(ch * 4096 + half * NCHK * 1024 - 8) : 0x80000000u, rsHh, 0);
#pragma unroll
    for (int i = 0; i < NCHK; ++i) ldV<AUXP>(hc[i], ch * 4096 + ((half * NCHK + i) * 64 + lane) * 16, rsHh, 0);
  };
  auto ld_u = [&]() {
#pragma unroll
    for (int i = 0; i < NCHK; ++i) ldV<SC1>(uc[i], ch * 4096 + ((half * NCHK + i) * 64 + lane) * 16, rsU, 0);
  };
  auto fill_h = [&]() {
    asm volatile("" : "+v"(hp0));
#pragma unroll
    for (int i = 0; i < NCHK; ++i) asm volatile("" : "+v"(hc[i]));
    unsigned e0 = hp0.x, e1 = hp0.y;
#pragma unroll
    for (int i = 0; i < NCHK; ++i) {
      const int n = (half * NCHK + i) * 64 + lane;
      const v4u cur = hc[i];
      const v2u prv = v2u{(unsigned)__builtin_amdgcn_update_dpp((int)e0, (int)cur.z, 0x138, 0xf, 0xf, false), (unsigned)__builtin_amdgcn_update_dpp((int)e1, (int)cur.w, 0x138, 0xf, 0xf, false)};
      e0 = __builtin_amdgcn_readlane(cur.z, 63); e1 = __builtin_amdgcn_readlane(cur.w, 63);
      const unsigned om2 = __builtin_amdgcn_alignbit(prv.y, prv.x, 16), om1 = __builtin_amdgcn_alignbit(cur.x, prv.y, 16);
      const unsigned o0 = __builtin_amdgcn_alignbit(cur.y, cur.x, 16), o1 = __builtin_amdgcn_alignbit(cur.z, cur.y, 16), o2 = __builtin_amdgcn_alignbit(cur.w, cur.z, 16);
      LAS char* dst = lds + cb + (n + 2) * 16;
      *(LAS v4u*)(dst) = cur;
      *(LAS v4u*)(dst + CV_HC) = v4u{om1, o0, o1, o2};
      *(LAS v4u*)(dst + 2 * CV_HC) = v4u{prv.y, cur.x, cur.y, cur.z};
      *(LAS v4u*)(dst + 3 * CV_HC) = v4u{om2, om1, o0, o1};
    }
    if (half == 0) {
      if (lane < 8) *(LAS v4u*)(lds + cb + (lane >> 1) * CV_HC + (lane & 1) * 16) = v4u{0, 0, 0, 0};
      if (lane >= 32 && lane < 62) *(LAS v4u*)(lds + cb + CV_U + (lane - 32) * 16) = v4u{0, 0, 0, 0};
    }
  };
  auto fill_u = [&]() {
#pragma unroll
    for (int i = 0; i < NCHK; ++i) {
      asm volatile("" : "+v"(uc[i]));
      *(LAS v4u*)(lds + cb + CV_U + 480 + ((half * NCHK + i) * 64 + lane) * 16) = uc[i];
    }
  };
  unsigned fw;

  if constexpr (G1R) {
    const int sb = 4 * (x >> 1) + (r & 3), fs = 6 * (x & 1) + (r >> 2);
    f32x16 acc1[2][2];
    const rsrc_t rsX = mkrs(p.q, S * D * 2), rsW = mkrs(p.w1, D * F * 2);
    unsigned vX[4], vW[4];
#pragma unroll
    for (int ii = 0; ii < 4; ++ii) {
      vX[ii] = (32 * wave + 8 * ii + (lane >> 3)) * 1280 + (((lane & 7) ^ (4 * (ii & 1) + g)) << 4) - 1024 * ii;
      vW[ii] = (16 * wave + 4 * ii + g) * 3072 + (((((c16 >> 1) ^ (g << 1)) << 1) | (c16 & 1)) << 4) - 1024 * ii;
    }
    const unsigned sX0 = sb * 128 * 1280, sW0 = fs * 256;
    const unsigned mX = wave * 4096, mW = 16384 + wave * 4096;
    static_for<0, 7>([&](auto Q) { constexpr int q = Q.value, t = q >> 1;
      setm0_add<t * 32768>((q & 1) ? mW : mX); asm volatile("s_nop 0");
      static_for<0, 4>([&](auto I) { constexpr int i = I.value;
        if constexpr (q & 1) dma16<1024 * i, NT_W1>(vW[i], rsW, sW0 + t * (64 * 3072)); else dma16<1024 * i, NT_X>(vX[i], rsX, sX0 + t * 128); });
    });
    FT(11);
    const int ws_ = wave & 1, wf = wave >> 1, h = lane >> 5, r32 = lane & 31;
    unsigned aX[4], aW[2];
#pragma unroll
    for (int ks = 0; ks < 4; ++ks) aX[ks] = (64 * ws_ + r32) * 128 + (((2 * ks + h) ^ ((r32 >> 1) & 7)) << 4);
#pragma unroll
    for (int ft = 0; ft < 2; ++ft) aW[ft] = 16384 + (8 * h + q4) * 256 + (((2 * (2 * wf + ft) + (g & 1)) ^ (q4 << 1)) << 5) + 8 * p4;
#pragma unroll
    for (int a = 0; a < 2; ++a)
#pragma unroll
      for (int b = 0; b < 2; ++b)
#pragma unroll
        for (int e = 0; e < 16; ++e) acc1[a][b][e] = 0.f;
    asm volatile("s_nop 1" : "+v"(acc1[0][0]), "+v"(acc1[0][1]), "+v"(acc1[1][0]), "+v"(acc1[1][1]));
    v4u A[2][2][2], B[2][2][2];
    v2u At;
    unsigned cX[2], cW[2];
    auto rd = [&](auto HF, auto SET, auto N) {
      constexpr int hf = HF.value, set = SET.value, n = N.value;
      constexpr int kk = n / 6, rr = n % 6, ks = 2 * hf + kk;
      if constexpr (rr == 0) B[set][kk][0] = lds128(lds + cX[kk]);
      else if constexpr (rr == 3) B[set][kk][1] = lds128(lds + cX[kk] + 32 * 128);
      else { constexpr int ft = rr / 3, jj = (rr % 3) - 1;
        v2u xx = ldstr(lds + cW[ft] + ks * 4096 + jj * 1024);
        if constexpr (jj == 0) At = xx; else A[set][kk][ft] = cat(At, xx); }
    };
    auto set_addr = [&](int hf, unsigned bo) { cX[0] = aX[2 * hf] + bo; cX[1] = aX[2 * hf + 1] + bo; cW[0] = aW[0] + bo; cW[1] = aW[1] + bo; };
    unsigned fk;
    waitv<20>(); sbar();
    STAMP(1);
    set_addr(0, 0);
    static_for<0, 12>([&](auto N) { rd(IC(0), IC(0), N); });
    SCHED();
    unsigned bo = 0;
    unsigned bW = 3 * 32768, bX = 4 * 32768;
    unsigned sWn = sW0 + 3 * (64 * 3072), sXn = sX0 + 4 * 128;
    auto stage = [&](auto DMA, int t) {
      constexpr bool dma = DMA.value;
      const unsigned nbo = bo == 4 * 32768 ? 0 : bo + 32768;
      set_addr(1, bo);
      static_for<0, 8>([&](auto M) {
        constexpr int m = M.value, kk = m >> 2, ft = (m >> 1) & 1, st = m & 1;
        mfma32_ip(acc1[ft][st], A[0][kk][ft], B[0][kk][st]);
        if constexpr (m < 6) { rd(IC(1), IC(1), IC(2 * m)); rd(IC(1), IC(1), IC(2 * m + 1)); }
        if constexpr (dma && m == 0) asm volatile("s_add_u32 m0, %0, %1" ::"s"(mW), "s"(bW) : "scc");
        if constexpr (dma && (m & 1)) dma16<1024 * (m >> 1), NT_W1>(vW[m >> 1], rsW, sWn);
        SCHED();
      });
      if constexpr (dma) { waitv<16>(); sbar(); }
      else { if (t == 7) waitv<8>(); else waitv<0>(); sbar(); }
      SCHED();
      set_addr(0, nbo);
      const unsigned sx = (dma && t < 6) ? sXn : 0x80000000u;
      static_for<0, 8>([&](auto M) {
        constexpr int m = M.value, kk = m >> 2, ft = (m >> 1) & 1, st = m & 1;
        mfma32_ip(acc1[ft][st], A[1][kk][ft], B[1][kk][st]);
        if constexpr (m < 6) { rd(IC(0), IC(0), IC(2 * m)); rd(IC(0), IC(0), IC(2 * m + 1)); }
        if constexpr (dma && m == 0) asm volatile("s_add_u32 m0, %0, %1" ::"s"(mX), "s"(bX) : "scc");
        if constexpr (dma && (m & 1)) dma16<1024 * (m >> 1), NT_X>(vX[m >> 1], rsX, sx);
        SCHED();
      });
      bo = nbo;
      bW = bW == 4 * 32768 ? 0 : bW + 32768; bX = bX == 4 * 32768 ? 0 : bX + 32768;
      sWn += 64 * 3072; sXn += 128;
    };
#pragma unroll 1
    for (int t = 0; t < 7; ++t) stage(std::true_type{}, t);
#pragma unroll 1
    for (int t = 7; t < 10; ++t) stage(std::false_type{}, t);
    asm volatile("s_nop 15" : "+v"(acc1[0][0]), "+v"(acc1[0][1]), "+v"(acc1[1][0]), "+v"(acc1[1][1]));
    sbar();
    STAMP(2);
    tag = mktag(epoch);
    ld_h();
    const unsigned vo = (g & 1) * (48 * 1024) + (c16 + 16 * h) * 16;
    static_for<0, 4>([&](auto Q) {
      constexpr int qd = Q.value, st = qd >> 1, ft = qd & 1;
      const f32x16 a = acc1[ft][st];
      unsigned o[8];
      static_for<0, 2>([&](auto G) {
        constexpr int gq = G.value;
        static_for<0, 4>([&](auto E) {
          constexpr int e = 8 * gq + 2 * E.value;
          const float r0 = a[e] * __builtin_amdgcn_rcpf(1.f + __builtin_amdgcn_exp2f(a[e] * -1.44269504088896f));
          const float r1 = a[e + 1] * __builtin_amdgcn_rcpf(1.f + __builtin_amdgcn_exp2f(a[e + 1] * -1.44269504088896f));
          o[e >> 1] = pk2(r0, r1);
        });
        SCHED();
      });
      const unsigned so = ((8 * sb + 4 * ws_ + 2 * st) * 48 + 4 * fs + 2 * wf + ft) * 1024;
      __builtin_amdgcn_raw_buffer_store_b128(v4u{o[0], o[1], o[4], o[5]}, rsH, vo, so, SC1);
      __builtin_amdgcn_raw_buffer_store_b128(v4u{o[2], o[3], o[6], o[7]}, rsH, vo + 512, so, SC1);
      SCHED();
      FT(32 + qd); SCHED();
    });
    ld32_sc1(fk, foKV, rsC); ld32_sc1(fw, foW, rsC);
    waitv<10>();
    fill_h();
    FT(54);
    waitv<0>();
    if (lane == 0) st32_sc1(tag, C_FH + ((sb * 2 + ws_) * 24 + fs * 2 + wf) * FS, rsC);
    FT(55);
    poll_finish(fk, foKV, rsC, tag);
    ld_u();
    FT(36);
    poll_finish(fw, foW, rsC, tag);
    FT(37);
    static_for<0, WPRE>([&](auto I) { ldW(I); });
    STAMP(3);
    waitv<WPRE>();
    fill_u();
    waitl<0>(); sbar();
    STAMP(4);
  } else {
    const int j = (r - 24) * 8 + x, rbk = j >> 1, cg0 = 5 * (j & 1);
    const rsrc_t rsK = mkrs(p.k, S * D * 2), rsV = mkrs(p.v, S * D * 2), rsS = mkrs(p.sk, D * 8), rsO = mkrs(p.ws + WS_KVT, D * S * 2);
    const unsigned vr = (lane >> 3) * 1280 + ((lane & 7) << 4), vr4 = vr ^ 64;
    auto dmaK = [&](int cgl, int G) {
      const int row = 64 * rbk - 8 + 8 * G;
      dma16s(((G >> 1) & 1) ? vr4 : vr, rsK, row >= 0 ? (unsigned)(row * 1280 + (cg0 + cgl) * 128) : 0x80000000u, KV_BASE + cgl * KV_CG + G * KV_GS);
    };
    auto dmaV = [&](int cgl, int G) {
      dma16s(((G >> 1) & 1) ? vr4 : vr, rsV, (64 * rbk + 8 * G) * 1280 + (cg0 + cgl) * 128, KV_BASE + cgl * KV_CG + KV_V + G * KV_GS);
    };
#pragma unroll
    for (int G = 0; G < 9; ++G) dmaK(wave, G);
#pragma unroll
    for (int G = 0; G < 8; ++G) dmaV(wave, G);
    const unsigned pw = PW_BASE + wave * PW_SZ;
    dma16s(lane < 32 ? (unsigned)((cg0 + wave) * 512 + lane * 16) : (lane < 40 ? (unsigned)((64 * (cg0 + 4) + 16 * wave) * 8 + (lane - 32) * 16) : 0x80000000u), rsS, 0, pw);
    dmaK(4, wave); dmaK(4, wave + 4); dmaV(4, wave); dmaV(4, wave + 4);
    if (wave == 0) dmaK(4, 8);
    const int cc = lane >> 3, rg = lane & 7;
    FT(10);
    waitv<4>();
    STAMP(1);
    {
      const unsigned kb = KV_BASE + wave * KV_CG;
      const unsigned a0 = kb + rg * KV_GS + ((cc ^ (4 * ((rg >> 1) & 1))) << 4), a1 = kb + (rg + 1) * KV_GS + ((cc ^ (4 * (((rg + 1) >> 1) & 1))) << 4);
      v4u kk[11], vv[8], ww[4];
#pragma unroll
      for (int i = 0; i < 4; ++i) ww[i] = lds128(lds + pw + cc * 64 + i * 16);
#pragma unroll
      for (int i = 0; i < 11; ++i) kk[i] = i < 3 ? lds128(lds + a0 + (5 + i) * 128) : lds128(lds + a1 + (i - 3) * 128);
#pragma unroll
      for (int i = 0; i < 8; ++i) vv[i] = lds128(lds + a0 + KV_V + i * 128);
      const unsigned ob = (64 * (cg0 + wave) + 8 * cc) * 4096 + (64 * rbk + 8 * rg) * 2;
      kv_math<8>(kk, vv, ww, [&](int cp, const f32x2 (&rr)[8]) {
#pragma unroll
        for (int c = 0; c < 2; ++c) {
          v4u o{pk2(rr[0][c], rr[1][c]), pk2(rr[2][c], rr[3][c]), pk2(rr[4][c], rr[5][c]), pk2(rr[6][c], rr[7][c])};
          __builtin_amdgcn_raw_buffer_store_b128(o, rsO, ob + (2 * cp + c) * 4096, 0, SC1);
        }
      });
    }
    const int id = 4 * j + wave, pk = id / 5, blk = id % 5;
    const rsrc_t rsWs = mkrs(p.w2, F * D * 2);
    const unsigned ldsb = wave * 8192;
#pragma unroll
    for (int i = 0; i < 8; ++i) {
      const int row = 4 * i + g;
      dma16s((32 * pk + row) * 1280 + blk * 256 + (((((c16 >> 1) ^ (row & 7)) << 1) | (c16 & 1)) << 4), rsWs, id < 240 ? 0u : 0x80000000u, ldsb + i * 1024);
    }
    waitv<16>(); sbar();
    STAMP(2);
    {
      const int cc4 = 2 * wave + (lane >> 5), rp = lane & 31;
      const unsigned kb = KV_BASE + 4 * KV_CG;
      v4u kk[5], vv[2], w4[4];
#pragma unroll
      for (int i = 0; i < 4; ++i) w4[i] = lds128(lds + pw + 512 + (lane >> 5) * 64 + i * 16);
#pragma unroll
      for (int i = 0; i < 5; ++i) { const int row = 2 * rp + 5 + i; kk[i] = lds128(lds + kb + (row >> 3) * KV_GS + (row & 7) * 128 + ((cc4 ^ (4 * ((row >> 4) & 1))) << 4)); }
#pragma unroll
      for (int i = 0; i < 2; ++i) { const int row = 2 * rp + i; vv[i] = lds128(lds + kb + KV_V + (row >> 3) * KV_GS + (row & 7) * 128 + ((cc4 ^ (4 * ((row >> 4) & 1))) << 4)); }
      const unsigned ob = (64 * (cg0 + 4) + 8 * cc4) * 4096 + (64 * rbk + 2 * rp) * 2;
      kv_math<2>(kk, vv, w4, [&](int cp, const f32x2 (&rr)[2]) {
#pragma unroll
        for (int c = 0; c < 2; ++c) __builtin_amdgcn_raw_buffer_store_b32(pk2(rr[0][c], rr[1][c]), rsO, ob + (2 * cp + c) * 4096, 0, SC1);
      });
    }
    FT(11);
    waitv<8>();
    {
      const rsrc_t rsWo = mkrs(p.ws + WS_W2T, 8 * 48 * 5 * 1024);
      const int r0 = 4 * g + q4;
#pragma unroll
      for (int i = 0; i < 8; ++i) {
        const unsigned a = ldsb + r0 * 256 + ((i ^ (r0 & 7)) << 5) + 8 * p4;
        const v4u fr = cat(ldstr(lds + a), ldstr(lds + a + 4096));
        const int ctg = 8 * blk + i, ntt = ctg / 5, ct = ctg % 5;
        __builtin_amdgcn_raw_buffer_store_b128(fr, rsWo, vl, id < 240 ? (unsigned)(((ntt * 48 + pk) * 5 + ct) * 1024) : 0x80000000u, SC1);
      }
    }
    FT(0);
    waitv<8>();
    waitl<0>(); sbar();
    tag = mktag(epoch);
    if (threadIdx.x == 0) st32_sc1(tag, C_FKV + j * FS, rsC);
    STAMP(3);
    ld_h();
    waitv<5>(); sbar();
    if (threadIdx.x == 0) st32_sc1(tag, C_FW + j * FS, rsC);
    unsigned fk;
    ld32_sc1(fk, foKV, rsC);
    FT(3);
    waitv<1>();
    waitv<0>();
    fill_h();
    FT(2);
    poll_finish(fk, foKV, rsC, tag);
    FT(4);
    ld_u();
    const rsrc_t rsX = mkrs(p.q, S * D * 2), rsG = mkrs(p.gw, D * 2), rsGo = mkrs(p.ws + WS_G, S * 4);
    const int grow = 256 * x + 32 * (r - 24) + 8 * wave + (lane >> 3), part = lane & 7;
    v4u xx[10];
    dma16s(lane * 16, rsG, 0, pw + 1024); dma16s(1024 + lane * 16, rsG, 0, pw + 2048);
#pragma unroll
    for (int i = 0; i < 10; ++i) ldV<0>(xx[i], grow * 1280 + i * 128 + part * 16, rsX, 0);
    waitv<12>();
    fill_u();
    ld32_sc1(fw, foW, rsC);
    waitv<1>();
    {
      float a0 = 0.f, a1 = 0.f;
#pragma unroll
      for (int i = 0; i < 10; ++i) {
        asm volatile("" : "+v"(xx[i]));
        const v4u gg = lds128(lds + pw + 1024 + (i * 8 + part) * 16);
        a0 = dot2(xx[i].x, gg.x, a0); a1 = dot2(xx[i].y, gg.y, a1); a0 = dot2(xx[i].z, gg.z, a0); a1 = dot2(xx[i].w, gg.w, a1);
      }
      float a = a0 + a1;
      a += __shfl_xor(a, 1); a += __shfl_xor(a, 2); a += __shfl_xor(a, 4);
      const float gt = __builtin_amdgcn_rcpf(1.f + __builtin_amdgcn_exp2f(a * -1.44269504088896f));
      if (part == 0) __builtin_amdgcn_raw_buffer_store_b32(__builtin_bit_cast(unsigned, gt), rsGo, grow * 4, 0, SC1);
    }
    FT(5);
    waitl<0>();
    STAMP(4);
  }
  STAMP(5);
  constexpr int NZ = G1R ? 4 : 8;
  constexpr int W0 = G1R ? WPRE : 0, WG0 = G1R ? 0 : 14;
  auto wcount = [](int gap) constexpr { return gap < WG0 ? 0 : (G1R ? ((gap - WG0) * (60 - WPRE)) / 140 + 1 : (gap - WG0) / 4 + 1); };
  const unsigned foH = C_FH + (((rb >> 1) * 2 + (rb & 1)) * 24 + (lane < 24 ? lane : 0)) * FS;
  unsigned fh;
  constexpr int GPOLL = NZ * 36 - 12;
  f32x4 acc[8];
  {
    const unsigned aA = cb + q4 * CV_HC + 56 - 16 * g + 8 * p4 + 256 * half;
    const unsigned aB = cb + CV_U + 448 + 32 * c16 + 16 * g - 256 * half - 64 * (NZ - 1);
    v4u A[8], B[8];
#pragma unroll
    for (int i = 0; i < 8; ++i) {
      B[7 - i] = lds128(lds + aB + 64 * (NZ - 1) + 512 * (7 - i));
      A[i] = cat(ldstr(lds + aA + 512 * i + 8), ldstr(lds + aA + 512 * i));
    }
    SCHED();
    static_for<0, NZ>([&](auto Z) {
      constexpr int z = Z.value;
      static_for<0, 8>([&](auto I) {
        constexpr int i = I.value;
        static_for<0, 8 - i>([&](auto DD) {
          constexpr int d = 7 - i - DD.value;
          constexpr int gap = z * 36 + (i * (17 - i)) / 2 + DD.value;
          if constexpr (z == 0 && i == 0) mfma_z(acc[i + d], A[i], B[d]); else mfma_ip(acc[i + d], A[i], B[d]);
          if constexpr (d == 7 - i && z + 1 < NZ) B[7 - i] = lds128(lds + aB + 64 * (NZ - 2 - z) + 512 * (7 - i));
          if constexpr (!G1R && gap == WG0 - 2) { waitv<0>(); poll_finish(fw, foW, rsC, tag); }
          if constexpr (wcount(gap) > wcount(gap - 1)) ldW(IC(W0 + wcount(gap) - 1));
          if constexpr (gap == GPOLL) ld32_sc1(fh, foH, rsC);
          SCHED();
        });
        if constexpr (i == 7) { FT(40 + z); SCHED(); }
        if constexpr (z + 1 < NZ) { A[i] = cat(ldstr(lds + aA + 64 * (z + 1) + 512 * i + 8), ldstr(lds + aA + 64 * (z + 1) + 512 * i)); SCHED(); }
      });
    });
  }
  static_assert(W0 + wcount(NZ * 36 - 1) >= 60, "all W2 fragments issued");
  STAMP(6);
  constexpr int WAFT = 60 - (W0 + wcount(GPOLL));
  waitv<(WAFT > 0 ? WAFT : 0)>();
  FT(48);
  poll_finish(fh, foH, rsC, tag);
  FT(49);
  v4u Hf[HWM];
  const unsigned sH = (4 * rb * 48 + 12 * wave) * 1024;
  auto ldH = [&](auto N) { constexpr int n = N.value; if constexpr (n < 48) ldVo<(n & 3) * 1024, SC1>(Hf[n % HWM], vl, rsH, sH + ((n / 12) * 48 + ((n % 12) & ~3)) * 1024); };
  static_for<0, HW0>([&](auto I) { ldH(I); });
  FT(14);
  asm volatile("s_nop 7" : "+v"(acc[0]), "+v"(acc[1]), "+v"(acc[2]), "+v"(acc[3]), "+v"(acc[4]), "+v"(acc[5]), "+v"(acc[6]), "+v"(acc[7]));
  SCHED();
  int ctb = 0;
  if constexpr (G1R) {
    LAS char* ex = lds + CV_EX + cl * 8192;
    f32x4 keep[4];
#pragma unroll
    for (int i = 0; i < 4; ++i) {
      f32x4 lo = acc[i], hi = acc[4 + i];
      asm volatile("" : "+v"(lo), "+v"(hi));
      *(LAS f32x4*)(ex + half * 4096 + i * 1024 + lane * 16) = half == 0 ? hi : lo;
      keep[i] = half == 0 ? lo : hi;
    }
    waitl<0>(); sbar();
#pragma unroll
    for (int i = 0; i < 4; ++i) acc[i] = keep[i] + *(LAS f32x4*)(ex + (1 - half) * 4096 + i * 1024 + lane * 16);
    ctb = 4 * half;
  }
  FT(51);
  constexpr int NT = G1R ? 4 : 8, NYS = NT / 2;
  {
    asm volatile("" : "+v"(dv));
    const float Dc = blo(dv);
#pragma unroll
    for (int i = 0; i < NT; i += 2) {
      unsigned w0[2][2];
#pragma unroll
      for (int e = 0; e < 2; ++e) {
        const int ct = ctb + i + e;
        const f32x4 a = acc[i + e];
        const v2u uu = lds64(lds + cb + CV_U + 480 + 512 * ct + 32 * c16 + 8 * g);
        w0[e][0] = pk2(fmaf(blo(uu.x), Dc, a[0]), fmaf(bhi(uu.x), Dc, a[1]));
        w0[e][1] = pk2(fmaf(blo(uu.y), Dc, a[2]), fmaf(bhi(uu.y), Dc, a[3]));
      }
      auto s0 = __builtin_amdgcn_permlane16_swap(w0[0][0], w0[1][0], false, false);
      auto s1 = __builtin_amdgcn_permlane16_swap(w0[0][1], w0[1][1], false, false);
      const int ct = ctb + i + (g & 1);
      __builtin_amdgcn_raw_buffer_store_b128(v4u{s0[0], s1[0], s0[1], s1[1]}, rsY, ch * 4096 + 512 * ct + 32 * c16 + 8 * (g & 2), 0, SC1);
    }
  }
  FT(52);
  waitl<0>(); sbar();
  STAMP(7);
  unsigned fe[13];
  const bool bump = blockIdx.x == 0 && wave == 0;
  f32x4 ac2[2][5];
  const unsigned foY0 = C_FY + (nt * 128 + lane) * FS, foY1 = foY0 + 64 * FS;
  const unsigned foG = C_FY + ((rb >> 2) * 128 + 96 + (lane & 31)) * FS;
  unsigned fy0, fy1, fg, gtu;
  v4u yv[3]; v2u qq[5];
  const int s = 64 * rb + 16 * wave + c16;
#define SF_ 5
  constexpr int SF = SF_;
  constexpr int SP = 25;
  constexpr int SY = 40;
  static_assert(g2_issued(SP) == 48 && SY >= SP + 8, "");
  auto extras_at = [](int st) constexpr { return (st == SF ? 1 : 0) + ((st >= SP && st < SP + 8) ? 1 : 0) + (st == SY ? 4 : 0); };
  static_for<0, 48>([&](auto I) {
    constexpr int i = I.value, rt = i / 12, k = i % 12, set = rt & 1;
    constexpr int younger = [&]() constexpr {
      int y = g2_issued(i) - (i + 1);
      if (i < HW0) y += NYS;
      int si = -1; while (i >= HW0 && g2_issued(si + 1) <= i) ++si;
      for (int st = (si < 0 ? 0 : si); st < i; ++st) y += extras_at(st);
      return y; }();
    waitv<(younger < 63 ? younger : 63)>();
    static_for<0, 5>([&](auto C) { constexpr int ct = C.value;
      if constexpr (k == 0) mfma_zA(ac2[set][ct], W[k][ct], Hf[i % HWM]); else mfma_ipA(ac2[set][ct], W[k][ct], Hf[i % HWM]);
      if constexpr (rt > 0 && k >= 1 && k <= 5 && ct == 3) *(LAS f32x4*)(lds + G2_PART + (((rt - 1) * 4 + wave) * 5 + (k - 1)) * 1024 + lane * 16) = ac2[set ^ 1][k - 1];
      if constexpr (ct == 1) { static_for<g2_issued(i), g2_issued(i + 1)>([&](auto N) { ldH(N); }); }
    });
    if constexpr (i == SF) {
      waitv<g2_issued(SF + 1) - HW0>();
      if (lane == 0) st32_sc1(tag, C_FY + ((x * 32 + r) * 4 + wave) * FS, rsC);
    }
    if constexpr (i == SP) ld32_sc1(fy0, foY0, rsC);
    if constexpr (i == SP + 1) ld32_sc1(fy1, foY1, rsC);
    if constexpr (i == SP + 2) ld32_sc1(fg, foG, rsC);
    if constexpr (i >= SP + 3 && i < SP + 8) { const rsrc_t rsQ = mkrs(p.q, S * D * 2); ldV2<AUXP>(qq[i - SP - 3], (s * D + 80 * nt + 16 * (i - SP - 3) + 4 * g) * 2, rsQ, 0); }
    if constexpr (i == 34) {
      if (bump) {
#pragma unroll
        for (int e = 0; e < 13; ++e) ld32_sc1(fe[e], e < 12 ? C_FH + (e * 64 + lane) * FS : C_FKV + lane * FS, rsC);
      }
    }
    if constexpr (i == SY) {
      waitv<5>();
      poll_finish2(fy0, foY0, fy1, foY1, rsC, tag);
      poll_finish(fg, foG, rsC, tag);
      { const rsrc_t rsG = mkrs(p.ws + WS_G, S * 4); ld32_sc1(gtu, s * 4, rsG); }
#pragma unroll
      for (int u = 0; u < 3; ++u) {
        const int idx = u * 256 + wave * 64 + lane, d = idx >> 3, cch = idx & 7;
        ldV<SC1>(yv[u], idx < 640 ? (unsigned)((80 * nt + d) * 4096 + rb * 128 + cch * 16) : 0x80000000u, rsY, 0);
      }
    }
    if constexpr (i % 4 == 3) { FT(16 + i / 4); }
    SCHED();
  });
  STAMP(9);
  asm volatile("s_nop 7" : "+v"(ac2[1][0]), "+v"(ac2[1][1]), "+v"(ac2[1][2]), "+v"(ac2[1][3]), "+v"(ac2[1][4]));
#pragma unroll
  for (int ct = 0; ct < 5; ++ct) *(LAS f32x4*)(lds + G2_PART + ((3 * 4 + wave) * 5 + ct) * 1024 + lane * 16) = ac2[1][ct];
  waitv<0>();
#pragma unroll
  for (int u = 0; u < 3; ++u) {
    asm volatile("" : "+v"(yv[u]));
    const int idx = u * 256 + wave * 64 + lane, d = idx >> 3, cch = idx & 7;
    if (idx < 640) { *(LAS v2u*)(lds + G2_Y + d * 136 + cch * 16) = v2u{yv[u].x, yv[u].y}; *(LAS v2u*)(lds + G2_Y + d * 136 + cch * 16 + 8) = v2u{yv[u].z, yv[u].w}; }
  }
  asm volatile("" : "+v"(gtu));
#pragma unroll
  for (int ct = 0; ct < 5; ++ct) asm volatile("" : "+v"(qq[ct]));
  waitl<0>(); sbar();
  STAMP(10);
  const float gt = __builtin_bit_cast(float, gtu);
#pragma unroll
  for (int ct = 0; ct < 5; ++ct) {
    f32x4 b = *(LAS f32x4*)(lds + G2_PART + ((wave * 4 + 0) * 5 + ct) * 1024 + lane * 16);
#pragma unroll
    for (int w = 1; w < 4; ++w) b += *(LAS f32x4*)(lds + G2_PART + ((wave * 4 + w) * 5 + ct) * 1024 + lane * 16);
    const v2u yy = ldstr(lds + G2_Y + (16 * ct + 4 * g + q4) * 136 + (16 * wave + 4 * p4) * 2);
    const float o0 = gt * (blo(qq[ct].x) * blo(yy.x)) + (1.f - gt) * b[0];
    const float o1 = gt * (bhi(qq[ct].x) * bhi(yy.x)) + (1.f - gt) * b[1];
    const float o2 = gt * (blo(qq[ct].y) * blo(yy.y)) + (1.f - gt) * b[2];
    const float o3 = gt * (bhi(qq[ct].y) * bhi(yy.y)) + (1.f - gt) * b[3];
    *(GAS v2u*)(p.out + s * D + 80 * nt + 16 * ct + 4 * g) = v2u{pk2(o0, o1), pk2(o2, o3)};
  }
  if (bump) {
    unsigned bad = 0;
#pragma unroll
    for (int e = 0; e < 13; ++e) { asm volatile("" : "+v"(fe[e])); bad |= fe[e] ^ tag; }
    if (__builtin_amdgcn_ballot_w64(bad != 0) != 0) {
#pragma unroll 1
      for (int e = 0; e < 13; ++e) { const unsigned fo = (e < 12 ? C_FH + (e * 64 + lane) * FS : C_FKV + lane * FS); unsigned f; ld32_sc1(f, fo, rsC); waitv<0>(); poll_finish(f, fo, rsC, tag); }
    }
    if (lane == 0) st32_sc1(tag, C_EPOCH, rsC);
  }
  STAMP(11);
  STAMP_FLUSH(); FT_FLUSH();
}

__global__ __launch_bounds__(256, 1) void fused(Params p) {
  v4u pf;
  {
    unsigned long long pc; unsigned len;
    asm volatile("s_getpc_b64 %0\nLpf_here:\n\ts_mov_b32 %1, Lpf_end-Lpf_here" : "=s"(pc), "=s"(len));
    const rsrc_t rs = mkrs((const void*)pc, len);
    asm volatile("buffer_load_dwordx4 %0, %1, %2, 0 offen" : "=v"(pf) : "v"(((blockIdx.x >> 3) * 256 + threadIdx.x) * 16), "s"(rs));
    SCHED();
  }
  extern __shared__ __attribute__((aligned(16))) char smem[];
  LAS char* lds = (LAS char*)smem;
  const int lane = threadIdx.x & 63, wave = __builtin_amdgcn_readfirstlane(threadIdx.x >> 6);
  const int x = blockIdx.x & 7, r = blockIdx.x >> 3;
  unsigned long long m_c0 = __builtin_amdgcn_s_memtime(), m_r0 = __builtin_amdgcn_s_memrealtime();
  { f32x16 c0; for (int e = 0; e < 16; ++e) c0[e] = 0.f; f32x16 c1 = c0, c2 = c0, c3 = c0;
    v4u a = {threadIdx.x * 2654435761u | 0x3c003c00u, (threadIdx.x * 40503u) ^ 0x3f803e00u, threadIdx.x * 7919u + 0x3d003d00u, 0x3c013d02u ^ (threadIdx.x << 3)};
    v4u b = {a.y ^ 0x01230456u, a.z, a.w ^ 0x00770011u, a.x};
    asm volatile("s_nop 4" : "+v"(c0), "+v"(c1), "+v"(c2), "+v"(c3));
#pragma unroll 1
    for (int i = 0; i < 64; ++i) { asm volatile("v_mfma_f32_32x32x16_bf16 %0, %4, %5, %0\n v_mfma_f32_32x32x16_bf16 %1, %4, %5, %1\n v_mfma_f32_32x32x16_bf16 %2, %5, %4, %2\n v_mfma_f32_32x32x16_bf16 %3, %5, %4, %3\n v_mfma_f32_32x32x16_bf16 %0, %5, %5, %0\n v_mfma_f32_32x32x16_bf16 %1, %4, %4, %1\n v_mfma_f32_32x32x16_bf16 %2, %4, %5, %2\n v_mfma_f32_32x32x16_bf16 %3, %5, %4, %3" : "+v"(c0), "+v"(c1), "+v"(c2), "+v"(c3) : "v"(a), "v"(b)); }
    asm volatile("s_nop 15" :: "v"(c0), "v"(c1), "v"(c2), "v"(c3)); }
  unsigned long long m_c1 = __builtin_amdgcn_s_memtime(), m_r1 = __builtin_amdgcn_s_memrealtime();
  if (__builtin_expect(r < 24, 1)) body<true>(p, lds, x, r, wave, lane);
  else body<false>(p, lds, x, r, wave, lane);
  asm volatile("s_waitcnt vmcnt(0)\nLpf_end:" : "+v"(pf));
  if (blockIdx.x == 5) { const unsigned long long d_ = 0 + 200, e0 = __builtin_amdgcn_s_memrealtime(); while (__builtin_amdgcn_s_memrealtime() - e0 < d_) __builtin_amdgcn_s_sleep(1); }
  asm volatile("" :: "s"(m_c1), "s"(m_r1), "s"(m_c0), "s"(m_r0));
}

extern "C" void kernel_launch(void* const* d_in, const int* in_sizes, int n_in, void* d_out, int out_size, void* d_ws, size_t ws_size, hipStream_t stream) {
  Params p;
  p.q = (const u16*)d_in[0]; p.k = (const u16*)d_in[1]; p.v = (const u16*)d_in[2]; p.sk = (const u16*)d_in[3];
  p.h = (const u16*)d_in[4]; p.Dv = (const u16*)d_in[5]; p.w1 = (const u16*)d_in[7]; p.w2 = (const u16*)d_in[8]; p.gw = (const u16*)d_in[9];
  p.out = (u16*)d_out; p.ws = (char*)d_ws;
  (void)hipFuncSetAttribute((const void*)fused, hipFuncAttributeMaxDynamicSharedMemorySize, LDS_BYTES);
  fused<<<256, 256, LDS_BYTES, stream>>>(p);
}
```

```cpp
#include <hip/hip_runtime.h>
#include <type_traits>
#define SPIN_SLEEP 4
#define FSTR 64
#define TOUCH_T 1
#include <stdint.h>

typedef unsigned short u16;
typedef unsigned v4u __attribute__((ext_vector_type(4)));
typedef unsigned v2u __attribute__((ext_vector_type(2)));
typedef float f32x4 __attribute__((ext_vector_type(4)));
typedef float f32x2 __attribute__((ext_vector_type(2)));
typedef __bf16 bf16x8 __attribute__((ext_vector_type(8)));
typedef __bf16 bf16x2 __attribute__((ext_vector_type(2)));
typedef short v4s __attribute__((ext_vector_type(4)));
typedef short v8s __attribute__((ext_vector_type(8)));
#define GAS __attribute__((address_space(1)))
#define LAS __attribute__((address_space(3)))
#define DEV __device__ __forceinline__
typedef __amdgpu_buffer_rsrc_t rsrc_t;

constexpr int S = 2048, D = 640, F = 1536;

template <int N> DEV void waitv() { asm volatile("s_waitcnt vmcnt(%0)" ::"i"(N) : "memory"); }
template <int N> DEV void waitl() { asm volatile("s_waitcnt lgkmcnt(%0)" ::"i"(N) : "memory"); }
DEV void sbar() { asm volatile("s_barrier" ::: "memory"); }
#define SCHED() __builtin_amdgcn_sched_barrier(0)
DEV rsrc_t mkrs(const void* p, unsigned bytes) { return __builtin_amdgcn_make_buffer_rsrc((void*)p, 0, bytes, 0x00020000); }
DEV f32x4 mfma(v4u a, v4u b, f32x4 c) {
  return __builtin_amdgcn_mfma_f32_16x16x32_bf16(__builtin_bit_cast(bf16x8, a), __builtin_bit_cast(bf16x8, b), c, 0, 0, 0);
}
DEV v4u lds128(LAS const char* p) { return *(LAS const v4u*)p; }
DEV v2u lds64(LAS const char* p) { return *(LAS const v2u*)p; }
DEV v2u ldstr(LAS const char* p) { return __builtin_bit_cast(v2u, __builtin_amdgcn_ds_read_tr16_b64_v4i16((LAS v4s*)p)); }
DEV v4u cat(v2u a, v2u b) { return v4u{a.x, a.y, b.x, b.y}; }
DEV unsigned pk2(float a, float b) { bf16x2 r = __builtin_convertvector(f32x2{a, b}, bf16x2); return __builtin_bit_cast(unsigned, r); }
DEV float blo(unsigned d) { return __builtin_bit_cast(float, d << 16); }
DEV float bhi(unsigned d) { return __builtin_bit_cast(float, d & 0xffff0000u); }
DEV void setm0(unsigned v) { asm volatile("s_mov_b32 m0, %0\n\ts_nop 0" ::"s"(v)); }
template <int OFF, int NT = 0> DEV void dma16(unsigned voff, rsrc_t rs, unsigned soff) {
  if constexpr (NT) asm volatile("buffer_load_dwordx4 %0, %1, %2 offen offset:%3 nt lds" ::"v"(voff), "s"(rs), "s"(soff), "i"(OFF));
  else asm volatile("buffer_load_dwordx4 %0, %1, %2 offen offset:%3 lds" ::"v"(voff), "s"(rs), "s"(soff), "i"(OFF));
}
typedef float f32x16 __attribute__((ext_vector_type(16)));
DEV f32x16 mfma32(v4u a, v4u b, f32x16 c) {
  return __builtin_amdgcn_mfma_f32_32x32x16_bf16(__builtin_bit_cast(bf16x8, a), __builtin_bit_cast(bf16x8, b), c, 0, 0, 0);
}
template <int IMM> DEV void setm0_add(unsigned base) { asm volatile("s_add_u32 m0, %0, %1" ::"s"(base), "i"(IMM) : "scc"); }
template <int I, int N, class Fn> DEV void static_for(Fn&& f) { if constexpr (I < N) { f(std::integral_constant<int, I>{}); static_for<I + 1, N>(f); } }
DEV void mfma_ip(f32x4& c, const v4u& a, const v4u& b) { asm volatile("v_mfma_f32_16x16x32_bf16 %0, %1, %2, %0" : "+v"(c) : "v"(a), "v"(b)); }
DEV void mfma_ipA(f32x4& c, const v4u& a, const v4u& b) { asm volatile("v_mfma_f32_16x16x32_bf16 %0, %1, %2, %0" : "+v"(c) : "a"(a), "v"(b)); }
DEV void mfma_z(f32x4& c, const v4u& a, const v4u& b) { asm volatile("v_mfma_f32_16x16x32_bf16 %0, %1, %2, 0" : "=&v"(c) : "v"(a), "v"(b)); }
DEV void mfma_zA(f32x4& c, const v4u& a, const v4u& b) { asm volatile("v_mfma_f32_16x16x32_bf16 %0, %1, %2, 0" : "=&v"(c) : "a"(a), "v"(b)); }
template <int AUX> DEV void ldA(v4u& d, unsigned voff, rsrc_t rs, unsigned soff) {
  if constexpr (AUX) asm volatile("buffer_load_dwordx4 %0, %1, %2, %3 offen sc1" : "=a"(d) : "v"(voff), "s"(rs), "s"(soff));
  else asm volatile("buffer_load_dwordx4 %0, %1, %2, %3 offen" : "=a"(d) : "v"(voff), "s"(rs), "s"(soff));
}
template <int AUX> DEV void ldV(v4u& d, unsigned voff, rsrc_t rs, unsigned soff) {
  if constexpr (AUX == 2) asm volatile("buffer_load_dwordx4 %0, %1, %2, %3 offen nt" : "=v"(d) : "v"(voff), "s"(rs), "s"(soff));
  else if constexpr (AUX == 16) asm volatile("buffer_load_dwordx4 %0, %1, %2, %3 offen sc1" : "=v"(d) : "v"(voff), "s"(rs), "s"(soff));
  else asm volatile("buffer_load_dwordx4 %0, %1, %2, %3 offen" : "=v"(d) : "v"(voff), "s"(rs), "s"(soff));
}
DEV void nop8() { asm volatile("s_nop 7"); }

constexpr size_t WS_CTRL = 0;
constexpr size_t WS_KVT = 262144;
constexpr size_t WS_YT = WS_KVT + (size_t)D * S * 2;
constexpr size_t WS_W2T = WS_YT + (size_t)D * S * 2;
constexpr size_t WS_HF = WS_W2T + (size_t)8 * 48 * 5 * 1024;
constexpr size_t WS_G = WS_HF + (size_t)128 * 48 * 1024;
constexpr size_t WS_END = WS_G + S * 4;
constexpr unsigned C_EPOCH = 0, C_TMO = 256;
constexpr unsigned FS = FSTR;
constexpr unsigned C_FW = 4096;
constexpr unsigned C_FKV = C_FW + 64 * FS;
constexpr unsigned C_FH = C_FKV + 64 * FS;
constexpr unsigned C_FY = C_FH + 768 * FS;
constexpr unsigned C_BYTES = 262144;
static_assert(C_FY + 1024 * FS <= C_BYTES, "");

#define NSTAMP 16
#define STAMP_OFF (size_t)(32u << 20)
#define STAMP_DECL
#define STAMP(i)
#define STAMP_FLUSH()
#define FT_DECL
#define FT(i)
#define FT_FLUSH()

struct Params {
  const u16 *q, *k, *v, *sk, *h, *Dv, *w1, *w2, *gw;
  u16* out;
  char* ws;
};

constexpr int LDS_BYTES = 160 * 1024;
constexpr int SC1 = 16;
#define NT_X 0
#define NT_W1 0
#define NT_P 0
constexpr int AUXP = NT_P ? 2 : 0;
#define IC(n) std::integral_constant<int, (n)>{}

DEV void ld32_sc1(unsigned& v, unsigned voff, rsrc_t rs) { asm volatile("buffer_load_dword %0, %1, %2, 0 offen sc1" : "=v"(v) : "v"(voff), "s"(rs)); }
DEV void st32_sc1(unsigned v, unsigned voff, rsrc_t rs) { asm volatile("buffer_store_dword %0, %1, %2, 0 offen sc1" ::"v"(v), "v"(voff), "s"(rs)); }
DEV void poll_finish(unsigned v, unsigned voff, rsrc_t rsC, unsigned tag) {
  asm volatile("" : "+v"(v));
  if (__builtin_amdgcn_ballot_w64(v != tag) == 0) return;
#pragma unroll 1
  for (int spins = 0; spins < (1 << 15); ++spins) {
    __builtin_amdgcn_s_sleep(SPIN_SLEEP);
    ld32_sc1(v, voff, rsC);
    asm volatile("s_waitcnt vmcnt(0)" : "+v"(v)::"memory");
    if (__builtin_amdgcn_ballot_w64(v != tag) == 0) return;
  }
  st32_sc1(1u, C_TMO, rsC);
}

DEV void poll_finish2(unsigned a, unsigned oa, unsigned b, unsigned ob, rsrc_t rsC, unsigned tag) {
  asm volatile("" : "+v"(a), "+v"(b));
  if (__builtin_amdgcn_ballot_w64(a != tag || b != tag) == 0) return;
#pragma unroll 1
  for (int spins = 0; spins < (1 << 15); ++spins) {
    __builtin_amdgcn_s_sleep(SPIN_SLEEP);
    ld32_sc1(a, oa, rsC); ld32_sc1(b, ob, rsC);
    asm volatile("s_waitcnt vmcnt(0)" : "+v"(a), "+v"(b)::"memory");
    if (__builtin_amdgcn_ballot_w64(a != tag || b != tag) == 0) return;
  }
  st32_sc1(1u, C_TMO, rsC);
}

DEV void mfma32_ip(f32x16& c, const v4u& a, const v4u& b) { asm volatile("v_mfma_f32_32x32x16_bf16 %0, %1, %2, %0" : "+v"(c) : "v"(a), "v"(b)); }
DEV void mfma32_z(f32x16& c, const v4u& a, const v4u& b) { asm volatile("v_mfma_f32_32x32x16_bf16 %0, %1, %2, 0" : "=&v"(c) : "v"(a), "v"(b)); }

constexpr int KV_BASE = 32768, KV_GS = 1152, KV_V = 9 * KV_GS, KV_CG = 17 * KV_GS;
constexpr int CV_BASE = 32768;
constexpr int CV_HC = 4160;
constexpr int CV_U = 4 * CV_HC;
constexpr int CV_CH = CV_U + 4608;
constexpr int CV_EX = CV_BASE + 4 * CV_CH;
constexpr int G2_PART = 0;
constexpr int G2_Y = 81920;
constexpr int HW = 24;

template <int AUX> DEV void ldV2(v2u& d, unsigned voff, rsrc_t rs, unsigned soff) {
  if constexpr (AUX == 2) asm volatile("buffer_load_dwordx2 %0, %1, %2, %3 offen nt" : "=v"(d) : "v"(voff), "s"(rs), "s"(soff));
  else if constexpr (AUX == 16) asm volatile("buffer_load_dwordx2 %0, %1, %2, %3 offen sc1" : "=v"(d) : "v"(voff), "s"(rs), "s"(soff));
  else asm volatile("buffer_load_dwordx2 %0, %1, %2, %3 offen" : "=v"(d) : "v"(voff), "s"(rs), "s"(soff));
}
DEV void dma16s(unsigned voff, rsrc_t rs, unsigned soff, unsigned m0v) {
  if constexpr (NT_P) asm volatile("s_mov_b32 m0, %3\n\ts_nop 0\n\tbuffer_load_dwordx4 %0, %1, %2 offen nt lds" ::"v"(voff), "s"(rs), "s"(soff), "s"(m0v));
  else asm volatile("s_mov_b32 m0, %3\n\ts_nop 0\n\tbuffer_load_dwordx4 %0, %1, %2 offen lds" ::"v"(voff), "s"(rs), "s"(soff), "s"(m0v));
}
DEV float dot2(unsigned a, unsigned b, float c) { return __builtin_amdgcn_fdot2_f32_bf16(__builtin_bit_cast(bf16x2, a), __builtin_bit_cast(bf16x2, b), c, false); }
DEV unsigned mktag(unsigned& epoch) {
  asm volatile("" : "+v"(epoch));
  return __builtin_amdgcn_readfirstlane(epoch * 0x9E3779B1u + 0x7F4A7C15u);
}

template <int NR, class ST> DEV void kv_math(const v4u (&kk)[NR + 3], const v4u (&vv)[NR], const v4u (&ww)[4], ST&& st) {
#pragma unroll
  for (int cp = 0; cp < 4; ++cp) {
    f32x2 w[4];
    w[0] = f32x2{blo(ww[cp].x), blo(ww[cp].z)}; w[1] = f32x2{bhi(ww[cp].x), bhi(ww[cp].z)};
    w[2] = f32x2{blo(ww[cp].y), blo(ww[cp].w)}; w[3] = f32x2{bhi(ww[cp].y), bhi(ww[cp].w)};
    f32x2 kf[NR + 3];
#pragma unroll
    for (int i = 0; i < NR + 3; ++i) kf[i] = f32x2{blo(kk[i][cp]), bhi(kk[i][cp])};
    f32x2 r[NR];
#pragma unroll
    for (int i = 0; i < NR; ++i) {
      f32x2 a = kf[i + 3] * w[0];
      a = __builtin_elementwise_fma(kf[i + 2], w[1], a);
      a = __builtin_elementwise_fma(kf[i + 1], w[2], a);
      a = __builtin_elementwise_fma(kf[i], w[3], a);
      r[i] = a * f32x2{blo(vv[i][cp]), bhi(vv[i][cp])};
    }
    st(cp, r);
  }
}

#define Q0 7
#define ROT 0
#define PREP_FIRST 0
#define TOUCH 1
#define TOUCH_S (TOUCH_T < 0 ? 3 : TOUCH_T + 4)
#define HPUB 90
#define WPRE_ 16
constexpr int WPRE = WPRE_;
constexpr int HW0 = 16, HWM = 24;
constexpr int PW_BASE = 131072, PW_SZ = 3072;
constexpr int g2_issued(int i) { int r = HW0; for (int s = 0; s < i; ++s) r += (r - s < HWM) ? ((r + 1 - s < HWM) ? 2 : 1) : 0; return r < 48 ? r : 48; }

template <int OFF, int AUX> DEV void ldVo(v4u& d, unsigned voff, rsrc_t rs, unsigned soff) {
  static_assert(AUX == 16, "");
  asm volatile("buffer_load_dwordx4 %0, %1, %2, %3 offen offset:%4 sc1" : "=v"(d) : "v"(voff), "s"(rs), "s"(soff), "i"(OFF));
}
template <int OFF> DEV void ldAo(v4u& d, unsigned voff, rsrc_t rs, unsigned soff) {
  asm volatile("buffer_load_dwordx4 %0, %1, %2, %3 offen offset:%4 sc1" : "=a"(d) : "v"(voff), "s"(rs), "s"(soff), "i"(OFF));
}

template <bool G1R>
DEV void body(const Params& p, LAS char* lds, const int x, const int r, const int wave, const int lane) {
  const int g = lane >> 4, q4 = (lane >> 2) & 3, p4 = lane & 3, c16 = lane & 15;
  STAMP_DECL; FT_DECL;
  STAMP(0); FT(13);
  const rsrc_t rsC = mkrs(p.ws + WS_CTRL, C_BYTES);
  unsigned epoch, tag;
  ld32_sc1(epoch, C_EPOCH, rsC);
  const int cl = G1R ? (wave >> 1) : wave, half = G1R ? (wave & 1) : 0;
  const int ch = (G1R ? 80 * x + 2 * r : 80 * x + 48 + 4 * (r - 24)) + cl;
  const unsigned cb = CV_BASE + cl * CV_CH;
  constexpr int NCHK = G1R ? 2 : 4;
  unsigned dv;
  { const rsrc_t rsD = mkrs(p.Dv, D * 2); asm volatile("buffer_load_ushort %0, %1, %2, 0 offen" : "=v"(dv) : "v"(ch * 2), "s"(rsD)); }
  const rsrc_t rsHh = mkrs(p.h, D * S * 2), rsU = mkrs(p.ws + WS_KVT, D * S * 2), rsY = mkrs(p.ws + WS_YT, D * S * 2);
  const rsrc_t rsW2 = mkrs(p.ws + WS_W2T, 8 * 48 * 5 * 1024), rsH = mkrs(p.ws + WS_HF, 128 * 48 * 1024);
#define MAP42 1
  const int rb = MAP42 ? 8 * (x >> 1) + (r & 7) : 4 * x + (r & 3), nt = MAP42 ? 4 * (x & 1) + (r >> 3) : r >> 2;
  const unsigned vl = lane * 16;
  const unsigned sW = (nt * 48 + 12 * wave) * 5 * 1024;
  const unsigned foW = C_FW + lane * FS, foKV = C_FKV + lane * FS;
  v4u W[12][5];
  auto ldW = [&](auto I) { constexpr int i = I.value; if constexpr (i >= 0 && i < 60) ldAo<(i & 3) * 1024>(W[i / 5][i % 5], vl, rsW2, sW + (i & ~3) * 1024); };
  v4u hc[NCHK], uc[NCHK]; v2u hp0;
  auto ld_h = [&]() {
    ldV2<AUXP>(hp0, half > 0 ? (unsigned)(ch * 4096 + half * NCHK * 1024 - 8) : 0x80000000u, rsHh, 0);
#pragma unroll
    for (int i = 0; i < NCHK; ++i) ldV<AUXP>(hc[i], ch * 4096 + ((half * NCHK + i) * 64 + lane) * 16, rsHh, 0);
  };
  auto ld_u = [&]() {
#pragma unroll
    for (int i = 0; i < NCHK; ++i) ldV<SC1>(uc[i], ch * 4096 + ((half * NCHK + i) * 64 + lane) * 16, rsU, 0);
  };
  auto fill_h = [&]() {
    asm volatile("" : "+v"(hp0));
#pragma unroll
    for (int i = 0; i < NCHK; ++i) asm volatile("" : "+v"(hc[i]));
    unsigned e0 = hp0.x, e1 = hp0.y;
#pragma unroll
    for (int i = 0; i < NCHK; ++i) {
      const int n = (half * NCHK + i) * 64 + lane;
      const v4u cur = hc[i];
      const v2u prv = v2u{(unsigned)__builtin_amdgcn_update_dpp((int)e0, (int)cur.z, 0x138, 0xf, 0xf, false), (unsigned)__builtin_amdgcn_update_dpp((int)e1, (int)cur.w, 0x138, 0xf, 0xf, false)};
      e0 = __builtin_amdgcn_readlane(cur.z, 63); e1 = __builtin_amdgcn_readlane(cur.w, 63);
      const unsigned om2 = __builtin_amdgcn_alignbit(prv.y, prv.x, 16), om1 = __builtin_amdgcn_alignbit(cur.x, prv.y, 16);
      const unsigned o0 = __builtin_amdgcn_alignbit(cur.y, cur.x, 16), o1 = __builtin_amdgcn_alignbit(cur.z, cur.y, 16), o2 = __builtin_amdgcn_alignbit(cur.w, cur.z, 16);
      LAS char* dst = lds + cb + (n + 2) * 16;
      *(LAS v4u*)(dst) = cur;
      *(LAS v4u*)(dst + CV_HC) = v4u{om1, o0, o1, o2};
      *(LAS v4u*)(dst + 2 * CV_HC) = v4u{prv.y, cur.x, cur.y, cur.z};
      *(LAS v4u*)(dst + 3 * CV_HC) = v4u{om2, om1, o0, o1};
    }
    if (half == 0) {
      if (lane < 8) *(LAS v4u*)(lds + cb + (lane >> 1) * CV_HC + (lane & 1) * 16) = v4u{0, 0, 0, 0};
      if (lane >= 32 && lane < 62) *(LAS v4u*)(lds + cb + CV_U + (lane - 32) * 16) = v4u{0, 0, 0, 0};
    }
  };
  auto fill_u = [&]() {
#pragma unroll
    for (int i = 0; i < NCHK; ++i) {
      asm volatile("" : "+v"(uc[i]));
      *(LAS v4u*)(lds + cb + CV_U + 480 + ((half * NCHK + i) * 64 + lane) * 16) = uc[i];
    }
  };
  unsigned fw;

  if constexpr (G1R) {
    const int sb = 4 * (x >> 1) + (r & 3), fs = 6 * (x & 1) + (r >> 2);
    f32x16 acc1[2][2];
    const rsrc_t rsX = mkrs(p.q, S * D * 2), rsW = mkrs(p.w1, D * F * 2);
    unsigned vX[4], vW[4];
#pragma unroll
    for (int ii = 0; ii < 4; ++ii) {
      vX[ii] = (32 * wave + 8 * ii + (lane >> 3)) * 1280 + (((lane & 7) ^ (4 * (ii & 1) + g)) << 4) - 1024 * ii;
      vW[ii] = (16 * wave + 4 * ii + g) * 3072 + (((((c16 >> 1) ^ (g << 1)) << 1) | (c16 & 1)) << 4) - 1024 * ii;
    }
    const unsigned sX0 = sb * 128 * 1280, sW0 = fs * 256;
    const unsigned mX = wave * 4096, mW = 16384 + wave * 4096;
    static_for<0, 7>([&](auto Q) { constexpr int q = Q.value, t = q >> 1;
      setm0_add<t * 32768>((q & 1) ? mW : mX); asm volatile("s_nop 0");
      static_for<0, 4>([&](auto I) { constexpr int i = I.value;
        if constexpr (q & 1) dma16<1024 * i, NT_W1>(vW[i], rsW, sW0 + t * (64 * 3072)); else dma16<1024 * i, NT_X>(vX[i], rsX, sX0 + t * 128); });
    });
    unsigned tch0 = 0, tch1 = 0;
    auto touch = [&]() {
      const int fsl = r >> 2, sbl = r & 3;
      const int slw = (wave < 2 ? TOUCH_S : TOUCH_S + 4) + sbl;
      const unsigned ow = (unsigned)((64 * slw + (wave & 1) * 32 + (lane >> 1)) * 3072 + (lane & 1) * 128);
      asm volatile("buffer_load_dword %0, %1, %2, %3 offen" : "=v"(tch0) : "v"(slw < 10 ? ow : 0x80000000u), "s"(rsW), "s"(sW0));
      const int slx = TOUCH_S + 1 + fsl;
      asm volatile("buffer_load_dword %0, %1, %2, %3 offen" : "=v"(tch1) : "v"((wave < 2 && slx < 10) ? (unsigned)((wave * 64 + lane) * 1280 + slx * 128) : 0x80000000u), "s"(rsX), "s"(sX0));
    };
    if (TOUCH_T < 0) touch();
    FT(11);
    const int ws_ = wave & 1, wf = wave >> 1, h = lane >> 5, r32 = lane & 31;
    unsigned aX[4], aW[2];
#pragma unroll
    for (int ks = 0; ks < 4; ++ks) aX[ks] = (64 * ws_ + r32) * 128 + (((2 * ks + h) ^ ((r32 >> 1) & 7)) << 4);
#pragma unroll
    for (int ft = 0; ft < 2; ++ft) aW[ft] = 16384 + (8 * h + q4) * 256 + (((2 * (2 * wf + ft) + (g & 1)) ^ (q4 << 1)) << 5) + 8 * p4;
#pragma unroll
    for (int a = 0; a < 2; ++a)
#pragma unroll
      for (int b = 0; b < 2; ++b)
#pragma unroll
        for (int e = 0; e < 16; ++e) acc1[a][b][e] = 0.f;
    asm volatile("s_nop 1" : "+v"(acc1[0][0]), "+v"(acc1[0][1]), "+v"(acc1[1][0]), "+v"(acc1[1][1]));
    v4u A[2][2][2], B[2][2][2];
    v2u At;
    unsigned cX[2], cW[2];
    auto rd = [&](auto HF, auto SET, auto N) {
      constexpr int hf = HF.value, set = SET.value, n = N.value;
      constexpr int kk = n / 6, rr = n % 6, ks = 2 * hf + kk;
      if constexpr (rr == 0) B[set][kk][0] = lds128(lds + cX[kk]);
      else if constexpr (rr == 3) B[set][kk][1] = lds128(lds + cX[kk] + 32 * 128);
      else { constexpr int ft = rr / 3, jj = (rr % 3) - 1;
        v2u xx = ldstr(lds + cW[ft] + ks * 4096 + jj * 1024);
        if constexpr (jj == 0) At = xx; else A[set][kk][ft] = cat(At, xx); }
    };
    auto set_addr = [&](int hf, unsigned bo) { cX[0] = aX[2 * hf] + bo; cX[1] = aX[2 * hf + 1] + bo; cW[0] = aW[0] + bo; cW[1] = aW[1] + bo; };
    unsigned fk;
    waitv<20>(); sbar();
    STAMP(1);
    set_addr(0, 0);
    static_for<0, 12>([&](auto N) { rd(IC(0), IC(0), N); });
    SCHED();
    unsigned bo = 0;
    unsigned bW = 3 * 32768, bX = 4 * 32768;
    unsigned sWn = sW0 + 3 * (64 * 3072), sXn = sX0 + 4 * 128;
    auto stage = [&](auto DMA, int t) {
      constexpr bool dma = DMA.value;
      const unsigned nbo = bo == 4 * 32768 ? 0 : bo + 32768;
      set_addr(1, bo);
      static_for<0, 8>([&](auto M) {
        constexpr int m = M.value, kk = m >> 2, ft = (m >> 1) & 1, st = m & 1;
        mfma32_ip(acc1[ft][st], A[0][kk][ft], B[0][kk][st]);
        if constexpr (m < 6) { rd(IC(1), IC(1), IC(2 * m)); rd(IC(1), IC(1), IC(2 * m + 1)); }
        if constexpr (dma && m == 0) asm volatile("s_add_u32 m0, %0, %1" ::"s"(mW), "s"(bW) : "scc");
        if constexpr (dma && (m & 1)) dma16<1024 * (m >> 1), NT_W1>(vW[m >> 1], rsW, sWn);
        SCHED();
      });
      if constexpr (dma) { waitv<16>(); sbar(); }
      else { if (t == 7) waitv<8>(); else waitv<0>(); sbar(); }
      SCHED();
      set_addr(0, nbo);
      const unsigned sx = (dma && t < 6) ? sXn : 0x80000000u;
      static_for<0, 8>([&](auto M) {
        constexpr int m = M.value, kk = m >> 2, ft = (m >> 1) & 1, st = m & 1;
        mfma32_ip(acc1[ft][st], A[1][kk][ft], B[1][kk][st]);
        if constexpr (m < 6) { rd(IC(0), IC(0), IC(2 * m)); rd(IC(0), IC(0), IC(2 * m + 1)); }
        if constexpr (dma && m == 0) asm volatile("s_add_u32 m0, %0, %1" ::"s"(mX), "s"(bX) : "scc");
        if constexpr (dma && (m & 1)) dma16<1024 * (m >> 1), NT_X>(vX[m >> 1], rsX, sx);
        SCHED();
      });
      bo = nbo;
      bW = bW == 4 * 32768 ? 0 : bW + 32768; bX = bX == 4 * 32768 ? 0 : bX + 32768;
      sWn += 64 * 3072; sXn += 128;
      if constexpr (dma) { if (t == TOUCH_T) touch(); }
    };
#pragma unroll 1
    for (int t = 0; t < 7; ++t) stage(std::true_type{}, t);
#pragma unroll 1
    for (int t = 7; t < 10; ++t) stage(std::false_type{}, t);
    asm volatile("" ::"v"(tch0), "v"(tch1));
    asm volatile("s_nop 15" : "+v"(acc1[0][0]), "+v"(acc1[0][1]), "+v"(acc1[1][0]), "+v"(acc1[1][1]));
    sbar();
    STAMP(2);
    tag = mktag(epoch);
    ld_h();
    const unsigned vo = (g & 1) * (48 * 1024) + (c16 + 16 * h) * 16;
    static_for<0, 4>([&](auto Q) {
      constexpr int qd = Q.value, st = qd >> 1, ft = qd & 1;
      const f32x16 a = acc1[ft][st];
      unsigned o[8];
      static_for<0, 2>([&](auto G) {
        constexpr int gq = G.value;
        static_for<0, 4>([&](auto E) {
          constexpr int e = 8 * gq + 2 * E.value;
          const float r0 = a[e] * __builtin_amdgcn_rcpf(1.f + __builtin_amdgcn_exp2f(a[e] * -1.44269504088896f));
          const float r1 = a[e + 1] * __builtin_amdgcn_rcpf(1.f + __builtin_amdgcn_exp2f(a[e + 1] * -1.44269504088896f));
          o[e >> 1] = pk2(r0, r1);
        });
        SCHED();
      });
      const unsigned so = ((8 * sb + 4 * ws_ + 2 * st) * 48 + 4 * fs + 2 * wf + ft) * 1024;
      __builtin_amdgcn_raw_buffer_store_b128(v4u{o[0], o[1], o[4], o[5]}, rsH, vo, so, SC1);
      __builtin_amdgcn_raw_buffer_store_b128(v4u{o[2], o[3], o[6], o[7]}, rsH, vo + 512, so, SC1);
      SCHED();
      FT(32 + qd); SCHED();
    });
    ld32_sc1(fk, foKV, rsC); ld32_sc1(fw, foW, rsC);
    waitv<10>();
    fill_h();
    FT(54);
    waitv<0>();
    if (lane == 0) st32_sc1(tag, C_FH + ((sb * 2 + ws_) * 24 + fs * 2 + wf) * FS, rsC);
    FT(55);
    poll_finish(fk, foKV, rsC, tag);
    ld_u();
    FT(36);
    poll_finish(fw, foW, rsC, tag);
    FT(37);
    static_for<0, WPRE>([&](auto I) { ldW(I); });
    STAMP(3);
    waitv<WPRE>();
    fill_u();
    waitl<0>(); sbar();
    STAMP(4);
  } else {
    const int j = (r - 24) * 8 + x, rbk = j >> 1, cg0 = 5 * (j & 1);
    const rsrc_t rsK = mkrs(p.k, S * D * 2), rsV = mkrs(p.v, S * D * 2), rsS = mkrs(p.sk, D * 8), rsO = mkrs(p.ws + WS_KVT, D * S * 2);
    const unsigned vr = (lane >> 3) * 1280 + ((lane & 7) << 4), vr4 = vr ^ 64;
    auto dmaK = [&](int cgl, int G) {
      const int row = 64 * rbk - 8 + 8 * G;
      dma16s(((G >> 1) & 1) ? vr4 : vr, rsK, row >= 0 ? (unsigned)(row * 1280 + (cg0 + cgl) * 128) : 0x80000000u, KV_BASE + cgl * KV_CG + G * KV_GS);
    };
    auto dmaV = [&](int cgl, int G) {
      dma16s(((G >> 1) & 1) ? vr4 : vr, rsV, (64 * rbk + 8 * G) * 1280 + (cg0 + cgl) * 128, KV_BASE + cgl * KV_CG + KV_V + G * KV_GS);
    };
#pragma unroll
    for (int G = 0; G < 9; ++G) dmaK(wave, G);
#pragma unroll
    for (int G = 0; G < 8; ++G) dmaV(wave, G);
    const unsigned pw = PW_BASE + wave * PW_SZ;
    dma16s(lane < 32 ? (unsigned)((cg0 + wave) * 512 + lane * 16) : (lane < 40 ? (unsigned)((64 * (cg0 + 4) + 16 * wave) * 8 + (lane - 32) * 16) : 0x80000000u), rsS, 0, pw);
    dmaK(4, wave); dmaK(4, wave + 4); dmaV(4, wave); dmaV(4, wave + 4);
    if (wave == 0) dmaK(4, 8);
    const int cc = lane >> 3, rg = lane & 7;
    FT(10);
    waitv<4>();
    STAMP(1);
    {
      const unsigned kb = KV_BASE + wave * KV_CG;
      const unsigned a0 = kb + rg * KV_GS + ((cc ^ (4 * ((rg >> 1) & 1))) << 4), a1 = kb + (rg + 1) * KV_GS + ((cc ^ (4 * (((rg + 1) >> 1) & 1))) << 4);
      v4u kk[11], vv[8], ww[4];
#pragma unroll
      for (int i = 0; i < 4; ++i) ww[i] = lds128(lds + pw + cc * 64 + i * 16);
#pragma unroll
      for (int i = 0; i < 11; ++i) kk[i] = i < 3 ? lds128(lds + a0 + (5 + i) * 128) : lds128(lds + a1 + (i - 3) * 128);
#pragma unroll
      for (int i = 0; i < 8; ++i) vv[i] = lds128(lds + a0 + KV_V + i * 128);
      const unsigned ob = (64 * (cg0 + wave) + 8 * cc) * 4096 + (64 * rbk + 8 * rg) * 2;
      kv_math<8>(kk, vv, ww, [&](int cp, const f32x2 (&rr)[8]) {
#pragma unroll
        for (int c = 0; c < 2; ++c) {
          v4u o{pk2(rr[0][c], rr[1][c]), pk2(rr[2][c], rr[3][c]), pk2(rr[4][c], rr[5][c]), pk2(rr[6][c], rr[7][c])};
          __builtin_amdgcn_raw_buffer_store_b128(o, rsO, ob + (2 * cp + c) * 4096, 0, SC1);
        }
      });
    }
    const int id = 4 * j + wave, pk = id / 5, blk = id % 5;
    const rsrc_t rsWs = mkrs(p.w2, F * D * 2);
    const unsigned ldsb = wave * 8192;
#pragma unroll
    for (int i = 0; i < 8; ++i) {
      const int row = 4 * i + g;
      dma16s((32 * pk + row) * 1280 + blk * 256 + (((((c16 >> 1) ^ (row & 7)) << 1) | (c16 & 1)) << 4), rsWs, id < 240 ? 0u : 0x80000000u, ldsb + i * 1024);
    }
    waitv<16>(); sbar();
    STAMP(2);
    {
      const int cc4 = 2 * wave + (lane >> 5), rp = lane & 31;
      const unsigned kb = KV_BASE + 4 * KV_CG;
      v4u kk[5], vv[2], w4[4];
#pragma unroll
      for (int i = 0; i < 4; ++i) w4[i] = lds128(lds + pw + 512 + (lane >> 5) * 64 + i * 16);
#pragma unroll
      for (int i = 0; i < 5; ++i) { const int row = 2 * rp + 5 + i; kk[i] = lds128(lds + kb + (row >> 3) * KV_GS + (row & 7) * 128 + ((cc4 ^ (4 * ((row >> 4) & 1))) << 4)); }
#pragma unroll
      for (int i = 0; i < 2; ++i) { const int row = 2 * rp + i; vv[i] = lds128(lds + kb + KV_V + (row >> 3) * KV_GS + (row & 7) * 128 + ((cc4 ^ (4 * ((row >> 4) & 1))) << 4)); }
      const unsigned ob = (64 * (cg0 + 4) + 8 * cc4) * 4096 + (64 * rbk + 2 * rp) * 2;
      kv_math<2>(kk, vv, w4, [&](int cp, const f32x2 (&rr)[2]) {
#pragma unroll
        for (int c = 0; c < 2; ++c) __builtin_amdgcn_raw_buffer_store_b32(pk2(rr[0][c], rr[1][c]), rsO, ob + (2 * cp + c) * 4096, 0, SC1);
      });
    }
    FT(11);
    waitv<8>();
    {
      const rsrc_t rsWo = mkrs(p.ws + WS_W2T, 8 * 48 * 5 * 1024);
      const int r0 = 4 * g + q4;
#pragma unroll
      for (int i = 0; i < 8; ++i) {
        const unsigned a = ldsb + r0 * 256 + ((i ^ (r0 & 7)) << 5) + 8 * p4;
        const v4u fr = cat(ldstr(lds + a), ldstr(lds + a + 4096));
        const int ctg = 8 * blk + i, ntt = ctg / 5, ct = ctg % 5;
        __builtin_amdgcn_raw_buffer_store_b128(fr, rsWo, vl, id < 240 ? (unsigned)(((ntt * 48 + pk) * 5 + ct) * 1024) : 0x80000000u, SC1);
      }
    }
    FT(0);
    waitv<8>();
    waitl<0>(); sbar();
    tag = mktag(epoch);
    if (threadIdx.x == 0) st32_sc1(tag, C_FKV + j * FS, rsC);
    STAMP(3);
    ld_h();
    waitv<5>(); sbar();
    if (threadIdx.x == 0) st32_sc1(tag, C_FW + j * FS, rsC);
    unsigned fk;
    ld32_sc1(fk, foKV, rsC);
    FT(3);
    waitv<1>();
    waitv<0>();
    fill_h();
    FT(2);
    poll_finish(fk, foKV, rsC, tag);
    FT(4);
    ld_u();
    const rsrc_t rsX = mkrs(p.q, S * D * 2), rsG = mkrs(p.gw, D * 2), rsGo = mkrs(p.ws + WS_G, S * 4);
    const int grow = 256 * x + 32 * (r - 24) + 8 * wave + (lane >> 3), part = lane & 7;
    v4u xx[10];
    dma16s(lane * 16, rsG, 0, pw + 1024); dma16s(1024 + lane * 16, rsG, 0, pw + 2048);
#pragma unroll
    for (int i = 0; i < 10; ++i) ldV<0>(xx[i], grow * 1280 + i * 128 + part * 16, rsX, 0);
    waitv<12>();
    fill_u();
    ld32_sc1(fw, foW, rsC);
    waitv<1>();
    {
      float a0 = 0.f, a1 = 0.f;
#pragma unroll
      for (int i = 0; i < 10; ++i) {
        asm volatile("" : "+v"(xx[i]));
        const v4u gg = lds128(lds + pw + 1024 + (i * 8 + part) * 16);
        a0 = dot2(xx[i].x, gg.x, a0); a1 = dot2(xx[i].y, gg.y, a1); a0 = dot2(xx[i].z, gg.z, a0); a1 = dot2(xx[i].w, gg.w, a1);
      }
      float a = a0 + a1;
      a += __shfl_xor(a, 1); a += __shfl_xor(a, 2); a += __shfl_xor(a, 4);
      const float gt = __builtin_amdgcn_rcpf(1.f + __builtin_amdgcn_exp2f(a * -1.44269504088896f));
      if (part == 0) __builtin_amdgcn_raw_buffer_store_b32(__builtin_bit_cast(unsigned, gt), rsGo, grow * 4, 0, SC1);
    }
    FT(5);
    waitl<0>();
    STAMP(4);
  }
  STAMP(5);
  constexpr int NZ = G1R ? 4 : 8;
  constexpr int W0 = G1R ? WPRE : 0, WG0 = G1R ? 0 : 14;
  auto wcount = [](int gap) constexpr { return gap < WG0 ? 0 : (G1R ? ((gap - WG0) * (60 - WPRE)) / 140 + 1 : (gap - WG0) / 4 + 1); };
  const unsigned foH = C_FH + (((rb >> 1) * 2 + (rb & 1)) * 24 + (lane < 24 ? lane : 0)) * FS;
  unsigned fh;
  constexpr int GPOLL = NZ * 36 - 12;
  f32x4 acc[8];
  {
    const unsigned aA = cb + q4 * CV_HC + 56 - 16 * g + 8 * p4 + 256 * half;
    const unsigned aB = cb + CV_U + 448 + 32 * c16 + 16 * g - 256 * half - 64 * (NZ - 1);
    v4u A[8], B[8];
#pragma unroll
    for (int i = 0; i < 8; ++i) {
      B[7 - i] = lds128(lds + aB + 64 * (NZ - 1) + 512 * (7 - i));
      A[i] = cat(ldstr(lds + aA + 512 * i + 8), ldstr(lds + aA + 512 * i));
    }
    SCHED();
    static_for<0, NZ>([&](auto Z) {
      constexpr int z = Z.value;
      static_for<0, 8>([&](auto I) {
        constexpr int i = I.value;
        static_for<0, 8 - i>([&](auto DD) {
          constexpr int d = 7 - i - DD.value;
          constexpr int gap = z * 36 + (i * (17 - i)) / 2 + DD.value;
          if constexpr (z == 0 && i == 0) mfma_z(acc[i + d], A[i], B[d]); else mfma_ip(acc[i + d], A[i], B[d]);
          if constexpr (d == 7 - i && z + 1 < NZ) B[7 - i] = lds128(lds + aB + 64 * (NZ - 2 - z) + 512 * (7 - i));
          if constexpr (!G1R && gap == WG0 - 2) { waitv<0>(); poll_finish(fw, foW, rsC, tag); }
          if constexpr (wcount(gap) > wcount(gap - 1)) ldW(IC(W0 + wcount(gap) - 1));
          if constexpr (gap == GPOLL) ld32_sc1(fh, foH, rsC);
          SCHED();
        });
        if constexpr (i == 7) { FT(40 + z); SCHED(); }
        if constexpr (z + 1 < NZ) { A[i] = cat(ldstr(lds + aA + 64 * (z + 1) + 512 * i + 8), ldstr(lds + aA + 64 * (z + 1) + 512 * i)); SCHED(); }
      });
    });
  }
  static_assert(W0 + wcount(NZ * 36 - 1) >= 60, "all W2 fragments issued");
  STAMP(6);
  constexpr int WAFT = 60 - (W0 + wcount(GPOLL));
  waitv<(WAFT > 0 ? WAFT : 0)>();
  FT(48);
  poll_finish(fh, foH, rsC, tag);
  FT(49);
  v4u Hf[HWM];
  const unsigned sH = (4 * rb * 48 + 12 * wave) * 1024;
  auto ldH = [&](auto N) { constexpr int n = N.value; if constexpr (n < 48) ldVo<(n & 3) * 1024, SC1>(Hf[n % HWM], vl, rsH, sH + ((n / 12) * 48 + ((n % 12) & ~3)) * 1024); };
  static_for<0, HW0>([&](auto I) { ldH(I); });
  FT(14);
  asm volatile("s_nop 7" : "+v"(acc[0]), "+v"(acc[1]), "+v"(acc[2]), "+v"(acc[3]), "+v"(acc[4]), "+v"(acc[5]), "+v"(acc[6]), "+v"(acc[7]));
  SCHED();
  int ctb = 0;
  if constexpr (G1R) {
    LAS char* ex = lds + CV_EX + cl * 8192;
    f32x4 keep[4];
#pragma unroll
    for (int i = 0; i < 4; ++i) {
      f32x4 lo = acc[i], hi = acc[4 + i];
      asm volatile("" : "+v"(lo), "+v"(hi));
      *(LAS f32x4*)(ex + half * 4096 + i * 1024 + lane * 16) = half == 0 ? hi : lo;
      keep[i] = half == 0 ? lo : hi;
    }
    waitl<0>(); sbar();
#pragma unroll
    for (int i = 0; i < 4; ++i) acc[i] = keep[i] + *(LAS f32x4*)(ex + (1 - half) * 4096 + i * 1024 + lane * 16);
    ctb = 4 * half;
  }
  FT(51);
  constexpr int NT = G1R ? 4 : 8, NYS = NT / 2;
  {
    asm volatile("" : "+v"(dv));
    const float Dc = blo(dv);
#pragma unroll
    for (int i = 0; i < NT; i += 2) {
      unsigned w0[2][2];
#pragma unroll
      for (int e = 0; e < 2; ++e) {
        const int ct = ctb + i + e;
        const f32x4 a = acc[i + e];
        const v2u uu = lds64(lds + cb + CV_U + 480 + 512 * ct + 32 * c16 + 8 * g);
        w0[e][0] = pk2(fmaf(blo(uu.x), Dc, a[0]), fmaf(bhi(uu.x), Dc, a[1]));
        w0[e][1] = pk2(fmaf(blo(uu.y), Dc, a[2]), fmaf(bhi(uu.y), Dc, a[3]));
      }
      auto s0 = __builtin_amdgcn_permlane16_swap(w0[0][0], w0[1][0], false, false);
      auto s1 = __builtin_amdgcn_permlane16_swap(w0[0][1], w0[1][1], false, false);
      const int ct = ctb + i + (g & 1);
      __builtin_amdgcn_raw_buffer_store_b128(v4u{s0[0], s1[0], s0[1], s1[1]}, rsY, ch * 4096 + 512 * ct + 32 * c16 + 8 * (g & 2), 0, SC1);
    }
  }
  FT(52);
  waitl<0>(); sbar();
  STAMP(7);
  unsigned fe[13];
  const bool bump = blockIdx.x == 0 && wave == 0;
  f32x4 ac2[2][5];
  const unsigned foY0 = C_FY + (nt * 128 + lane) * FS, foY1 = foY0 + 64 * FS;
  const unsigned foG = C_FY + ((rb >> 2) * 128 + 96 + (lane & 31)) * FS;
  unsigned fy0, fy1, fg, gtu;
  v4u yv[3]; v2u qq[5];
  const int s = 64 * rb + 16 * wave + c16;
#define SF_ 5
  constexpr int SF = SF_;
  constexpr int SP = 25;
  constexpr int SY = 40;
  static_assert(g2_issued(SP) == 48 && SY >= SP + 8, "");
  auto extras_at = [](int st) constexpr { return (st == SF ? 1 : 0) + ((st >= SP && st < SP + 8) ? 1 : 0) + (st == SY ? 4 : 0); };
  static_for<0, 48>([&](auto I) {
    constexpr int i = I.value, rt = i / 12, k = i % 12, set = rt & 1;
    constexpr int younger = [&]() constexpr {
      int y = g2_issued(i) - (i + 1);
      if (i < HW0) y += NYS;
      int si = -1; while (i >= HW0 && g2_issued(si + 1) <= i) ++si;
      for (int st = (si < 0 ? 0 : si); st < i; ++st) y += extras_at(st);
      return y; }();
    waitv<(younger < 63 ? younger : 63)>();
    static_for<0, 5>([&](auto C) { constexpr int ct = C.value;
      if constexpr (k == 0) mfma_zA(ac2[set][ct], W[k][ct], Hf[i % HWM]); else mfma_ipA(ac2[set][ct], W[k][ct], Hf[i % HWM]);
      if constexpr (rt > 0 && k >= 1 && k <= 5 && ct == 3) *(LAS f32x4*)(lds + G2_PART + (((rt - 1) * 4 + wave) * 5 + (k - 1)) * 1024 + lane * 16) = ac2[set ^ 1][k - 1];
      if constexpr (ct == 1) { static_for<g2_issued(i), g2_issued(i + 1)>([&](auto N) { ldH(N); }); }
    });
    if constexpr (i == SF) {
      waitv<g2_issued(SF + 1) - HW0>();
      if (lane == 0) st32_sc1(tag, C_FY + ((x * 32 + r) * 4 + wave) * FS, rsC);
    }
    if constexpr (i == SP) ld32_sc1(fy0, foY0, rsC);
    if constexpr (i == SP + 1) ld32_sc1(fy1, foY1, rsC);
    if constexpr (i == SP + 2) ld32_sc1(fg, foG, rsC);
    if constexpr (i >= SP + 3 && i < SP + 8) { const rsrc_t rsQ = mkrs(p.q, S * D * 2); ldV2<AUXP>(qq[i - SP - 3], (s * D + 80 * nt + 16 * (i - SP - 3) + 4 * g) * 2, rsQ, 0); }
    if constexpr (i == 34) {
      if (bump) {
#pragma unroll
        for (int e = 0; e < 13; ++e) ld32_sc1(fe[e], e < 12 ? C_FH + (e * 64 + lane) * FS : C_FKV + lane * FS, rsC);
      }
    }
    if constexpr (i == SY) {
      waitv<5>();
      poll_finish2(fy0, foY0, fy1, foY1, rsC, tag);
      poll_finish(fg, foG, rsC, tag);
      { const rsrc_t rsG = mkrs(p.ws + WS_G, S * 4); ld32_sc1(gtu, s * 4, rsG); }
#pragma unroll
      for (int u = 0; u < 3; ++u) {
        const int idx = u * 256 + wave * 64 + lane, d = idx >> 3, cch = idx & 7;
        ldV<SC1>(yv[u], idx < 640 ? (unsigned)((80 * nt + d) * 4096 + rb * 128 + cch * 16) : 0x80000000u, rsY, 0);
      }
    }
    if constexpr (i % 4 == 3) { FT(16 + i / 4); }
    SCHED();
  });
  STAMP(9);
  asm volatile("s_nop 7" : "+v"(ac2[1][0]), "+v"(ac2[1][1]), "+v"(ac2[1][2]), "+v"(ac2[1][3]), "+v"(ac2[1][4]));
#pragma unroll
  for (int ct = 0; ct < 5; ++ct) *(LAS f32x4*)(lds + G2_PART + ((3 * 4 + wave) * 5 + ct) * 1024 + lane * 16) = ac2[1][ct];
  waitv<0>();
#pragma unroll
  for (int u = 0; u < 3; ++u) {
    asm volatile("" : "+v"(yv[u]));
    const int idx = u * 256 + wave * 64 + lane, d = idx >> 3, cch = idx & 7;
    if (idx < 640) { *(LAS v2u*)(lds + G2_Y + d * 136 + cch * 16) = v2u{yv[u].x, yv[u].y}; *(LAS v2u*)(lds + G2_Y + d * 136 + cch * 16 + 8) = v2u{yv[u].z, yv[u].w}; }
  }
  asm volatile("" : "+v"(gtu));
#pragma unroll
  for (int ct = 0; ct < 5; ++ct) asm volatile("" : "+v"(qq[ct]));
  waitl<0>(); sbar();
  STAMP(10);
  const float gt = __builtin_bit_cast(float, gtu);
#pragma unroll
  for (int ct = 0; ct < 5; ++ct) {
    f32x4 b = *(LAS f32x4*)(lds + G2_PART + ((wave * 4 + 0) * 5 + ct) * 1024 + lane * 16);
#pragma unroll
    for (int w = 1; w < 4; ++w) b += *(LAS f32x4*)(lds + G2_PART + ((wave * 4 + w) * 5 + ct) * 1024 + lane * 16);
    const v2u yy = ldstr(lds + G2_Y + (16 * ct + 4 * g + q4) * 136 + (16 * wave + 4 * p4) * 2);
    const float o0 = gt * (blo(qq[ct].x) * blo(yy.x)) + (1.f - gt) * b[0];
    const float o1 = gt * (bhi(qq[ct].x) * bhi(yy.x)) + (1.f - gt) * b[1];
    const float o2 = gt * (blo(qq[ct].y) * blo(yy.y)) + (1.f - gt) * b[2];
    const float o3 = gt * (bhi(qq[ct].y) * bhi(yy.y)) + (1.f - gt) * b[3];
    *(GAS v2u*)(p.out + s * D + 80 * nt + 16 * ct + 4 * g) = v2u{pk2(o0, o1), pk2(o2, o3)};
  }
  if (bump) {
    unsigned bad = 0;
#pragma unroll
    for (int e = 0; e < 13; ++e) { asm volatile("" : "+v"(fe[e])); bad |= fe[e] ^ tag; }
    if (__builtin_amdgcn_ballot_w64(bad != 0) != 0) {
#pragma unroll 1
      for (int e = 0; e < 13; ++e) { const unsigned fo = (e < 12 ? C_FH + (e * 64 + lane) * FS : C_FKV + lane * FS); unsigned f; ld32_sc1(f, fo, rsC); waitv<0>(); poll_finish(f, fo, rsC, tag); }
    }
    if (lane == 0) st32_sc1(tag, C_EPOCH, rsC);
  }
  STAMP(11);
  STAMP_FLUSH(); FT_FLUSH();
}

__global__ __launch_bounds__(256, 1) void fused(Params p) {
  v4u pf;
  {
    unsigned long long pc; unsigned len;
    asm volatile("s_getpc_b64 %0\nLpf_here:\n\ts_mov_b32 %1, Lpf_end-Lpf_here" : "=s"(pc), "=s"(len));
    const rsrc_t rs = mkrs((const void*)pc, len);
    asm volatile("buffer_load_dwordx4 %0, %1, %2, 0 offen" : "=v"(pf) : "v"(((blockIdx.x >> 3) * 256 + threadIdx.x) * 16), "s"(rs));
    SCHED();
  }
  extern __shared__ __attribute__((aligned(16))) char smem[];
  LAS char* lds = (LAS char*)smem;
  const int lane = threadIdx.x & 63, wave = __builtin_amdgcn_readfirstlane(threadIdx.x >> 6);
  const int x = blockIdx.x & 7, r = blockIdx.x >> 3;
  if (__builtin_expect(r < 24, 1)) body<true>(p, lds, x, r, wave, lane);
  else body<false>(p, lds, x, r, wave, lane);
  asm volatile("s_waitcnt vmcnt(0)\nLpf_end:" : "+v"(pf));
}

extern "C" void kernel_launch(void* const* d_in, const int* in_sizes, int n_in, void* d_out, int out_size, void* d_ws, size_t ws_size, hipStream_t stream) {
  Params p;
  p.q = (const u16*)d_in[0]; p.k = (const u16*)d_in[1]; p.v = (const u16*)d_in[2]; p.sk = (const u16*)d_in[3];
  p.h = (const u16*)d_in[4]; p.Dv = (const u16*)d_in[5]; p.w1 = (const u16*)d_in[7]; p.w2 = (const u16*)d_in[8]; p.gw = (const u16*)d_in[9];
  p.out = (u16*)d_out; p.ws = (char*)d_ws;
  (void)hipFuncSetAttribute((const void*)fused, hipFuncAttributeMaxDynamicSharedMemorySize, LDS_BYTES);
  fused<<<256, 256, LDS_BYTES, stream>>>(p);
}
```

```cpp
#include <hip/hip_runtime.h>
#include <type_traits>
#define PROBE_A 0
#define PROBE_B 0
#define PROBE_CYC 0
#define PROBE_WG 5
#define SPIN_SLEEP 4
#define FSTR 64
#include <stdint.h>

typedef unsigned short u16;
typedef unsigned v4u __attribute__((ext_vector_type(4)));
typedef unsigned v2u __attribute__((ext_vector_type(2)));
typedef float f32x4 __attribute__((ext_vector_type(4)));
typedef float f32x2 __attribute__((ext_vector_type(2)));
typedef __bf16 bf16x8 __attribute__((ext_vector_type(8)));
typedef __bf16 bf16x2 __attribute__((ext_vector_type(2)));
typedef short v4s __attribute__((ext_vector_type(4)));
typedef short v8s __attribute__((ext_vector_type(8)));
#define GAS __attribute__((address_space(1)))
#define LAS __attribute__((address_space(3)))
#define DEV __device__ __forceinline__
typedef __amdgpu_buffer_rsrc_t rsrc_t;

constexpr int S = 2048, D = 640, F = 1536;

template <int N> DEV void waitv() { asm volatile("s_waitcnt vmcnt(%0)" ::"i"(N) : "memory"); }
template <int N> DEV void waitl() { asm volatile("s_waitcnt lgkmcnt(%0)" ::"i"(N) : "memory"); }
DEV void sbar() { asm volatile("s_barrier" ::: "memory"); }
#define SCHED() __builtin_amdgcn_sched_barrier(0)
DEV rsrc_t mkrs(const void* p, unsigned bytes) { return __builtin_amdgcn_make_buffer_rsrc((void*)p, 0, bytes, 0x00020000); }
DEV f32x4 mfma(v4u a, v4u b, f32x4 c) {
  return __builtin_amdgcn_mfma_f32_16x16x32_bf16(__builtin_bit_cast(bf16x8, a), __builtin_bit_cast(bf16x8, b), c, 0, 0, 0);
}
DEV v4u lds128(LAS const char* p) { return *(LAS const v4u*)p; }
DEV v2u lds64(LAS const char* p) { return *(LAS const v2u*)p; }
DEV v2u ldstr(LAS const char* p) { return __builtin_bit_cast(v2u, __builtin_amdgcn_ds_read_tr16_b64_v4i16((LAS v4s*)p)); }
DEV v4u cat(v2u a, v2u b) { return v4u{a.x, a.y, b.x, b.y}; }
DEV unsigned pk2(float a, float b) { bf16x2 r = __builtin_convertvector(f32x2{a, b}, bf16x2); return __builtin_bit_cast(unsigned, r); }
DEV float blo(unsigned d) { return __builtin_bit_cast(float, d << 16); }
DEV float bhi(unsigned d) { return __builtin_bit_cast(float, d & 0xffff0000u); }
DEV void setm0(unsigned v) { asm volatile("s_mov_b32 m0, %0\n\ts_nop 0" ::"s"(v)); }
template <int OFF, int NT = 0> DEV void dma16(unsigned voff, rsrc_t rs, unsigned soff) {
  if constexpr (NT) asm volatile("buffer_load_dwordx4 %0, %1, %2 offen offset:%3 nt lds" ::"v"(voff), "s"(rs), "s"(soff), "i"(OFF));
  else asm volatile("buffer_load_dwordx4 %0, %1, %2 offen offset:%3 lds" ::"v"(voff), "s"(rs), "s"(soff), "i"(OFF));
}
typedef float f32x16 __attribute__((ext_vector_type(16)));
DEV f32x16 mfma32(v4u a, v4u b, f32x16 c) {
  return __builtin_amdgcn_mfma_f32_32x32x16_bf16(__builtin_bit_cast(bf16x8, a), __builtin_bit_cast(bf16x8, b), c, 0, 0, 0);
}
template <int IMM> DEV void setm0_add(unsigned base) { asm volatile("s_add_u32 m0, %0, %1" ::"s"(base), "i"(IMM) : "scc"); }
template <int I, int N, class Fn> DEV void static_for(Fn&& f) { if constexpr (I < N) { f(std::integral_constant<int, I>{}); static_for<I + 1, N>(f); } }
DEV void mfma_ip(f32x4& c, const v4u& a, const v4u& b) { asm volatile("v_mfma_f32_16x16x32_bf16 %0, %1, %2, %0" : "+v"(c) : "v"(a), "v"(b)); }
DEV void mfma_ipA(f32x4& c, const v4u& a, const v4u& b) { asm volatile("v_mfma_f32_16x16x32_bf16 %0, %1, %2, %0" : "+v"(c) : "a"(a), "v"(b)); }
DEV void mfma_z(f32x4& c, const v4u& a, const v4u& b) { asm volatile("v_mfma_f32_16x16x32_bf16 %0, %1, %2, 0" : "=&v"(c) : "v"(a), "v"(b)); }
DEV void mfma_zA(f32x4& c, const v4u& a, const v4u& b) { asm volatile("v_mfma_f32_16x16x32_bf16 %0, %1, %2, 0" : "=&v"(c) : "a"(a), "v"(b)); }
template <int AUX> DEV void ldA(v4u& d, unsigned voff, rsrc_t rs, unsigned soff) {
  if constexpr (AUX) asm volatile("buffer_load_dwordx4 %0, %1, %2, %3 offen sc1" : "=a"(d) : "v"(voff), "s"(rs), "s"(soff));
  else asm volatile("buffer_load_dwordx4 %0, %1, %2, %3 offen" : "=a"(d) : "v"(voff), "s"(rs), "s"(soff));
}
template <int AUX> DEV void ldV(v4u& d, unsigned voff, rsrc_t rs, unsigned soff) {
  if constexpr (AUX == 2) asm volatile("buffer_load_dwordx4 %0, %1, %2, %3 offen nt" : "=v"(d) : "v"(voff), "s"(rs), "s"(soff));
  else if constexpr (AUX == 16) asm volatile("buffer_load_dwordx4 %0, %1, %2, %3 offen sc1" : "=v"(d) : "v"(voff), "s"(rs), "s"(soff));
  else asm volatile("buffer_load_dwordx4 %0, %1, %2, %3 offen" : "=v"(d) : "v"(voff), "s"(rs), "s"(soff));
}
DEV void nop8() { asm volatile("s_nop 7"); }

#define PROBE_DECL unsigned long long pr_a = 0, pr_d = 0
#define PROBE_NOW() (PROBE_CYC ? __builtin_amdgcn_s_memtime() : __builtin_amdgcn_s_memrealtime())
#define PROBE(i) do { if constexpr ((i) == PROBE_A) pr_a = PROBE_NOW(); if constexpr ((i) == PROBE_B) { pr_d = (PROBE_NOW() - pr_a) * 8; if (PROBE_CYC) pr_d /= 24; pr_d += 200; } } while (0)
#define PROBE_RT(t) do { if ((t) + 100 == PROBE_A) pr_a = PROBE_NOW(); if ((t) + 100 == PROBE_B) { pr_d = (PROBE_NOW() - pr_a) * 8; if (PROBE_CYC) pr_d /= 24; pr_d += 200; } } while (0)
#define PROBE_END() do { if (blockIdx.x == PROBE_WG) { const unsigned long long e0 = __builtin_amdgcn_s_memrealtime(); while (__builtin_amdgcn_s_memrealtime() - e0 < pr_d) __builtin_amdgcn_s_sleep(1); } } while (0)

constexpr size_t WS_CTRL = 0;
constexpr size_t WS_KVT = 262144;
constexpr size_t WS_YT = WS_KVT + (size_t)D * S * 2;
constexpr size_t WS_W2T = WS_YT + (size_t)D * S * 2;
constexpr size_t WS_HF = WS_W2T + (size_t)8 * 48 * 5 * 1024;
constexpr size_t WS_G = WS_HF + (size_t)128 * 48 * 1024;
constexpr size_t WS_END = WS_G + S * 4;
constexpr unsigned C_EPOCH = 0, C_TMO = 256;
constexpr unsigned FS = FSTR;
constexpr unsigned C_FW = 4096;
constexpr unsigned C_FKV = C_FW + 64 * FS;
constexpr unsigned C_FH = C_FKV + 64 * FS;
constexpr unsigned C_FY = C_FH + 768 * FS;
constexpr unsigned C_BYTES = 262144;
static_assert(C_FY + 1024 * FS <= C_BYTES, "");

#define NSTAMP 16
#define STAMP_OFF (size_t)(32u << 20)
#define STAMP_DECL PROBE_DECL
#define STAMP(i) PROBE(i)
#define STAMP_FLUSH() PROBE_END()
#define FT_DECL
#define FT(i)
#define FT_FLUSH()

struct Params {
  const u16 *q, *k, *v, *sk, *h, *Dv, *w1, *w2, *gw;
  u16* out;
  char* ws;
};

constexpr int LDS_BYTES = 160 * 1024;
constexpr int SC1 = 16;
#define NT_X 0
#define NT_W1 0
#define NT_P 0
constexpr int AUXP = NT_P ? 2 : 0;
#define IC(n) std::integral_constant<int, (n)>{}

DEV void ld32_sc1(unsigned& v, unsigned voff, rsrc_t rs) { asm volatile("buffer_load_dword %0, %1, %2, 0 offen sc1" : "=v"(v) : "v"(voff), "s"(rs)); }
DEV void st32_sc1(unsigned v, unsigned voff, rsrc_t rs) { asm volatile("buffer_store_dword %0, %1, %2, 0 offen sc1" ::"v"(v), "v"(voff), "s"(rs)); }
DEV void poll_finish(unsigned v, unsigned voff, rsrc_t rsC, unsigned tag) {
  asm volatile("" : "+v"(v));
  if (__builtin_amdgcn_ballot_w64(v != tag) == 0) return;
#pragma unroll 1
  for (int spins = 0; spins < (1 << 15); ++spins) {
    __builtin_amdgcn_s_sleep(SPIN_SLEEP);
    ld32_sc1(v, voff, rsC);
    asm volatile("s_waitcnt vmcnt(0)" : "+v"(v)::"memory");
    if (__builtin_amdgcn_ballot_w64(v != tag) == 0) return;
  }
  st32_sc1(1u, C_TMO, rsC);
}

DEV void poll_finish2(unsigned a, unsigned oa, unsigned b, unsigned ob, rsrc_t rsC, unsigned tag) {
  asm volatile("" : "+v"(a), "+v"(b));
  if (__builtin_amdgcn_ballot_w64(a != tag || b != tag) == 0) return;
#pragma unroll 1
  for (int spins = 0; spins < (1 << 15); ++spins) {
    __builtin_amdgcn_s_sleep(SPIN_SLEEP);
    ld32_sc1(a, oa, rsC); ld32_sc1(b, ob, rsC);
    asm volatile("s_waitcnt vmcnt(0)" : "+v"(a), "+v"(b)::"memory");
    if (__builtin_amdgcn_ballot_w64(a != tag || b != tag) == 0) return;
  }
  st32_sc1(1u, C_TMO, rsC);
}

DEV void mfma32_ip(f32x16& c, const v4u& a, const v4u& b) { asm volatile("v_mfma_f32_32x32x16_bf16 %0, %1, %2, %0" : "+v"(c) : "v"(a), "v"(b)); }
DEV void mfma32_z(f32x16& c, const v4u& a, const v4u& b) { asm volatile("v_mfma_f32_32x32x16_bf16 %0, %1, %2, 0" : "=&v"(c) : "v"(a), "v"(b)); }

constexpr int KV_BASE = 32768, KV_GS = 1152, KV_V = 9 * KV_GS, KV_CG = 17 * KV_GS;
constexpr int CV_BASE = 32768;
constexpr int CV_HC = 4160;
constexpr int CV_U = 4 * CV_HC;
constexpr int CV_CH = CV_U + 4608;
constexpr int CV_EX = CV_BASE + 4 * CV_CH;
constexpr int G2_PART = 0;
constexpr int G2_Y = 81920;
constexpr int HW = 24;

template <int AUX> DEV void ldV2(v2u& d, unsigned voff, rsrc_t rs, unsigned soff) {
  if constexpr (AUX == 2) asm volatile("buffer_load_dwordx2 %0, %1, %2, %3 offen nt" : "=v"(d) : "v"(voff), "s"(rs), "s"(soff));
  else if constexpr (AUX == 16) asm volatile("buffer_load_dwordx2 %0, %1, %2, %3 offen sc1" : "=v"(d) : "v"(voff), "s"(rs), "s"(soff));
  else asm volatile("buffer_load_dwordx2 %0, %1, %2, %3 offen" : "=v"(d) : "v"(voff), "s"(rs), "s"(soff));
}
DEV void dma16s(unsigned voff, rsrc_t rs, unsigned soff, unsigned m0v) {
  if constexpr (NT_P) asm volatile("s_mov_b32 m0, %3\n\ts_nop 0\n\tbuffer_load_dwordx4 %0, %1, %2 offen nt lds" ::"v"(voff), "s"(rs), "s"(soff), "s"(m0v));
  else asm volatile("s_mov_b32 m0, %3\n\ts_nop 0\n\tbuffer_load_dwordx4 %0, %1, %2 offen lds" ::"v"(voff), "s"(rs), "s"(soff), "s"(m0v));
}
DEV float dot2(unsigned a, unsigned b, float c) { return __builtin_amdgcn_fdot2_f32_bf16(__builtin_bit_cast(bf16x2, a), __builtin_bit_cast(bf16x2, b), c, false); }
DEV unsigned mktag(unsigned& epoch) {
  asm volatile("" : "+v"(epoch));
  return __builtin_amdgcn_readfirstlane(epoch * 0x9E3779B1u + 0x7F4A7C15u);
}

template <int NR, class ST> DEV void kv_math(const v4u (&kk)[NR + 3], const v4u (&vv)[NR], const v4u (&ww)[4], ST&& st) {
#pragma unroll
  for (int cp = 0; cp < 4; ++cp) {
    f32x2 w[4];
    w[0] = f32x2{blo(ww[cp].x), blo(ww[cp].z)}; w[1] = f32x2{bhi(ww[cp].x), bhi(ww[cp].z)};
    w[2] = f32x2{blo(ww[cp].y), blo(ww[cp].w)}; w[3] = f32x2{bhi(ww[cp].y), bhi(ww[cp].w)};
    f32x2 kf[NR + 3];
#pragma unroll
    for (int i = 0; i < NR + 3; ++i) kf[i] = f32x2{blo(kk[i][cp]), bhi(kk[i][cp])};
    f32x2 r[NR];
#pragma unroll
    for (int i = 0; i < NR; ++i) {
      f32x2 a = kf[i + 3] * w[0];
      a = __builtin_elementwise_fma(kf[i + 2], w[1], a);
      a = __builtin_elementwise_fma(kf[i + 1], w[2], a);
      a = __builtin_elementwise_fma(kf[i], w[3], a);
      r[i] = a * f32x2{blo(vv[i][cp]), bhi(vv[i][cp])};
    }
    st(cp, r);
  }
}

#define Q0 7
#define ROT 0
#define PREP_FIRST 0
#define HPUB 90
#define WPRE_ 16
constexpr int WPRE = WPRE_;
constexpr int HW0 = 16, HWM = 24;
constexpr int PW_BASE = 131072, PW_SZ = 3072;
constexpr int g2_issued(int i) { int r = HW0; for (int s = 0; s < i; ++s) r += (r - s < HWM) ? ((r + 1 - s < HWM) ? 2 : 1) : 0; return r < 48 ? r : 48; }

template <int OFF, int AUX> DEV void ldVo(v4u& d, unsigned voff, rsrc_t rs, unsigned soff) {
  static_assert(AUX == 16, "");
  asm volatile("buffer_load_dwordx4 %0, %1, %2, %3 offen offset:%4 sc1" : "=v"(d) : "v"(voff), "s"(rs), "s"(soff), "i"(OFF));
}
template <int OFF> DEV void ldAo(v4u& d, unsigned voff, rsrc_t rs, unsigned soff) {
  asm volatile("buffer_load_dwordx4 %0, %1, %2, %3 offen offset:%4 sc1" : "=a"(d) : "v"(voff), "s"(rs), "s"(soff), "i"(OFF));
}

template <bool G1R>
DEV void body(const Params& p, LAS char* lds, const int x, const int r, const int wave, const int lane) {
  const int g = lane >> 4, q4 = (lane >> 2) & 3, p4 = lane & 3, c16 = lane & 15;
  STAMP_DECL; FT_DECL;
  STAMP(0); FT(13);
  const rsrc_t rsC = mkrs(p.ws + WS_CTRL, C_BYTES);
  unsigned epoch, tag;
  ld32_sc1(epoch, C_EPOCH, rsC);
  const int cl = G1R ? (wave >> 1) : wave, half = G1R ? (wave & 1) : 0;
  const int ch = (G1R ? 80 * x + 2 * r : 80 * x + 48 + 4 * (r - 24)) + cl;
  const unsigned cb = CV_BASE + cl * CV_CH;
  constexpr int NCHK = G1R ? 2 : 4;
  unsigned dv;
  { const rsrc_t rsD = mkrs(p.Dv, D * 2); asm volatile("buffer_load_ushort %0, %1, %2, 0 offen" : "=v"(dv) : "v"(ch * 2), "s"(rsD)); }
  const rsrc_t rsHh = mkrs(p.h, D * S * 2), rsU = mkrs(p.ws + WS_KVT, D * S * 2), rsY = mkrs(p.ws + WS_YT, D * S * 2);
  const rsrc_t rsW2 = mkrs(p.ws + WS_W2T, 8 * 48 * 5 * 1024), rsH = mkrs(p.ws + WS_HF, 128 * 48 * 1024);
#define MAP42 1
  const int rb = MAP42 ? 8 * (x >> 1) + (r & 7) : 4 * x + (r & 3), nt = MAP42 ? 4 * (x & 1) + (r >> 3) : r >> 2;
  const unsigned vl = lane * 16;
  const unsigned sW = (nt * 48 + 12 * wave) * 5 * 1024;
  const unsigned foW = C_FW + lane * FS, foKV = C_FKV + lane * FS;
  v4u W[12][5];
  auto ldW = [&](auto I) { constexpr int i = I.value; if constexpr (i >= 0 && i < 60) ldAo<(i & 3) * 1024>(W[i / 5][i % 5], vl, rsW2, sW + (i & ~3) * 1024); };
  v4u hc[NCHK], uc[NCHK]; v2u hp0;
  auto ld_h = [&]() {
    ldV2<AUXP>(hp0, half > 0 ? (unsigned)(ch * 4096 + half * NCHK * 1024 - 8) : 0x80000000u, rsHh, 0);
#pragma unroll
    for (int i = 0; i < NCHK; ++i) ldV<AUXP>(hc[i], ch * 4096 + ((half * NCHK + i) * 64 + lane) * 16, rsHh, 0);
  };
  auto ld_u = [&]() {
#pragma unroll
    for (int i = 0; i < NCHK; ++i) ldV<SC1>(uc[i], ch * 4096 + ((half * NCHK + i) * 64 + lane) * 16, rsU, 0);
  };
  auto fill_h = [&]() {
    asm volatile("" : "+v"(hp0));
#pragma unroll
    for (int i = 0; i < NCHK; ++i) asm volatile("" : "+v"(hc[i]));
    unsigned e0 = hp0.x, e1 = hp0.y;
#pragma unroll
    for (int i = 0; i < NCHK; ++i) {
      const int n = (half * NCHK + i) * 64 + lane;
      const v4u cur = hc[i];
      const v2u prv = v2u{(unsigned)__builtin_amdgcn_update_dpp((int)e0, (int)cur.z, 0x138, 0xf, 0xf, false), (unsigned)__builtin_amdgcn_update_dpp((int)e1, (int)cur.w, 0x138, 0xf, 0xf, false)};
      e0 = __builtin_amdgcn_readlane(cur.z, 63); e1 = __builtin_amdgcn_readlane(cur.w, 63);
      const unsigned om2 = __builtin_amdgcn_alignbit(prv.y, prv.x, 16), om1 = __builtin_amdgcn_alignbit(cur.x, prv.y, 16);
      const unsigned o0 = __builtin_amdgcn_alignbit(cur.y, cur.x, 16), o1 = __builtin_amdgcn_alignbit(cur.z, cur.y, 16), o2 = __builtin_amdgcn_alignbit(cur.w, cur.z, 16);
      LAS char* dst = lds + cb + (n + 2) * 16;
      *(LAS v4u*)(dst) = cur;
      *(LAS v4u*)(dst + CV_HC) = v4u{om1, o0, o1, o2};
      *(LAS v4u*)(dst + 2 * CV_HC) = v4u{prv.y, cur.x, cur.y, cur.z};
      *(LAS v4u*)(dst + 3 * CV_HC) = v4u{om2, om1, o0, o1};
    }
    if (half == 0) {
      if (lane < 8) *(LAS v4u*)(lds + cb + (lane >> 1) * CV_HC + (lane & 1) * 16) = v4u{0, 0, 0, 0};
      if (lane >= 32 && lane < 62) *(LAS v4u*)(lds + cb + CV_U + (lane - 32) * 16) = v4u{0, 0, 0, 0};
    }
  };
  auto fill_u = [&]() {
#pragma unroll
    for (int i = 0; i < NCHK; ++i) {
      asm volatile("" : "+v"(uc[i]));
      *(LAS v4u*)(lds + cb + CV_U + 480 + ((half * NCHK + i) * 64 + lane) * 16) = uc[i];
    }
  };
  unsigned fw;

  if constexpr (G1R) {
    const int sb = 4 * (x >> 1) + (r & 3), fs = 6 * (x & 1) + (r >> 2);
    f32x16 acc1[2][2];
    const rsrc_t rsX = mkrs(p.q, S * D * 2), rsW = mkrs(p.w1, D * F * 2);
    unsigned vX[4], vW[4];
#pragma unroll
    for (int ii = 0; ii < 4; ++ii) {
      vX[ii] = (32 * wave + 8 * ii + (lane >> 3)) * 1280 + (((lane & 7) ^ (4 * (ii & 1) + g)) << 4) - 1024 * ii;
      vW[ii] = (16 * wave + 4 * ii + g) * 3072 + (((((c16 >> 1) ^ (g << 1)) << 1) | (c16 & 1)) << 4) - 1024 * ii;
    }
    const unsigned sX0 = sb * 128 * 1280, sW0 = fs * 256;
    const unsigned mX = wave * 4096, mW = 16384 + wave * 4096;
    static_for<0, 7>([&](auto Q) { constexpr int q = Q.value, t = q >> 1;
      setm0_add<t * 32768>((q & 1) ? mW : mX); asm volatile("s_nop 0");
      static_for<0, 4>([&](auto I) { constexpr int i = I.value;
        if constexpr (q & 1) dma16<1024 * i, NT_W1>(vW[i], rsW, sW0 + t * (64 * 3072)); else dma16<1024 * i, NT_X>(vX[i], rsX, sX0 + t * 128); });
    });
    FT(11);
    const int ws_ = wave & 1, wf = wave >> 1, h = lane >> 5, r32 = lane & 31;
    unsigned aX[4], aW[2];
#pragma unroll
    for (int ks = 0; ks < 4; ++ks) aX[ks] = (64 * ws_ + r32) * 128 + (((2 * ks + h) ^ ((r32 >> 1) & 7)) << 4);
#pragma unroll
    for (int ft = 0; ft < 2; ++ft) aW[ft] = 16384 + (8 * h + q4) * 256 + (((2 * (2 * wf + ft) + (g & 1)) ^ (q4 << 1)) << 5) + 8 * p4;
#pragma unroll
    for (int a = 0; a < 2; ++a)
#pragma unroll
      for (int b = 0; b < 2; ++b)
#pragma unroll
        for (int e = 0; e < 16; ++e) acc1[a][b][e] = 0.f;
    asm volatile("s_nop 1" : "+v"(acc1[0][0]), "+v"(acc1[0][1]), "+v"(acc1[1][0]), "+v"(acc1[1][1]));
    v4u A[2][2][2], B[2][2][2];
    v2u At;
    unsigned cX[2], cW[2];
    auto rd = [&](auto HF, auto SET, auto N) {
      constexpr int hf = HF.value, set = SET.value, n = N.value;
      constexpr int kk = n / 6, rr = n % 6, ks = 2 * hf + kk;
      if constexpr (rr == 0) B[set][kk][0] = lds128(lds + cX[kk]);
      else if constexpr (rr == 3) B[set][kk][1] = lds128(lds + cX[kk] + 32 * 128);
      else { constexpr int ft = rr / 3, jj = (rr % 3) - 1;
        v2u xx = ldstr(lds + cW[ft] + ks * 4096 + jj * 1024);
        if constexpr (jj == 0) At = xx; else A[set][kk][ft] = cat(At, xx); }
    };
    auto set_addr = [&](int hf, unsigned bo) { cX[0] = aX[2 * hf] + bo; cX[1] = aX[2 * hf + 1] + bo; cW[0] = aW[0] + bo; cW[1] = aW[1] + bo; };
    unsigned fk;
    waitv<20>(); sbar();
    STAMP(1);
    set_addr(0, 0);
    static_for<0, 12>([&](auto N) { rd(IC(0), IC(0), N); });
    SCHED();
    unsigned bo = 0;
    unsigned bW = 3 * 32768, bX = 4 * 32768;
    unsigned sWn = sW0 + 3 * (64 * 3072), sXn = sX0 + 4 * 128;
    auto stage = [&](auto DMA, int t) {
      constexpr bool dma = DMA.value;
      const unsigned nbo = bo == 4 * 32768 ? 0 : bo + 32768;
      set_addr(1, bo);
      static_for<0, 8>([&](auto M) {
        constexpr int m = M.value, kk = m >> 2, ft = (m >> 1) & 1, st = m & 1;
        mfma32_ip(acc1[ft][st], A[0][kk][ft], B[0][kk][st]);
        if constexpr (m < 6) { rd(IC(1), IC(1), IC(2 * m)); rd(IC(1), IC(1), IC(2 * m + 1)); }
        if constexpr (dma && m == 0) asm volatile("s_add_u32 m0, %0, %1" ::"s"(mW), "s"(bW) : "scc");
        if constexpr (dma && (m & 1)) dma16<1024 * (m >> 1), NT_W1>(vW[m >> 1], rsW, sWn);
        SCHED();
      });
      if constexpr (dma) { waitv<16>(); sbar(); }
      else { if (t == 7) waitv<8>(); else waitv<0>(); sbar(); }
      SCHED();
      set_addr(0, nbo);
      const unsigned sx = (dma && t < 6) ? sXn : 0x80000000u;
      static_for<0, 8>([&](auto M) {
        constexpr int m = M.value, kk = m >> 2, ft = (m >> 1) & 1, st = m & 1;
        mfma32_ip(acc1[ft][st], A[1][kk][ft], B[1][kk][st]);
        if constexpr (m < 6) { rd(IC(0), IC(0), IC(2 * m)); rd(IC(0), IC(0), IC(2 * m + 1)); }
        if constexpr (dma && m == 0) asm volatile("s_add_u32 m0, %0, %1" ::"s"(mX), "s"(bX) : "scc");
        if constexpr (dma && (m & 1)) dma16<1024 * (m >> 1), NT_X>(vX[m >> 1], rsX, sx);
        SCHED();
      });
      bo = nbo;
      bW = bW == 4 * 32768 ? 0 : bW + 32768; bX = bX == 4 * 32768 ? 0 : bX + 32768;
      sWn += 64 * 3072; sXn += 128;
      PROBE_RT(t);
    };
#pragma unroll 1
    for (int t = 0; t < 7; ++t) stage(std::true_type{}, t);
#pragma unroll 1
    for (int t = 7; t < 10; ++t) stage(std::false_type{}, t);
    asm volatile("s_nop 15" : "+v"(acc1[0][0]), "+v"(acc1[0][1]), "+v"(acc1[1][0]), "+v"(acc1[1][1]));
    sbar();
    STAMP(2);
    tag = mktag(epoch);
    ld_h();
    const unsigned vo = (g & 1) * (48 * 1024) + (c16 + 16 * h) * 16;
    static_for<0, 4>([&](auto Q) {
      constexpr int qd = Q.value, st = qd >> 1, ft = qd & 1;
      const f32x16 a = acc1[ft][st];
      unsigned o[8];
      static_for<0, 2>([&](auto G) {
        constexpr int gq = G.value;
        static_for<0, 4>([&](auto E) {
          constexpr int e = 8 * gq + 2 * E.value;
          const float r0 = a[e] * __builtin_amdgcn_rcpf(1.f + __builtin_amdgcn_exp2f(a[e] * -1.44269504088896f));
          const float r1 = a[e + 1] * __builtin_amdgcn_rcpf(1.f + __builtin_amdgcn_exp2f(a[e + 1] * -1.44269504088896f));
          o[e >> 1] = pk2(r0, r1);
        });
        SCHED();
      });
      const unsigned so = ((8 * sb + 4 * ws_ + 2 * st) * 48 + 4 * fs + 2 * wf + ft) * 1024;
      __builtin_amdgcn_raw_buffer_store_b128(v4u{o[0], o[1], o[4], o[5]}, rsH, vo, so, SC1);
      __builtin_amdgcn_raw_buffer_store_b128(v4u{o[2], o[3], o[6], o[7]}, rsH, vo + 512, so, SC1);
      SCHED();
      FT(32 + qd); SCHED();
    });
    ld32_sc1(fk, foKV, rsC); ld32_sc1(fw, foW, rsC);
    waitv<10>();
    fill_h();
    FT(54);
    waitv<0>();
    if (lane == 0) st32_sc1(tag, C_FH + ((sb * 2 + ws_) * 24 + fs * 2 + wf) * FS, rsC);
    FT(55);
    poll_finish(fk, foKV, rsC, tag);
    ld_u();
    FT(36);
    poll_finish(fw, foW, rsC, tag);
    FT(37);
    static_for<0, WPRE>([&](auto I) { ldW(I); });
    STAMP(3);
    waitv<WPRE>();
    fill_u();
    waitl<0>(); sbar();
    STAMP(4);
  } else {
    const int j = (r - 24) * 8 + x, rbk = j >> 1, cg0 = 5 * (j & 1);
    const rsrc_t rsK = mkrs(p.k, S * D * 2), rsV = mkrs(p.v, S * D * 2), rsS = mkrs(p.sk, D * 8), rsO = mkrs(p.ws + WS_KVT, D * S * 2);
    const unsigned vr = (lane >> 3) * 1280 + ((lane & 7) << 4), vr4 = vr ^ 64;
    auto dmaK = [&](int cgl, int G) {
      const int row = 64 * rbk - 8 + 8 * G;
      dma16s(((G >> 1) & 1) ? vr4 : vr, rsK, row >= 0 ? (unsigned)(row * 1280 + (cg0 + cgl) * 128) : 0x80000000u, KV_BASE + cgl * KV_CG + G * KV_GS);
    };
    auto dmaV = [&](int cgl, int G) {
      dma16s(((G >> 1) & 1) ? vr4 : vr, rsV, (64 * rbk + 8 * G) * 1280 + (cg0 + cgl) * 128, KV_BASE + cgl * KV_CG + KV_V + G * KV_GS);
    };
#pragma unroll
    for (int G = 0; G < 9; ++G) dmaK(wave, G);
#pragma unroll
    for (int G = 0; G < 8; ++G) dmaV(wave, G);
    const unsigned pw = PW_BASE + wave * PW_SZ;
    dma16s(lane < 32 ? (unsigned)((cg0 + wave) * 512 + lane * 16) : (lane < 40 ? (unsigned)((64 * (cg0 + 4) + 16 * wave) * 8 + (lane - 32) * 16) : 0x80000000u), rsS, 0, pw);
    dmaK(4, wave); dmaK(4, wave + 4); dmaV(4, wave); dmaV(4, wave + 4);
    if (wave == 0) dmaK(4, 8);
    const int cc = lane >> 3, rg = lane & 7;
    FT(10);
    waitv<4>();
    STAMP(1);
    {
      const unsigned kb = KV_BASE + wave * KV_CG;
      const unsigned a0 = kb + rg * KV_GS + ((cc ^ (4 * ((rg >> 1) & 1))) << 4), a1 = kb + (rg + 1) * KV_GS + ((cc ^ (4 * (((rg + 1) >> 1) & 1))) << 4);
      v4u kk[11], vv[8], ww[4];
#pragma unroll
      for (int i = 0; i < 4; ++i) ww[i] = lds128(lds + pw + cc * 64 + i * 16);
#pragma unroll
      for (int i = 0; i < 11; ++i) kk[i] = i < 3 ? lds128(lds + a0 + (5 + i) * 128) : lds128(lds + a1 + (i - 3) * 128);
#pragma unroll
      for (int i = 0; i < 8; ++i) vv[i] = lds128(lds + a0 + KV_V + i * 128);
      const unsigned ob = (64 * (cg0 + wave) + 8 * cc) * 4096 + (64 * rbk + 8 * rg) * 2;
      kv_math<8>(kk, vv, ww, [&](int cp, const f32x2 (&rr)[8]) {
#pragma unroll
        for (int c = 0; c < 2; ++c) {
          v4u o{pk2(rr[0][c], rr[1][c]), pk2(rr[2][c], rr[3][c]), pk2(rr[4][c], rr[5][c]), pk2(rr[6][c], rr[7][c])};
          __builtin_amdgcn_raw_buffer_store_b128(o, rsO, ob + (2 * cp + c) * 4096, 0, SC1);
        }
      });
    }
    const int id = 4 * j + wave, pk = id / 5, blk = id % 5;
    const rsrc_t rsWs = mkrs(p.w2, F * D * 2);
    const unsigned ldsb = wave * 8192;
#pragma unroll
    for (int i = 0; i < 8; ++i) {
      const int row = 4 * i + g;
      dma16s((32 * pk + row) * 1280 + blk * 256 + (((((c16 >> 1) ^ (row & 7)) << 1) | (c16 & 1)) << 4), rsWs, id < 240 ? 0u : 0x80000000u, ldsb + i * 1024);
    }
    waitv<16>(); sbar();
    STAMP(2);
    {
      const int cc4 = 2 * wave + (lane >> 5), rp = lane & 31;
      const unsigned kb = KV_BASE + 4 * KV_CG;
      v4u kk[5], vv[2], w4[4];
#pragma unroll
      for (int i = 0; i < 4; ++i) w4[i] = lds128(lds + pw + 512 + (lane >> 5) * 64 + i * 16);
#pragma unroll
      for (int i = 0; i < 5; ++i) { const int row = 2 * rp + 5 + i; kk[i] = lds128(lds + kb + (row >> 3) * KV_GS + (row & 7) * 128 + ((cc4 ^ (4 * ((row >> 4) & 1))) << 4)); }
#pragma unroll
      for (int i = 0; i < 2; ++i) { const int row = 2 * rp + i; vv[i] = lds128(lds + kb + KV_V + (row >> 3) * KV_GS + (row & 7) * 128 + ((cc4 ^ (4 * ((row >> 4) & 1))) << 4)); }
      const unsigned ob = (64 * (cg0 + 4) + 8 * cc4) * 4096 + (64 * rbk + 2 * rp) * 2;
      kv_math<2>(kk, vv, w4, [&](int cp, const f32x2 (&rr)[2]) {
#pragma unroll
        for (int c = 0; c < 2; ++c) __builtin_amdgcn_raw_buffer_store_b32(pk2(rr[0][c], rr[1][c]), rsO, ob + (2 * cp + c) * 4096, 0, SC1);
      });
    }
    FT(11);
    waitv<8>();
    {
      const rsrc_t rsWo = mkrs(p.ws + WS_W2T, 8 * 48 * 5 * 1024);
      const int r0 = 4 * g + q4;
#pragma unroll
      for (int i = 0; i < 8; ++i) {
        const unsigned a = ldsb + r0 * 256 + ((i ^ (r0 & 7)) << 5) + 8 * p4;
        const v4u fr = cat(ldstr(lds + a), ldstr(lds + a + 4096));
        const int ctg = 8 * blk + i, ntt = ctg / 5, ct = ctg % 5;
        __builtin_amdgcn_raw_buffer_store_b128(fr, rsWo, vl, id < 240 ? (unsigned)(((ntt * 48 + pk) * 5 + ct) * 1024) : 0x80000000u, SC1);
      }
    }
    FT(0);
    waitv<8>();
    waitl<0>(); sbar();
    tag = mktag(epoch);
    if (threadIdx.x == 0) st32_sc1(tag, C_FKV + j * FS, rsC);
    STAMP(3);
    ld_h();
    waitv<5>(); sbar();
    if (threadIdx.x == 0) st32_sc1(tag, C_FW + j * FS, rsC);
    unsigned fk;
    ld32_sc1(fk, foKV, rsC);
    FT(3);
    waitv<1>();
    waitv<0>();
    fill_h();
    FT(2);
    poll_finish(fk, foKV, rsC, tag);
    FT(4);
    ld_u();
    const rsrc_t rsX = mkrs(p.q, S * D * 2), rsG = mkrs(p.gw, D * 2), rsGo = mkrs(p.ws + WS_G, S * 4);
    const int grow = 256 * x + 32 * (r - 24) + 8 * wave + (lane >> 3), part = lane & 7;
    v4u xx[10];
    dma16s(lane * 16, rsG, 0, pw + 1024); dma16s(1024 + lane * 16, rsG, 0, pw + 2048);
#pragma unroll
    for (int i = 0; i < 10; ++i) ldV<0>(xx[i], grow * 1280 + i * 128 + part * 16, rsX, 0);
    waitv<12>();
    fill_u();
    ld32_sc1(fw, foW, rsC);
    waitv<1>();
    {
      float a0 = 0.f, a1 = 0.f;
#pragma unroll
      for (int i = 0; i < 10; ++i) {
        asm volatile("" : "+v"(xx[i]));
        const v4u gg = lds128(lds + pw + 1024 + (i * 8 + part) * 16);
        a0 = dot2(xx[i].x, gg.x, a0); a1 = dot2(xx[i].y, gg.y, a1); a0 = dot2(xx[i].z, gg.z, a0); a1 = dot2(xx[i].w, gg.w, a1);
      }
      float a = a0 + a1;
      a += __shfl_xor(a, 1); a += __shfl_xor(a, 2); a += __shfl_xor(a, 4);
      const float gt = __builtin_amdgcn_rcpf(1.f + __builtin_amdgcn_exp2f(a * -1.44269504088896f));
      if (part == 0) __builtin_amdgcn_raw_buffer_store_b32(__builtin_bit_cast(unsigned, gt), rsGo, grow * 4, 0, SC1);
    }
    FT(5);
    waitl<0>();
    STAMP(4);
  }
  STAMP(5);
  constexpr int NZ = G1R ? 4 : 8;
  constexpr int W0 = G1R ? WPRE : 0, WG0 = G1R ? 0 : 14;
  auto wcount = [](int gap) constexpr { return gap < WG0 ? 0 : (G1R ? ((gap - WG0) * (60 - WPRE)) / 140 + 1 : (gap - WG0) / 4 + 1); };
  const unsigned foH = C_FH + (((rb >> 1) * 2 + (rb & 1)) * 24 + (lane < 24 ? lane : 0)) * FS;
  unsigned fh;
  constexpr int GPOLL = NZ * 36 - 12;
  f32x4 acc[8];
  {
    const unsigned aA = cb + q4 * CV_HC + 56 - 16 * g + 8 * p4 + 256 * half;
    const unsigned aB = cb + CV_U + 448 + 32 * c16 + 16 * g - 256 * half - 64 * (NZ - 1);
    v4u A[8], B[8];
#pragma unroll
    for (int i = 0; i < 8; ++i) {
      B[7 - i] = lds128(lds + aB + 64 * (NZ - 1) + 512 * (7 - i));
      A[i] = cat(ldstr(lds + aA + 512 * i + 8), ldstr(lds + aA + 512 * i));
    }
    SCHED();
    static_for<0, NZ>([&](auto Z) {
      constexpr int z = Z.value;
      static_for<0, 8>([&](auto I) {
        constexpr int i = I.value;
        static_for<0, 8 - i>([&](auto DD) {
          constexpr int d = 7 - i - DD.value;
          constexpr int gap = z * 36 + (i * (17 - i)) / 2 + DD.value;
          if constexpr (z == 0 && i == 0) mfma_z(acc[i + d], A[i], B[d]); else mfma_ip(acc[i + d], A[i], B[d]);
          if constexpr (d == 7 - i && z + 1 < NZ) B[7 - i] = lds128(lds + aB + 64 * (NZ - 2 - z) + 512 * (7 - i));
          if constexpr (!G1R && gap == WG0 - 2) { waitv<0>(); poll_finish(fw, foW, rsC, tag); }
          if constexpr (wcount(gap) > wcount(gap - 1)) ldW(IC(W0 + wcount(gap) - 1));
          if constexpr (gap == GPOLL) ld32_sc1(fh, foH, rsC);
          SCHED();
        });
        if constexpr (i == 7) { FT(40 + z); SCHED(); }
        if constexpr (z + 1 < NZ) { A[i] = cat(ldstr(lds + aA + 64 * (z + 1) + 512 * i + 8), ldstr(lds + aA + 64 * (z + 1) + 512 * i)); SCHED(); }
      });
    });
  }
  static_assert(W0 + wcount(NZ * 36 - 1) >= 60, "all W2 fragments issued");
  STAMP(6);
  constexpr int WAFT = 60 - (W0 + wcount(GPOLL));
  waitv<(WAFT > 0 ? WAFT : 0)>();
  FT(48);
  poll_finish(fh, foH, rsC, tag);
  FT(49);
  v4u Hf[HWM];
  const unsigned sH = (4 * rb * 48 + 12 * wave) * 1024;
  auto ldH = [&](auto N) { constexpr int n = N.value; if constexpr (n < 48) ldVo<(n & 3) * 1024, SC1>(Hf[n % HWM], vl, rsH, sH + ((n / 12) * 48 + ((n % 12) & ~3)) * 1024); };
  static_for<0, HW0>([&](auto I) { ldH(I); });
  FT(14);
  asm volatile("s_nop 7" : "+v"(acc[0]), "+v"(acc[1]), "+v"(acc[2]), "+v"(acc[3]), "+v"(acc[4]), "+v"(acc[5]), "+v"(acc[6]), "+v"(acc[7]));
  SCHED();
  int ctb = 0;
  if constexpr (G1R) {
    LAS char* ex = lds + CV_EX + cl * 8192;
    f32x4 keep[4];
#pragma unroll
    for (int i = 0; i < 4; ++i) {
      f32x4 lo = acc[i], hi = acc[4 + i];
      asm volatile("" : "+v"(lo), "+v"(hi));
      *(LAS f32x4*)(ex + half * 4096 + i * 1024 + lane * 16) = half == 0 ? hi : lo;
      keep[i] = half == 0 ? lo : hi;
    }
    waitl<0>(); sbar();
#pragma unroll
    for (int i = 0; i < 4; ++i) acc[i] = keep[i] + *(LAS f32x4*)(ex + (1 - half) * 4096 + i * 1024 + lane * 16);
    ctb = 4 * half;
  }
  FT(51);
  constexpr int NT = G1R ? 4 : 8, NYS = NT / 2;
  {
    asm volatile("" : "+v"(dv));
    const float Dc = blo(dv);
#pragma unroll
    for (int i = 0; i < NT; i += 2) {
      unsigned w0[2][2];
#pragma unroll
      for (int e = 0; e < 2; ++e) {
        const int ct = ctb + i + e;
        const f32x4 a = acc[i + e];
        const v2u uu = lds64(lds + cb + CV_U + 480 + 512 * ct + 32 * c16 + 8 * g);
        w0[e][0] = pk2(fmaf(blo(uu.x), Dc, a[0]), fmaf(bhi(uu.x), Dc, a[1]));
        w0[e][1] = pk2(fmaf(blo(uu.y), Dc, a[2]), fmaf(bhi(uu.y), Dc, a[3]));
      }
      auto s0 = __builtin_amdgcn_permlane16_swap(w0[0][0], w0[1][0], false, false);
      auto s1 = __builtin_amdgcn_permlane16_swap(w0[0][1], w0[1][1], false, false);
      const int ct = ctb + i + (g & 1);
      __builtin_amdgcn_raw_buffer_store_b128(v4u{s0[0], s1[0], s0[1], s1[1]}, rsY, ch * 4096 + 512 * ct + 32 * c16 + 8 * (g & 2), 0, SC1);
    }
  }
  FT(52);
  waitl<0>(); sbar();
  STAMP(7);
  unsigned fe[13];
  const bool bump = blockIdx.x == 0 && wave == 0;
  f32x4 ac2[2][5];
  const unsigned foY0 = C_FY + (nt * 128 + lane) * FS, foY1 = foY0 + 64 * FS;
  const unsigned foG = C_FY + ((rb >> 2) * 128 + 96 + (lane & 31)) * FS;
  unsigned fy0, fy1, fg, gtu;
  v4u yv[3]; v2u qq[5];
  const int s = 64 * rb + 16 * wave + c16;
#define SF_ 5
  constexpr int SF = SF_;
  constexpr int SP = 25;
  constexpr int SY = 40;
  static_assert(g2_issued(SP) == 48 && SY >= SP + 8, "");
  auto extras_at = [](int st) constexpr { return (st == SF ? 1 : 0) + ((st >= SP && st < SP + 8) ? 1 : 0) + (st == SY ? 4 : 0); };
  static_for<0, 48>([&](auto I) {
    constexpr int i = I.value, rt = i / 12, k = i % 12, set = rt & 1;
    constexpr int younger = [&]() constexpr {
      int y = g2_issued(i) - (i + 1);
      if (i < HW0) y += NYS;
      int si = -1; while (i >= HW0 && g2_issued(si + 1) <= i) ++si;
      for (int st = (si < 0 ? 0 : si); st < i; ++st) y += extras_at(st);
      return y; }();
    waitv<(younger < 63 ? younger : 63)>();
    static_for<0, 5>([&](auto C) { constexpr int ct = C.value;
      if constexpr (k == 0) mfma_zA(ac2[set][ct], W[k][ct], Hf[i % HWM]); else mfma_ipA(ac2[set][ct], W[k][ct], Hf[i % HWM]);
      if constexpr (rt > 0 && k >= 1 && k <= 5 && ct == 3) *(LAS f32x4*)(lds + G2_PART + (((rt - 1) * 4 + wave) * 5 + (k - 1)) * 1024 + lane * 16) = ac2[set ^ 1][k - 1];
      if constexpr (ct == 1) { static_for<g2_issued(i), g2_issued(i + 1)>([&](auto N) { ldH(N); }); }
    });
    if constexpr (i == SF) {
      waitv<g2_issued(SF + 1) - HW0>();
      if (lane == 0) st32_sc1(tag, C_FY + ((x * 32 + r) * 4 + wave) * FS, rsC);
    }
    if constexpr (i == SP) ld32_sc1(fy0, foY0, rsC);
    if constexpr (i == SP + 1) ld32_sc1(fy1, foY1, rsC);
    if constexpr (i == SP + 2) ld32_sc1(fg, foG, rsC);
    if constexpr (i >= SP + 3 && i < SP + 8) { const rsrc_t rsQ = mkrs(p.q, S * D * 2); ldV2<AUXP>(qq[i - SP - 3], (s * D + 80 * nt + 16 * (i - SP - 3) + 4 * g) * 2, rsQ, 0); }
    if constexpr (i == 34) {
      if (bump) {
#pragma unroll
        for (int e = 0; e < 13; ++e) ld32_sc1(fe[e], e < 12 ? C_FH + (e * 64 + lane) * FS : C_FKV + lane * FS, rsC);
      }
    }
    if constexpr (i == SY) {
      waitv<5>();
      poll_finish2(fy0, foY0, fy1, foY1, rsC, tag);
      poll_finish(fg, foG, rsC, tag);
      { const rsrc_t rsG = mkrs(p.ws + WS_G, S * 4); ld32_sc1(gtu, s * 4, rsG); }
#pragma unroll
      for (int u = 0; u < 3; ++u) {
        const int idx = u * 256 + wave * 64 + lane, d = idx >> 3, cch = idx & 7;
        ldV<SC1>(yv[u], idx < 640 ? (unsigned)((80 * nt + d) * 4096 + rb * 128 + cch * 16) : 0x80000000u, rsY, 0);
      }
    }
    if constexpr (i % 4 == 3) { FT(16 + i / 4); }
    SCHED();
  });
  STAMP(9);
  asm volatile("s_nop 7" : "+v"(ac2[1][0]), "+v"(ac2[1][1]), "+v"(ac2[1][2]), "+v"(ac2[1][3]), "+v"(ac2[1][4]));
#pragma unroll
  for (int ct = 0; ct < 5; ++ct) *(LAS f32x4*)(lds + G2_PART + ((3 * 4 + wave) * 5 + ct) * 1024 + lane * 16) = ac2[1][ct];
  waitv<0>();
#pragma unroll
  for (int u = 0; u < 3; ++u) {
    asm volatile("" : "+v"(yv[u]));
    const int idx = u * 256 + wave * 64 + lane, d = idx >> 3, cch = idx & 7;
    if (idx < 640) { *(LAS v2u*)(lds + G2_Y + d * 136 + cch * 16) = v2u{yv[u].x, yv[u].y}; *(LAS v2u*)(lds + G2_Y + d * 136 + cch * 16 + 8) = v2u{yv[u].z, yv[u].w}; }
  }
  asm volatile("" : "+v"(gtu));
#pragma unroll
  for (int ct = 0; ct < 5; ++ct) asm volatile("" : "+v"(qq[ct]));
  waitl<0>(); sbar();
  STAMP(10);
  const float gt = __builtin_bit_cast(float, gtu);
#pragma unroll
  for (int ct = 0; ct < 5; ++ct) {
    f32x4 b = *(LAS f32x4*)(lds + G2_PART + ((wave * 4 + 0) * 5 + ct) * 1024 + lane * 16);
#pragma unroll
    for (int w = 1; w < 4; ++w) b += *(LAS f32x4*)(lds + G2_PART + ((wave * 4 + w) * 5 + ct) * 1024 + lane * 16);
    const v2u yy = ldstr(lds + G2_Y + (16 * ct + 4 * g + q4) * 136 + (16 * wave + 4 * p4) * 2);
    const float o0 = gt * (blo(qq[ct].x) * blo(yy.x)) + (1.f - gt) * b[0];
    const float o1 = gt * (bhi(qq[ct].x) * bhi(yy.x)) + (1.f - gt) * b[1];
    const float o2 = gt * (blo(qq[ct].y) * blo(yy.y)) + (1.f - gt) * b[2];
    const float o3 = gt * (bhi(qq[ct].y) * bhi(yy.y)) + (1.f - gt) * b[3];
    *(GAS v2u*)(p.out + s * D + 80 * nt + 16 * ct + 4 * g) = v2u{pk2(o0, o1), pk2(o2, o3)};
  }
  if (bump) {
    unsigned bad = 0;
#pragma unroll
    for (int e = 0; e < 13; ++e) { asm volatile("" : "+v"(fe[e])); bad |= fe[e] ^ tag; }
    if (__builtin_amdgcn_ballot_w64(bad != 0) != 0) {
#pragma unroll 1
      for (int e = 0; e < 13; ++e) { const unsigned fo = (e < 12 ? C_FH + (e * 64 + lane) * FS : C_FKV + lane * FS); unsigned f; ld32_sc1(f, fo, rsC); waitv<0>(); poll_finish(f, fo, rsC, tag); }
    }
    if (lane == 0) st32_sc1(tag, C_EPOCH, rsC);
  }
  STAMP(11);
  STAMP_FLUSH(); FT_FLUSH();
}

__global__ __launch_bounds__(256, 1) void fused(Params p) {
  v4u pf;
  {
    unsigned long long pc; unsigned len;
    asm volatile("s_getpc_b64 %0\nLpf_here:\n\ts_mov_b32 %1, Lpf_end-Lpf_here" : "=s"(pc), "=s"(len));
    const rsrc_t rs = mkrs((const void*)pc, len);
    asm volatile("buffer_load_dwordx4 %0, %1, %2, 0 offen" : "=v"(pf) : "v"(((blockIdx.x >> 3) * 256 + threadIdx.x) * 16), "s"(rs));
    SCHED();
  }
  extern __shared__ __attribute__((aligned(16))) char smem[];
  LAS char* lds = (LAS char*)smem;
  const int lane = threadIdx.x & 63, wave = __builtin_amdgcn_readfirstlane(threadIdx.x >> 6);
  const int x = blockIdx.x & 7, r = blockIdx.x >> 3;
  if (__builtin_expect(r < 24, 1)) body<true>(p, lds, x, r, wave, lane);
  else body<false>(p, lds, x, r, wave, lane);
  asm volatile("s_waitcnt vmcnt(0)\nLpf_end:" : "+v"(pf));
}

extern "C" void kernel_launch(void* const* d_in, const int* in_sizes, int n_in, void* d_out, int out_size, void* d_ws, size_t ws_size, hipStream_t stream) {
  Params p;
  p.q = (const u16*)d_in[0]; p.k = (const u16*)d_in[1]; p.v = (const u16*)d_in[2]; p.sk = (const u16*)d_in[3];
  p.h = (const u16*)d_in[4]; p.Dv = (const u16*)d_in[5]; p.w1 = (const u16*)d_in[7]; p.w2 = (const u16*)d_in[8]; p.gw = (const u16*)d_in[9];
  p.out = (u16*)d_out; p.ws = (char*)d_ws;
  (void)hipFuncSetAttribute((const void*)fused, hipFuncAttributeMaxDynamicSharedMemorySize, LDS_BYTES);
  fused<<<256, 256, LDS_BYTES, stream>>>(p);
}
```

```cpp
#include <hip/hip_runtime.h>
#include <type_traits>
#define SPIN_SLEEP 4
#define FSTR 64
#include <stdint.h>

typedef unsigned short u16;
typedef unsigned v4u __attribute__((ext_vector_type(4)));
typedef unsigned v2u __attribute__((ext_vector_type(2)));
typedef float f32x4 __attribute__((ext_vector_type(4)));
typedef float f32x2 __attribute__((ext_vector_type(2)));
typedef __bf16 bf16x8 __attribute__((ext_vector_type(8)));
typedef __bf16 bf16x2 __attribute__((ext_vector_type(2)));
typedef short v4s __attribute__((ext_vector_type(4)));
typedef short v8s __attribute__((ext_vector_type(8)));
#define GAS __attribute__((address_space(1)))
#define LAS __attribute__((address_space(3)))
#define DEV __device__ __forceinline__
typedef __amdgpu_buffer_rsrc_t rsrc_t;

constexpr int S = 2048, D = 640, F = 1536;

template <int N> DEV void waitv() { asm volatile("s_waitcnt vmcnt(%0)" ::"i"(N) : "memory"); }
template <int N> DEV void waitl() { asm volatile("s_waitcnt lgkmcnt(%0)" ::"i"(N) : "memory"); }
DEV void sbar() { asm volatile("s_barrier" ::: "memory"); }
#define SCHED() __builtin_amdgcn_sched_barrier(0)
DEV rsrc_t mkrs(const void* p, unsigned bytes) { return __builtin_amdgcn_make_buffer_rsrc((void*)p, 0, bytes, 0x00020000); }
DEV f32x4 mfma(v4u a, v4u b, f32x4 c) {
  return __builtin_amdgcn_mfma_f32_16x16x32_bf16(__builtin_bit_cast(bf16x8, a), __builtin_bit_cast(bf16x8, b), c, 0, 0, 0);
}
DEV v4u lds128(LAS const char* p) { return *(LAS const v4u*)p; }
DEV v2u lds64(LAS const char* p) { return *(LAS const v2u*)p; }
DEV v2u ldstr(LAS const char* p) { return __builtin_bit_cast(v2u, __builtin_amdgcn_ds_read_tr16_b64_v4i16((LAS v4s*)p)); }
DEV v4u cat(v2u a, v2u b) { return v4u{a.x, a.y, b.x, b.y}; }
DEV unsigned pk2(float a, float b) { bf16x2 r = __builtin_convertvector(f32x2{a, b}, bf16x2); return __builtin_bit_cast(unsigned, r); }
DEV float blo(unsigned d) { return __builtin_bit_cast(float, d << 16); }
DEV float bhi(unsigned d) { return __builtin_bit_cast(float, d & 0xffff0000u); }
DEV void setm0(unsigned v) { asm volatile("s_mov_b32 m0, %0\n\ts_nop 0" ::"s"(v)); }
template <int OFF, int NT = 0> DEV void dma16(unsigned voff, rsrc_t rs, unsigned soff) {
  if constexpr (NT) asm volatile("buffer_load_dwordx4 %0, %1, %2 offen offset:%3 nt lds" ::"v"(voff), "s"(rs), "s"(soff), "i"(OFF));
  else asm volatile("buffer_load_dwordx4 %0, %1, %2 offen offset:%3 lds" ::"v"(voff), "s"(rs), "s"(soff), "i"(OFF));
}
typedef float f32x16 __attribute__((ext_vector_type(16)));
DEV f32x16 mfma32(v4u a, v4u b, f32x16 c) {
  return __builtin_amdgcn_mfma_f32_32x32x16_bf16(__builtin_bit_cast(bf16x8, a), __builtin_bit_cast(bf16x8, b), c, 0, 0, 0);
}
template <int IMM> DEV void setm0_add(unsigned base) { asm volatile("s_add_u32 m0, %0, %1" ::"s"(base), "i"(IMM) : "scc"); }
template <int I, int N, class Fn> DEV void static_for(Fn&& f) { if constexpr (I < N) { f(std::integral_constant<int, I>{}); static_for<I + 1, N>(f); } }
DEV void mfma_ip(f32x4& c, const v4u& a, const v4u& b) { asm volatile("v_mfma_f32_16x16x32_bf16 %0, %1, %2, %0" : "+v"(c) : "v"(a), "v"(b)); }
DEV void mfma_ipA(f32x4& c, const v4u& a, const v4u& b) { asm volatile("v_mfma_f32_16x16x32_bf16 %0, %1, %2, %0" : "+v"(c) : "a"(a), "v"(b)); }
DEV void mfma_z(f32x4& c, const v4u& a, const v4u& b) { asm volatile("v_mfma_f32_16x16x32_bf16 %0, %1, %2, 0" : "=&v"(c) : "v"(a), "v"(b)); }
DEV void mfma_zA(f32x4& c, const v4u& a, const v4u& b) { asm volatile("v_mfma_f32_16x16x32_bf16 %0, %1, %2, 0" : "=&v"(c) : "a"(a), "v"(b)); }
template <int AUX> DEV void ldA(v4u& d, unsigned voff, rsrc_t rs, unsigned soff) {
  if constexpr (AUX) asm volatile("buffer_load_dwordx4 %0, %1, %2, %3 offen sc1" : "=a"(d) : "v"(voff), "s"(rs), "s"(soff));
  else asm volatile("buffer_load_dwordx4 %0, %1, %2, %3 offen" : "=a"(d) : "v"(voff), "s"(rs), "s"(soff));
}
template <int AUX> DEV void ldV(v4u& d, unsigned voff, rsrc_t rs, unsigned soff) {
  if constexpr (AUX == 2) asm volatile("buffer_load_dwordx4 %0, %1, %2, %3 offen nt" : "=v"(d) : "v"(voff), "s"(rs), "s"(soff));
  else if constexpr (AUX == 16) asm volatile("buffer_load_dwordx4 %0, %1, %2, %3 offen sc1" : "=v"(d) : "v"(voff), "s"(rs), "s"(soff));
  else asm volatile("buffer_load_dwordx4 %0, %1, %2, %3 offen" : "=v"(d) : "v"(voff), "s"(rs), "s"(soff));
}
DEV void nop8() { asm volatile("s_nop 7"); }

constexpr size_t WS_CTRL = 0;
constexpr size_t WS_KVT = 262144;
constexpr size_t WS_YT = WS_KVT + (size_t)D * S * 2;
constexpr size_t WS_W2T = WS_YT + (size_t)D * S * 2;
constexpr size_t WS_HF = WS_W2T + (size_t)8 * 48 * 5 * 1024;
constexpr size_t WS_G = WS_HF + (size_t)128 * 48 * 1024;
constexpr size_t WS_END = WS_G + S * 4;
constexpr unsigned C_EPOCH = 0, C_TMO = 256;
constexpr unsigned FS = FSTR;
constexpr unsigned C_FW = 4096;
constexpr unsigned C_FKV = C_FW + 64 * FS;
constexpr unsigned C_FH = C_FKV + 64 * FS;
constexpr unsigned C_FY = C_FH + 768 * FS;
constexpr unsigned C_BYTES = 262144;
static_assert(C_FY + 1024 * FS <= C_BYTES, "");

#define NSTAMP 16
#define STAMP_OFF (size_t)(32u << 20)
#define STAMP_DECL
#define STAMP(i)
#define STAMP_FLUSH()
#define FT_DECL
#define FT(i)
#define FT_FLUSH()

struct Params {
  const u16 *q, *k, *v, *sk, *h, *Dv, *w1, *w2, *gw;
  u16* out;
  char* ws;
};

constexpr int LDS_BYTES = 160 * 1024;
constexpr int SC1 = 16;
#define NT_X 0
#define NT_W1 0
#define NT_P 0
constexpr int AUXP = NT_P ? 2 : 0;
#define IC(n) std::integral_constant<int, (n)>{}

DEV void ld32_sc1(unsigned& v, unsigned voff, rsrc_t rs) { asm volatile("buffer_load_dword %0, %1, %2, 0 offen sc1" : "=v"(v) : "v"(voff), "s"(rs)); }
DEV void st32_sc1(unsigned v, unsigned voff, rsrc_t rs) { asm volatile("buffer_store_dword %0, %1, %2, 0 offen sc1" ::"v"(v), "v"(voff), "s"(rs)); }
DEV void poll_finish(unsigned v, unsigned voff, rsrc_t rsC, unsigned tag) {
  asm volatile("" : "+v"(v));
  if (__builtin_amdgcn_ballot_w64(v != tag) == 0) return;
#pragma unroll 1
  for (int spins = 0; spins < (1 << 15); ++spins) {
    __builtin_amdgcn_s_sleep(SPIN_SLEEP);
    ld32_sc1(v, voff, rsC);
    asm volatile("s_waitcnt vmcnt(0)" : "+v"(v)::"memory");
    if (__builtin_amdgcn_ballot_w64(v != tag) == 0) return;
  }
  st32_sc1(1u, C_TMO, rsC);
}

DEV void poll_finish2(unsigned a, unsigned oa, unsigned b, unsigned ob, rsrc_t rsC, unsigned tag) {
  asm volatile("" : "+v"(a), "+v"(b));
  if (__builtin_amdgcn_ballot_w64(a != tag || b != tag) == 0) return;
#pragma unroll 1
  for (int spins = 0; spins < (1 << 15); ++spins) {
    __builtin_amdgcn_s_sleep(SPIN_SLEEP);
    ld32_sc1(a, oa, rsC); ld32_sc1(b, ob, rsC);
    asm volatile("s_waitcnt vmcnt(0)" : "+v"(a), "+v"(b)::"memory");
    if (__builtin_amdgcn_ballot_w64(a != tag || b != tag) == 0) return;
  }
  st32_sc1(1u, C_TMO, rsC);
}

DEV void mfma32_ip(f32x16& c, const v4u& a, const v4u& b) { asm volatile("v_mfma_f32_32x32x16_bf16 %0, %1, %2, %0" : "+v"(c) : "v"(a), "v"(b)); }
DEV void mfma32_z(f32x16& c, const v4u& a, const v4u& b) { asm volatile("v_mfma_f32_32x32x16_bf16 %0, %1, %2, 0" : "=&v"(c) : "v"(a), "v"(b)); }

constexpr int KV_BASE = 32768, KV_GS = 1152, KV_V = 9 * KV_GS, KV_CG = 17 * KV_GS;
constexpr int CV_BASE = 32768;
constexpr int CV_HC = 4160;
constexpr int CV_U = 4 * CV_HC;
constexpr int CV_CH = CV_U + 4608;
constexpr int CV_EX = CV_BASE + 4 * CV_CH;
constexpr int G2_PART = 0;
constexpr int G2_Y = 81920;
constexpr int HW = 24;

template <int AUX> DEV void ldV2(v2u& d, unsigned voff, rsrc_t rs, unsigned soff) {
  if constexpr (AUX == 2) asm volatile("buffer_load_dwordx2 %0, %1, %2, %3 offen nt" : "=v"(d) : "v"(voff), "s"(rs), "s"(soff));
  else if constexpr (AUX == 16) asm volatile("buffer_load_dwordx2 %0, %1, %2, %3 offen sc1" : "=v"(d) : "v"(voff), "s"(rs), "s"(soff));
  else asm volatile("buffer_load_dwordx2 %0, %1, %2, %3 offen" : "=v"(d) : "v"(voff), "s"(rs), "s"(soff));
}
DEV void dma16s(unsigned voff, rsrc_t rs, unsigned soff, unsigned m0v) {
  if constexpr (NT_P) asm volatile("s_mov_b32 m0, %3\n\ts_nop 0\n\tbuffer_load_dwordx4 %0, %1, %2 offen nt lds" ::"v"(voff), "s"(rs), "s"(soff), "s"(m0v));
  else asm volatile("s_mov_b32 m0, %3\n\ts_nop 0\n\tbuffer_load_dwordx4 %0, %1, %2 offen lds" ::"v"(voff), "s"(rs), "s"(soff), "s"(m0v));
}
DEV float dot2(unsigned a, unsigned b, float c) { return __builtin_amdgcn_fdot2_f32_bf16(__builtin_bit_cast(bf16x2, a), __builtin_bit_cast(bf16x2, b), c, false); }
DEV unsigned mktag(unsigned& epoch) {
  asm volatile("" : "+v"(epoch));
  return __builtin_amdgcn_readfirstlane(epoch * 0x9E3779B1u + 0x7F4A7C15u);
}

template <int NR, class ST> DEV void kv_math(const v4u (&kk)[NR + 3], const v4u (&vv)[NR], const v4u (&ww)[4], ST&& st) {
#pragma unroll
  for (int cp = 0; cp < 4; ++cp) {
    f32x2 w[4];
    w[0] = f32x2{blo(ww[cp].x), blo(ww[cp].z)}; w[1] = f32x2{bhi(ww[cp].x), bhi(ww[cp].z)};
    w[2] = f32x2{blo(ww[cp].y), blo(ww[cp].w)}; w[3] = f32x2{bhi(ww[cp].y), bhi(ww[cp].w)};
    f32x2 kf[NR + 3];
#pragma unroll
    for (int i = 0; i < NR + 3; ++i) kf[i] = f32x2{blo(kk[i][cp]), bhi(kk[i][cp])};
    f32x2 r[NR];
#pragma unroll
    for (int i = 0; i < NR; ++i) {
      f32x2 a = kf[i + 3] * w[0];
      a = __builtin_elementwise_fma(kf[i + 2], w[1], a);
      a = __builtin_elementwise_fma(kf[i + 1], w[2], a);
      a = __builtin_elementwise_fma(kf[i], w[3], a);
      r[i] = a * f32x2{blo(vv[i][cp]), bhi(vv[i][cp])};
    }
    st(cp, r);
  }
}

#define Q0 7
#define ROT 0
#define PREP_FIRST 0
#define HPUB 90
#define WPRE_ 16
constexpr int WPRE = WPRE_;
constexpr int HW0 = 16, HWM = 24;
constexpr int PW_BASE = 131072, PW_SZ = 3072;
constexpr int g2_issued(int i) { int r = HW0; for (int s = 0; s < i; ++s) r += (r - s < HWM) ? ((r + 1 - s < HWM) ? 2 : 1) : 0; return r < 48 ? r : 48; }

template <int OFF, int AUX> DEV void ldVo(v4u& d, unsigned voff, rsrc_t rs, unsigned soff) {
  static_assert(AUX == 16, "");
  asm volatile("buffer_load_dwordx4 %0, %1, %2, %3 offen offset:%4 sc1" : "=v"(d) : "v"(voff), "s"(rs), "s"(soff), "i"(OFF));
}
template <int OFF> DEV void ldAo(v4u& d, unsigned voff, rsrc_t rs, unsigned soff) {
  asm volatile("buffer_load_dwordx4 %0, %1, %2, %3 offen offset:%4 sc1" : "=a"(d) : "v"(voff), "s"(rs), "s"(soff), "i"(OFF));
}

template <bool G1R>
DEV void body(const Params& p, LAS char* lds, const int x, const int r, const int wave, const int lane) {
  const int g = lane >> 4, q4 = (lane >> 2) & 3, p4 = lane & 3, c16 = lane & 15;
  STAMP_DECL; FT_DECL;
  STAMP(0); FT(13);
  const rsrc_t rsC = mkrs(p.ws + WS_CTRL, C_BYTES);
  unsigned epoch, tag;
  ld32_sc1(epoch, C_EPOCH, rsC);
  const int cl = G1R ? (wave >> 1) : wave, half = G1R ? (wave & 1) : 0;
  const int ch = (G1R ? 80 * x + 2 * r : 80 * x + 48 + 4 * (r - 24)) + cl;
  const unsigned cb = CV_BASE + cl * CV_CH;
  constexpr int NCHK = G1R ? 2 : 4;
  unsigned dv;
  { const rsrc_t rsD = mkrs(p.Dv, D * 2); asm volatile("buffer_load_ushort %0, %1, %2, 0 offen" : "=v"(dv) : "v"(ch * 2), "s"(rsD)); }
  const rsrc_t rsHh = mkrs(p.h, D * S * 2), rsU = mkrs(p.ws + WS_KVT, D * S * 2), rsY = mkrs(p.ws + WS_YT, D * S * 2);
  const rsrc_t rsW2 = mkrs(p.ws + WS_W2T, 8 * 48 * 5 * 1024), rsH = mkrs(p.ws + WS_HF, 128 * 48 * 1024);
#define MAP42 1
  const int rb = MAP42 ? 8 * (x >> 1) + (r & 7) : 4 * x + (r & 3), nt = MAP42 ? 4 * (x & 1) + (r >> 3) : r >> 2;
  const unsigned vl = lane * 16;
  const unsigned sW = (nt * 48 + 12 * wave) * 5 * 1024;
  const unsigned foW = C_FW + lane * FS, foKV = C_FKV + lane * FS;
  v4u W[12][5];
  auto ldW = [&](auto I) { constexpr int i = I.value; if constexpr (i >= 0 && i < 60) ldAo<(i & 3) * 1024>(W[i / 5][i % 5], vl, rsW2, sW + (i & ~3) * 1024); };
  v4u hc[NCHK], uc[NCHK]; v2u hp0;
  auto ld_h = [&]() {
    ldV2<AUXP>(hp0, half > 0 ? (unsigned)(ch * 4096 + half * NCHK * 1024 - 8) : 0x80000000u, rsHh, 0);
#pragma unroll
    for (int i = 0; i < NCHK; ++i) ldV<AUXP>(hc[i], ch * 4096 + ((half * NCHK + i) * 64 + lane) * 16, rsHh, 0);
  };
  auto ld_u = [&]() {
#pragma unroll
    for (int i = 0; i < NCHK; ++i) ldV<SC1>(uc[i], ch * 4096 + ((half * NCHK + i) * 64 + lane) * 16, rsU, 0);
  };
  auto fill_h = [&]() {
    asm volatile("" : "+v"(hp0));
#pragma unroll
    for (int i = 0; i < NCHK; ++i) asm volatile("" : "+v"(hc[i]));
    unsigned e0 = hp0.x, e1 = hp0.y;
#pragma unroll
    for (int i = 0; i < NCHK; ++i) {
      const int n = (half * NCHK + i) * 64 + lane;
      const v4u cur = hc[i];
      const v2u prv = v2u{(unsigned)__builtin_amdgcn_update_dpp((int)e0, (int)cur.z, 0x138, 0xf, 0xf, false), (unsigned)__builtin_amdgcn_update_dpp((int)e1, (int)cur.w, 0x138, 0xf, 0xf, false)};
      e0 = __builtin_amdgcn_readlane(cur.z, 63); e1 = __builtin_amdgcn_readlane(cur.w, 63);
      const unsigned om2 = __builtin_amdgcn_alignbit(prv.y, prv.x, 16), om1 = __builtin_amdgcn_alignbit(cur.x, prv.y, 16);
      const unsigned o0 = __builtin_amdgcn_alignbit(cur.y, cur.x, 16), o1 = __builtin_amdgcn_alignbit(cur.z, cur.y, 16), o2 = __builtin_amdgcn_alignbit(cur.w, cur.z, 16);
      LAS char* dst = lds + cb + (n + 2) * 16;
      *(LAS v4u*)(dst) = cur;
      *(LAS v4u*)(dst + CV_HC) = v4u{om1, o0, o1, o2};
      *(LAS v4u*)(dst + 2 * CV_HC) = v4u{prv.y, cur.x, cur.y, cur.z};
      *(LAS v4u*)(dst + 3 * CV_HC) = v4u{om2, om1, o0, o1};
    }
    if (half == 0) {
      if (lane < 8) *(LAS v4u*)(lds + cb + (lane >> 1) * CV_HC + (lane & 1) * 16) = v4u{0, 0, 0, 0};
      if (lane >= 32 && lane < 62) *(LAS v4u*)(lds + cb + CV_U + (lane - 32) * 16) = v4u{0, 0, 0, 0};
    }
  };
  auto fill_u = [&]() {
#pragma unroll
    for (int i = 0; i < NCHK; ++i) {
      asm volatile("" : "+v"(uc[i]));
      *(LAS v4u*)(lds + cb + CV_U + 480 + ((half * NCHK + i) * 64 + lane) * 16) = uc[i];
    }
  };
  unsigned fw;

  if constexpr (G1R) {
    const int sb = 4 * (x >> 1) + (r & 3), fs = 6 * (x & 1) + (r >> 2);
    f32x16 acc1[2][2];
    const rsrc_t rsX = mkrs(p.q, S * D * 2), rsW = mkrs(p.w1, D * F * 2);
    unsigned vX[4], vW[4];
#pragma unroll
    for (int ii = 0; ii < 4; ++ii) {
      vX[ii] = (32 * wave + 8 * ii + (lane >> 3)) * 1280 + (((lane & 7) ^ (4 * (ii & 1) + g)) << 4) - 1024 * ii;
      vW[ii] = (16 * wave + 4 * ii + g) * 3072 + (((((c16 >> 1) ^ (g << 1)) << 1) | (c16 & 1)) << 4) - 1024 * ii;
    }
    const unsigned sX0 = sb * 128 * 1280, sW0 = fs * 256;
    const unsigned mX = wave * 4096, mW = 16384 + wave * 4096;
    static_for<0, 7>([&](auto Q) { constexpr int q = Q.value, t = q >> 1;
      setm0_add<t * 32768>((q & 1) ? mW : mX); asm volatile("s_nop 0");
      static_for<0, 4>([&](auto I) { constexpr int i = I.value;
        if constexpr (q & 1) dma16<1024 * i, NT_W1>(vW[i], rsW, sW0 + t * (64 * 3072)); else dma16<1024 * i, NT_X>(vX[i], rsX, sX0 + t * 128); });
    });
    FT(11);
    const int ws_ = wave & 1, wf = wave >> 1, h = lane >> 5, r32 = lane & 31;
    unsigned aX[4], aW[2];
#pragma unroll
    for (int ks = 0; ks < 4; ++ks) aX[ks] = (64 * ws_ + r32) * 128 + (((2 * ks + h) ^ ((r32 >> 1) & 7)) << 4);
#pragma unroll
    for (int ft = 0; ft < 2; ++ft) aW[ft] = 16384 + (8 * h + q4) * 256 + (((2 * (2 * wf + ft) + (g & 1)) ^ (q4 << 1)) << 5) + 8 * p4;
#pragma unroll
    for (int a = 0; a < 2; ++a)
#pragma unroll
      for (int b = 0; b < 2; ++b)
#pragma unroll
        for (int e = 0; e < 16; ++e) acc1[a][b][e] = 0.f;
    asm volatile("s_nop 1" : "+v"(acc1[0][0]), "+v"(acc1[0][1]), "+v"(acc1[1][0]), "+v"(acc1[1][1]));
    v4u A[2][2][2], B[2][2][2];
    v2u At;
    unsigned cX[2], cW[2];
    auto rd = [&](auto HF, auto SET, auto N) {
      constexpr int hf = HF.value, set = SET.value, n = N.value;
      constexpr int kk = n / 6, rr = n % 6, ks = 2 * hf + kk;
      if constexpr (rr == 0) B[set][kk][0] = lds128(lds + cX[kk]);
      else if constexpr (rr == 3) B[set][kk][1] = lds128(lds + cX[kk] + 32 * 128);
      else { constexpr int ft = rr / 3, jj = (rr % 3) - 1;
        v2u xx = ldstr(lds + cW[ft] + ks * 4096 + jj * 1024);
        if constexpr (jj == 0) At = xx; else A[set][kk][ft] = cat(At, xx); }
    };
    auto set_addr = [&](int hf, unsigned bo) { cX[0] = aX[2 * hf] + bo; cX[1] = aX[2 * hf + 1] + bo; cW[0] = aW[0] + bo; cW[1] = aW[1] + bo; };
    unsigned fk;
    waitv<20>(); sbar();
    STAMP(1);
    set_addr(0, 0);
    static_for<0, 12>([&](auto N) { rd(IC(0), IC(0), N); });
    SCHED();
    unsigned bo = 0;
    unsigned bW = 3 * 32768, bX = 4 * 32768;
    unsigned sWn = sW0 + 3 * (64 * 3072), sXn = sX0 + 4 * 128;
    auto stage = [&](auto DMA, int t) {
      constexpr bool dma = DMA.value;
      const unsigned nbo = bo == 4 * 32768 ? 0 : bo + 32768;
      set_addr(1, bo);
      static_for<0, 8>([&](auto M) {
        constexpr int m = M.value, kk = m >> 2, ft = (m >> 1) & 1, st = m & 1;
        mfma32_ip(acc1[ft][st], A[0][kk][ft], B[0][kk][st]);
        if constexpr (m < 6) { rd(IC(1), IC(1), IC(2 * m)); rd(IC(1), IC(1), IC(2 * m + 1)); }
        if constexpr (dma && m == 0) asm volatile("s_add_u32 m0, %0, %1" ::"s"(mW), "s"(bW) : "scc");
        if constexpr (dma && (m & 1)) dma16<1024 * (m >> 1), NT_W1>(vW[m >> 1], rsW, sWn);
        SCHED();
      });
      if constexpr (dma) { waitv<16>(); sbar(); }
      else { if (t == 7) waitv<8>(); else waitv<0>(); sbar(); }
      SCHED();
      set_addr(0, nbo);
      const unsigned sx = (dma && t < 6) ? sXn : 0x80000000u;
      static_for<0, 8>([&](auto M) {
        constexpr int m = M.value, kk = m >> 2, ft = (m >> 1) & 1, st = m & 1;
        mfma32_ip(acc1[ft][st], A[1][kk][ft], B[1][kk][st]);
        if constexpr (m < 6) { rd(IC(0), IC(0), IC(2 * m)); rd(IC(0), IC(0), IC(2 * m + 1)); }
        if constexpr (dma && m == 0) asm volatile("s_add_u32 m0, %0, %1" ::"s"(mX), "s"(bX) : "scc");
        if constexpr (dma && (m & 1)) dma16<1024 * (m >> 1), NT_X>(vX[m >> 1], rsX, sx);
        SCHED();
      });
      bo = nbo;
      bW = bW == 4 * 32768 ? 0 : bW + 32768; bX = bX == 4 * 32768 ? 0 : bX + 32768;
      sWn += 64 * 3072; sXn += 128;
    };
#pragma unroll 1
    for (int t = 0; t < 7; ++t) stage(std::true_type{}, t);
#pragma unroll 1
    for (int t = 7; t < 10; ++t) stage(std::false_type{}, t);
    asm volatile("s_nop 15" : "+v"(acc1[0][0]), "+v"(acc1[0][1]), "+v"(acc1[1][0]), "+v"(acc1[1][1]));
    sbar();
    STAMP(2);
    tag = mktag(epoch);
    ld_h();
    const unsigned vo = (g & 1) * (48 * 1024) + (c16 + 16 * h) * 16;
    static_for<0, 4>([&](auto Q) {
      constexpr int qd = Q.value, st = qd >> 1, ft = qd & 1;
      const f32x16 a = acc1[ft][st];
      unsigned o[8];
      static_for<0, 2>([&](auto G) {
        constexpr int gq = G.value;
        static_for<0, 4>([&](auto E) {
          constexpr int e = 8 * gq + 2 * E.value;
          const float r0 = a[e] * __builtin_amdgcn_rcpf(1.f + __builtin_amdgcn_exp2f(a[e] * -1.44269504088896f));
          const float r1 = a[e + 1] * __builtin_amdgcn_rcpf(1.f + __builtin_amdgcn_exp2f(a[e + 1] * -1.44269504088896f));
          o[e >> 1] = pk2(r0, r1);
        });
        SCHED();
      });
      const unsigned so = ((8 * sb + 4 * ws_ + 2 * st) * 48 + 4 * fs + 2 * wf + ft) * 1024;
      __builtin_amdgcn_raw_buffer_store_b128(v4u{o[0], o[1], o[4], o[5]}, rsH, vo, so, SC1);
      __builtin_amdgcn_raw_buffer_store_b128(v4u{o[2], o[3], o[6], o[7]}, rsH, vo + 512, so, SC1);
      SCHED();
      FT(32 + qd); SCHED();
    });
    ld32_sc1(fk, foKV, rsC); ld32_sc1(fw, foW, rsC);
    waitv<10>();
    fill_h();
    FT(54);
    waitv<0>();
    if (lane == 0) st32_sc1(tag, C_FH + ((sb * 2 + ws_) * 24 + fs * 2 + wf) * FS, rsC);
    FT(55);
    poll_finish(fk, foKV, rsC, tag);
    ld_u();
    FT(36);
    poll_finish(fw, foW, rsC, tag);
    FT(37);
    static_for<0, WPRE>([&](auto I) { ldW(I); });
    STAMP(3);
    waitv<WPRE>();
    fill_u();
    waitl<0>(); sbar();
    STAMP(4);
  } else {
    const int j = (r - 24) * 8 + x, rbk = j >> 1, cg0 = 5 * (j & 1);
    const rsrc_t rsK = mkrs(p.k, S * D * 2), rsV = mkrs(p.v, S * D * 2), rsS = mkrs(p.sk, D * 8), rsO = mkrs(p.ws + WS_KVT, D * S * 2);
    const unsigned vr = (lane >> 3) * 1280 + ((lane & 7) << 4), vr4 = vr ^ 64;
    auto dmaK = [&](int cgl, int G) {
      const int row = 64 * rbk - 8 + 8 * G;
      dma16s(((G >> 1) & 1) ? vr4 : vr, rsK, row >= 0 ? (unsigned)(row * 1280 + (cg0 + cgl) * 128) : 0x80000000u, KV_BASE + cgl * KV_CG + G * KV_GS);
    };
    auto dmaV = [&](int cgl, int G) {
      dma16s(((G >> 1) & 1) ? vr4 : vr, rsV, (64 * rbk + 8 * G) * 1280 + (cg0 + cgl) * 128, KV_BASE + cgl * KV_CG + KV_V + G * KV_GS);
    };
#pragma unroll
    for (int G = 0; G < 9; ++G) dmaK(wave, G);
#pragma unroll
    for (int G = 0; G < 8; ++G) dmaV(wave, G);
    const unsigned pw = PW_BASE + wave * PW_SZ;
    dma16s(lane < 32 ? (unsigned)((cg0 + wave) * 512 + lane * 16) : (lane < 40 ? (unsigned)((64 * (cg0 + 4) + 16 * wave) * 8 + (lane - 32) * 16) : 0x80000000u), rsS, 0, pw);
    dmaK(4, wave); dmaK(4, wave + 4); dmaV(4, wave); dmaV(4, wave + 4);
    if (wave == 0) dmaK(4, 8);
    const int cc = lane >> 3, rg = lane & 7;
    FT(10);
    waitv<4>();
    STAMP(1);
    {
      const unsigned kb = KV_BASE + wave * KV_CG;
      const unsigned a0 = kb + rg * KV_GS + ((cc ^ (4 * ((rg >> 1) & 1))) << 4), a1 = kb + (rg + 1) * KV_GS + ((cc ^ (4 * (((rg + 1) >> 1) & 1))) << 4);
      v4u kk[11], vv[8], ww[4];
#pragma unroll
      for (int i = 0; i < 4; ++i) ww[i] = lds128(lds + pw + cc * 64 + i * 16);
#pragma unroll
      for (int i = 0; i < 11; ++i) kk[i] = i < 3 ? lds128(lds + a0 + (5 + i) * 128) : lds128(lds + a1 + (i - 3) * 128);
#pragma unroll
      for (int i = 0; i < 8; ++i) vv[i] = lds128(lds + a0 + KV_V + i * 128);
      const unsigned ob = (64 * (cg0 + wave) + 8 * cc) * 4096 + (64 * rbk + 8 * rg) * 2;
      kv_math<8>(kk, vv, ww, [&](int cp, const f32x2 (&rr)[8]) {
#pragma unroll
        for (int c = 0; c < 2; ++c) {
          v4u o{pk2(rr[0][c], rr[1][c]), pk2(rr[2][c], rr[3][c]), pk2(rr[4][c], rr[5][c]), pk2(rr[6][c], rr[7][c])};
          __builtin_amdgcn_raw_buffer_store_b128(o, rsO, ob + (2 * cp + c) * 4096, 0, SC1);
        }
      });
    }
    const int id = 4 * j + wave, pk = id / 5, blk = id % 5;
    const rsrc_t rsWs = mkrs(p.w2, F * D * 2);
    const unsigned ldsb = wave * 8192;
#pragma unroll
    for (int i = 0; i < 8; ++i) {
      const int row = 4 * i + g;
      dma16s((32 * pk + row) * 1280 + blk * 256 + (((((c16 >> 1) ^ (row & 7)) << 1) | (c16 & 1)) << 4), rsWs, id < 240 ? 0u : 0x80000000u, ldsb + i * 1024);
    }
    waitv<16>(); sbar();
    STAMP(2);
    {
      const int cc4 = 2 * wave + (lane >> 5), rp = lane & 31;
      const unsigned kb = KV_BASE + 4 * KV_CG;
      v4u kk[5], vv[2], w4[4];
#pragma unroll
      for (int i = 0; i < 4; ++i) w4[i] = lds128(lds + pw + 512 + (lane >> 5) * 64 + i * 16);
#pragma unroll
      for (int i = 0; i < 5; ++i) { const int row = 2 * rp + 5 + i; kk[i] = lds128(lds + kb + (row >> 3) * KV_GS + (row & 7) * 128 + ((cc4 ^ (4 * ((row >> 4) & 1))) << 4)); }
#pragma unroll
      for (int i = 0; i < 2; ++i) { const int row = 2 * rp + i; vv[i] = lds128(lds + kb + KV_V + (row >> 3) * KV_GS + (row & 7) * 128 + ((cc4 ^ (4 * ((row >> 4) & 1))) << 4)); }
      const unsigned ob = (64 * (cg0 + 4) + 8 * cc4) * 4096 + (64 * rbk + 2 * rp) * 2;
      kv_math<2>(kk, vv, w4, [&](int cp, const f32x2 (&rr)[2]) {
#pragma unroll
        for (int c = 0; c < 2; ++c) __builtin_amdgcn_raw_buffer_store_b32(pk2(rr[0][c], rr[1][c]), rsO, ob + (2 * cp + c) * 4096, 0, SC1);
      });
    }
    FT(11);
    waitv<8>();
    {
      const rsrc_t rsWo = mkrs(p.ws + WS_W2T, 8 * 48 * 5 * 1024);
      const int r0 = 4 * g + q4;
#pragma unroll
      for (int i = 0; i < 8; ++i) {
        const unsigned a = ldsb + r0 * 256 + ((i ^ (r0 & 7)) << 5) + 8 * p4;
        const v4u fr = cat(ldstr(lds + a), ldstr(lds + a + 4096));
        const int ctg = 8 * blk + i, ntt = ctg / 5, ct = ctg % 5;
        __builtin_amdgcn_raw_buffer_store_b128(fr, rsWo, vl, id < 240 ? (unsigned)(((ntt * 48 + pk) * 5 + ct) * 1024) : 0x80000000u, SC1);
      }
    }
    FT(0);
    waitv<8>();
    waitl<0>(); sbar();
    tag = mktag(epoch);
    if (threadIdx.x == 0) st32_sc1(tag, C_FKV + j * FS, rsC);
    STAMP(3);
    ld_h();
    waitv<5>(); sbar();
    if (threadIdx.x == 0) st32_sc1(tag, C_FW + j * FS, rsC);
    unsigned fk;
    ld32_sc1(fk, foKV, rsC);
    FT(3);
    waitv<1>();
    waitv<0>();
    fill_h();
    FT(2);
    poll_finish(fk, foKV, rsC, tag);
    FT(4);
    ld_u();
    const rsrc_t rsX = mkrs(p.q, S * D * 2), rsG = mkrs(p.gw, D * 2), rsGo = mkrs(p.ws + WS_G, S * 4);
    const int grow = 256 * x + 32 * (r - 24) + 8 * wave + (lane >> 3), part = lane & 7;
    v4u xx[10];
    dma16s(lane * 16, rsG, 0, pw + 1024); dma16s(1024 + lane * 16, rsG, 0, pw + 2048);
#pragma unroll
    for (int i = 0; i < 10; ++i) ldV<0>(xx[i], grow * 1280 + i * 128 + part * 16, rsX, 0);
    waitv<12>();
    fill_u();
    ld32_sc1(fw, foW, rsC);
    waitv<1>();
    {
      float a0 = 0.f, a1 = 0.f;
#pragma unroll
      for (int i = 0; i < 10; ++i) {
        asm volatile("" : "+v"(xx[i]));
        const v4u gg = lds128(lds + pw + 1024 + (i * 8 + part) * 16);
        a0 = dot2(xx[i].x, gg.x, a0); a1 = dot2(xx[i].y, gg.y, a1); a0 = dot2(xx[i].z, gg.z, a0); a1 = dot2(xx[i].w, gg.w, a1);
      }
      float a = a0 + a1;
      a += __shfl_xor(a, 1); a += __shfl_xor(a, 2); a += __shfl_xor(a, 4);
      const float gt = __builtin_amdgcn_rcpf(1.f + __builtin_amdgcn_exp2f(a * -1.44269504088896f));
      if (part == 0) __builtin_amdgcn_raw_buffer_store_b32(__builtin_bit_cast(unsigned, gt), rsGo, grow * 4, 0, SC1);
    }
    FT(5);
    waitl<0>();
    STAMP(4);
  }
  STAMP(5);
  constexpr int NZ = G1R ? 4 : 8;
  constexpr int W0 = G1R ? WPRE : 0, WG0 = G1R ? 0 : 14;
  auto wcount = [](int gap) constexpr { return gap < WG0 ? 0 : (G1R ? ((gap - WG0) * (60 - WPRE)) / 140 + 1 : (gap - WG0) / 4 + 1); };
  const unsigned foH = C_FH + (((rb >> 1) * 2 + (rb & 1)) * 24 + (lane < 24 ? lane : 0)) * FS;
  unsigned fh;
  constexpr int GPOLL = NZ * 36 - 12;
  f32x4 acc[8];
  {
    const unsigned aA = cb + q4 * CV_HC + 56 - 16 * g + 8 * p4 + 256 * half;
    const unsigned aB = cb + CV_U + 448 + 32 * c16 + 16 * g - 256 * half - 64 * (NZ - 1);
    v4u A[8], B[8];
#pragma unroll
    for (int i = 0; i < 8; ++i) {
      B[7 - i] = lds128(lds + aB + 64 * (NZ - 1) + 512 * (7 - i));
      A[i] = cat(ldstr(lds + aA + 512 * i + 8), ldstr(lds + aA + 512 * i));
    }
    SCHED();
    static_for<0, NZ>([&](auto Z) {
      constexpr int z = Z.value;
      static_for<0, 8>([&](auto I) {
        constexpr int i = I.value;
        static_for<0, 8 - i>([&](auto DD) {
          constexpr int d = 7 - i - DD.value;
          constexpr int gap = z * 36 + (i * (17 - i)) / 2 + DD.value;
          if constexpr (z == 0 && i == 0) mfma_z(acc[i + d], A[i], B[d]); else mfma_ip(acc[i + d], A[i], B[d]);
          if constexpr (d == 7 - i && z + 1 < NZ) B[7 - i] = lds128(lds + aB + 64 * (NZ - 2 - z) + 512 * (7 - i));
          if constexpr (!G1R && gap == WG0 - 2) { waitv<0>(); poll_finish(fw, foW, rsC, tag); }
          if constexpr (wcount(gap) > wcount(gap - 1)) ldW(IC(W0 + wcount(gap) - 1));
          if constexpr (gap == GPOLL) ld32_sc1(fh, foH, rsC);
          SCHED();
        });
        if constexpr (i == 7) { FT(40 + z); SCHED(); }
        if constexpr (z + 1 < NZ) { A[i] = cat(ldstr(lds + aA + 64 * (z + 1) + 512 * i + 8), ldstr(lds + aA + 64 * (z + 1) + 512 * i)); SCHED(); }
      });
    });
  }
  static_assert(W0 + wcount(NZ * 36 - 1) >= 60, "all W2 fragments issued");
  STAMP(6);
  constexpr int WAFT = 60 - (W0 + wcount(GPOLL));
  waitv<(WAFT > 0 ? WAFT : 0)>();
  FT(48);
  poll_finish(fh, foH, rsC, tag);
  FT(49);
  v4u Hf[HWM];
  const unsigned sH = (4 * rb * 48 + 12 * wave) * 1024;
  auto ldH = [&](auto N) { constexpr int n = N.value; if constexpr (n < 48) ldVo<(n & 3) * 1024, SC1>(Hf[n % HWM], vl, rsH, sH + ((n / 12) * 48 + ((n % 12) & ~3)) * 1024); };
  static_for<0, HW0>([&](auto I) { ldH(I); });
  FT(14);
  asm volatile("s_nop 7" : "+v"(acc[0]), "+v"(acc[1]), "+v"(acc[2]), "+v"(acc[3]), "+v"(acc[4]), "+v"(acc[5]), "+v"(acc[6]), "+v"(acc[7]));
  SCHED();
  int ctb = 0;
  if constexpr (G1R) {
    LAS char* ex = lds + CV_EX + cl * 8192;
    f32x4 keep[4];
#pragma unroll
    for (int i = 0; i < 4; ++i) {
      f32x4 lo = acc[i], hi = acc[4 + i];
      asm volatile("" : "+v"(lo), "+v"(hi));
      *(LAS f32x4*)(ex + half * 4096 + i * 1024 + lane * 16) = half == 0 ? hi : lo;
      keep[i] = half == 0 ? lo : hi;
    }
    waitl<0>(); sbar();
#pragma unroll
    for (int i = 0; i < 4; ++i) acc[i] = keep[i] + *(LAS f32x4*)(ex + (1 - half) * 4096 + i * 1024 + lane * 16);
    ctb = 4 * half;
  }
  FT(51);
  constexpr int NT = G1R ? 4 : 8, NYS = NT / 2;
  {
    asm volatile("" : "+v"(dv));
    const float Dc = blo(dv);
#pragma unroll
    for (int i = 0; i < NT; i += 2) {
      unsigned w0[2][2];
#pragma unroll
      for (int e = 0; e < 2; ++e) {
        const int ct = ctb + i + e;
        const f32x4 a = acc[i + e];
        const v2u uu = lds64(lds + cb + CV_U + 480 + 512 * ct + 32 * c16 + 8 * g);
        w0[e][0] = pk2(fmaf(blo(uu.x), Dc, a[0]), fmaf(bhi(uu.x), Dc, a[1]));
        w0[e][1] = pk2(fmaf(blo(uu.y), Dc, a[2]), fmaf(bhi(uu.y), Dc, a[3]));
      }
      auto s0 = __builtin_amdgcn_permlane16_swap(w0[0][0], w0[1][0], false, false);
      auto s1 = __builtin_amdgcn_permlane16_swap(w0[0][1], w0[1][1], false, false);
      const int ct = ctb + i + (g & 1);
      __builtin_amdgcn_raw_buffer_store_b128(v4u{s0[0], s1[0], s0[1], s1[1]}, rsY, ch * 4096 + 512 * ct + 32 * c16 + 8 * (g & 2), 0, SC1);
    }
  }
  FT(52);
  waitl<0>(); sbar();
  STAMP(7);
  unsigned fe[13];
  const bool bump = blockIdx.x == 0 && wave == 0;
  f32x4 ac2[2][5];
  const unsigned foY0 = C_FY + (nt * 128 + lane) * FS, foY1 = foY0 + 64 * FS;
  const unsigned foG = C_FY + ((rb >> 2) * 128 + 96 + (lane & 31)) * FS;
  unsigned fy0, fy1, fg, gtu;
  v4u yv[3]; v2u qq[5];
  const int s = 64 * rb + 16 * wave + c16;
#define SF_ 5
  constexpr int SF = SF_;
  constexpr int SP = 25;
  constexpr int SY = 40;
  static_assert(g2_issued(SP) == 48 && SY >= SP + 8, "");
  auto extras_at = [](int st) constexpr { return (st == SF ? 1 : 0) + ((st >= SP && st < SP + 8) ? 1 : 0) + (st == SY ? 4 : 0); };
  static_for<0, 48>([&](auto I) {
    constexpr int i = I.value, rt = i / 12, k = i % 12, set = rt & 1;
    constexpr int younger = [&]() constexpr {
      int y = g2_issued(i) - (i + 1);
      if (i < HW0) y += NYS;
      int si = -1; while (i >= HW0 && g2_issued(si + 1) <= i) ++si;
      for (int st = (si < 0 ? 0 : si); st < i; ++st) y += extras_at(st);
      return y; }();
    waitv<(younger < 63 ? younger : 63)>();
    static_for<0, 5>([&](auto C) { constexpr int ct = C.value;
      if constexpr (k == 0) mfma_zA(ac2[set][ct], W[k][ct], Hf[i % HWM]); else mfma_ipA(ac2[set][ct], W[k][ct], Hf[i % HWM]);
      if constexpr (rt > 0 && k >= 1 && k <= 5 && ct == 3) *(LAS f32x4*)(lds + G2_PART + (((rt - 1) * 4 + wave) * 5 + (k - 1)) * 1024 + lane * 16) = ac2[set ^ 1][k - 1];
      if constexpr (ct == 1) { static_for<g2_issued(i), g2_issued(i + 1)>([&](auto N) { ldH(N); }); }
    });
    if constexpr (i == SF) {
      waitv<g2_issued(SF + 1) - HW0>();
      if (lane == 0) st32_sc1(tag, C_FY + ((x * 32 + r) * 4 + wave) * FS, rsC);
    }
    if constexpr (i == SP) ld32_sc1(fy0, foY0, rsC);
    if constexpr (i == SP + 1) ld32_sc1(fy1, foY1, rsC);
    if constexpr (i == SP + 2) ld32_sc1(fg, foG, rsC);
    if constexpr (i >= SP + 3 && i < SP + 8) { const rsrc_t rsQ = mkrs(p.q, S * D * 2); ldV2<AUXP>(qq[i - SP - 3], (s * D + 80 * nt + 16 * (i - SP - 3) + 4 * g) * 2, rsQ, 0); }
    if constexpr (i == 34) {
      if (bump) {
#pragma unroll
        for (int e = 0; e < 13; ++e) ld32_sc1(fe[e], e < 12 ? C_FH + (e * 64 + lane) * FS : C_FKV + lane * FS, rsC);
      }
    }
    if constexpr (i == SY) {
      waitv<5>();
      poll_finish2(fy0, foY0, fy1, foY1, rsC, tag);
      poll_finish(fg, foG, rsC, tag);
      { const rsrc_t rsG = mkrs(p.ws + WS_G, S * 4); ld32_sc1(gtu, s * 4, rsG); }
#pragma unroll
      for (int u = 0; u < 3; ++u) {
        const int idx = u * 256 + wave * 64 + lane, d = idx >> 3, cch = idx & 7;
        ldV<SC1>(yv[u], idx < 640 ? (unsigned)((80 * nt + d) * 4096 + rb * 128 + cch * 16) : 0x80000000u, rsY, 0);
      }
    }
    if constexpr (i % 4 == 3) { FT(16 + i / 4); }
    SCHED();
  });
  STAMP(9);
  asm volatile("s_nop 7" : "+v"(ac2[1][0]), "+v"(ac2[1][1]), "+v"(ac2[1][2]), "+v"(ac2[1][3]), "+v"(ac2[1][4]));
#pragma unroll
  for (int ct = 0; ct < 5; ++ct) *(LAS f32x4*)(lds + G2_PART + ((3 * 4 + wave) * 5 + ct) * 1024 + lane * 16) = ac2[1][ct];
  waitv<0>();
#pragma unroll
  for (int u = 0; u < 3; ++u) {
    asm volatile("" : "+v"(yv[u]));
    const int idx = u * 256 + wave * 64 + lane, d = idx >> 3, cch = idx & 7;
    if (idx < 640) { *(LAS v2u*)(lds + G2_Y + d * 136 + cch * 16) = v2u{yv[u].x, yv[u].y}; *(LAS v2u*)(lds + G2_Y + d * 136 + cch * 16 + 8) = v2u{yv[u].z, yv[u].w}; }
  }
  asm volatile("" : "+v"(gtu));
#pragma unroll
  for (int ct = 0; ct < 5; ++ct) asm volatile("" : "+v"(qq[ct]));
  waitl<0>(); sbar();
  STAMP(10);
  const float gt = __builtin_bit_cast(float, gtu);
#pragma unroll
  for (int ct = 0; ct < 5; ++ct) {
    f32x4 b = *(LAS f32x4*)(lds + G2_PART + ((wave * 4 + 0) * 5 + ct) * 1024 + lane * 16);
#pragma unroll
    for (int w = 1; w < 4; ++w) b += *(LAS f32x4*)(lds + G2_PART + ((wave * 4 + w) * 5 + ct) * 1024 + lane * 16);
    const v2u yy = ldstr(lds + G2_Y + (16 * ct + 4 * g + q4) * 136 + (16 * wave + 4 * p4) * 2);
    const float o0 = gt * (blo(qq[ct].x) * blo(yy.x)) + (1.f - gt) * b[0];
    const float o1 = gt * (bhi(qq[ct].x) * bhi(yy.x)) + (1.f - gt) * b[1];
    const float o2 = gt * (blo(qq[ct].y) * blo(yy.y)) + (1.f - gt) * b[2];
    const float o3 = gt * (bhi(qq[ct].y) * bhi(yy.y)) + (1.f - gt) * b[3];
    *(GAS v2u*)(p.out + s * D + 80 * nt + 16 * ct + 4 * g) = v2u{pk2(o0, o1), pk2(o2, o3)};
  }
  if (bump) {
    unsigned bad = 0;
#pragma unroll
    for (int e = 0; e < 13; ++e) { asm volatile("" : "+v"(fe[e])); bad |= fe[e] ^ tag; }
    if (__builtin_amdgcn_ballot_w64(bad != 0) != 0) {
#pragma unroll 1
      for (int e = 0; e < 13; ++e) { const unsigned fo = (e < 12 ? C_FH + (e * 64 + lane) * FS : C_FKV + lane * FS); unsigned f; ld32_sc1(f, fo, rsC); waitv<0>(); poll_finish(f, fo, rsC, tag); }
    }
    if (lane == 0) st32_sc1(tag, C_EPOCH, rsC);
  }
  STAMP(11);
  STAMP_FLUSH(); FT_FLUSH();
}

__global__ __launch_bounds__(256, 1) void fused(Params p) {
  v4u pf;
  {
    unsigned long long pc; unsigned len;
    asm volatile("s_getpc_b64 %0\nLpf_here:\n\ts_mov_b32 %1, Lpf_end-Lpf_here" : "=s"(pc), "=s"(len));
    const rsrc_t rs = mkrs((const void*)pc, len);
    asm volatile("buffer_load_dwordx4 %0, %1, %2, 0 offen" : "=v"(pf) : "v"(((blockIdx.x >> 3) * 256 + threadIdx.x) * 16), "s"(rs));
    SCHED();
  }
  extern __shared__ __attribute__((aligned(16))) char smem[];
  LAS char* lds = (LAS char*)smem;
  const int lane = threadIdx.x & 63, wave = __builtin_amdgcn_readfirstlane(threadIdx.x >> 6);
  const int x = blockIdx.x & 7, r = blockIdx.x >> 3;
  if (__builtin_expect(r < 24, 1)) body<true>(p, lds, x, r, wave, lane);
  else body<false>(p, lds, x, r, wave, lane);
  asm volatile("s_waitcnt vmcnt(0)\nLpf_end:" : "+v"(pf));
}

extern "C" void kernel_launch(void* const* d_in, const int* in_sizes, int n_in, void* d_out, int out_size, void* d_ws, size_t ws_size, hipStream_t stream) {
  Params p;
  p.q = (const u16*)d_in[0]; p.k = (const u16*)d_in[1]; p.v = (const u16*)d_in[2]; p.sk = (const u16*)d_in[3];
  p.h = (const u16*)d_in[4]; p.Dv = (const u16*)d_in[5]; p.w1 = (const u16*)d_in[7]; p.w2 = (const u16*)d_in[8]; p.gw = (const u16*)d_in[9];
  p.out = (u16*)d_out; p.ws = (char*)d_ws;
  (void)hipFuncSetAttribute((const void*)fused, hipFuncAttributeMaxDynamicSharedMemorySize, LDS_BYTES);
  fused<<<256, 256, LDS_BYTES, stream>>>(p);
}
```
